```python
import torch

def _softmax(x, axis=-1):
    return torch.softmax(x, dim=axis)
def _gelu(x):
    return torch.nn.functional.gelu(x, approximate="tanh")
def _silu(x):
    return torch.nn.functional.silu(x)
def _sigmoid(x):
    return torch.sigmoid(x)
def _layernorm(x, axis=-1, eps=1e-5):
    m = x.mean(dim=axis, keepdim=True)
    v = x.var(dim=axis, keepdim=True, unbiased=False)
    return (x - m) / torch.sqrt(v + eps)
def _scatter_add(tgt, idx, src, axis=0):
    return tgt.clone().index_add_(axis, idx, src)
def _segment_sum(src, seg, n):
    out = torch.zeros((n,) + src.shape[1:], dtype=src.dtype)
    return out.index_add_(0, seg, src)
def _pad(x, pw):
    flat = tuple(v for pair in reversed(pw) for v in pair)
    return torch.nn.functional.pad(x, flat)

kernel_name = 'standalone__mha_softmax__near_ridge'

def setup_inputs():

    _w = (lambda a: (lambda t: t.to(torch.bfloat16) if t.is_floating_point() else t)(torch.as_tensor(a)))
    B, H, Sq, Sk, D = 8, 32, 288, 2048, 128
    return {
        "q": _w((lambda *s: torch.randn(*s, dtype=torch.bfloat16) * 0.8)(B, H, Sq, D)),
        "k": _w((lambda *s: torch.randn(*s, dtype=torch.bfloat16) * 0.8)(B, H, Sk, D)),
        "v": _w((lambda *s: torch.randn(*s, dtype=torch.bfloat16) * 0.05)(B, H, Sk, D)),
    }

def reference(q, k, v):
    scale = q.shape[-1] ** -0.5
    scores = torch.einsum("bhqd,bhkd->bhqk", q, k) * scale
    p = _softmax(scores, axis=-1)
    return torch.einsum("bhqk,bhkd->bhqd", p, v)
```

```cpp
#include <hip/hip_runtime.h>
#include <hip/hip_bf16.h>

constexpr int B_ = 8, H_ = 32, SQ = 288, SK = 2048, D_ = 128;
__device__ __forceinline__ float bf2f(unsigned short x) { return __uint_as_float(((unsigned)x) << 16); }
__global__ __launch_bounds__(256) void naive_attn(const unsigned short* __restrict__ q, const unsigned short* __restrict__ k,
                                                  const unsigned short* __restrict__ v, __hip_bfloat16* __restrict__ o, float* __restrict__ of) {
  extern __shared__ float sm[];
  float* S = sm;
  float* Q = sm + 16 * SK;
  float* red = Q + 16 * D_;
  const int bh = blockIdx.x / (SQ / 16), rb = blockIdx.x % (SQ / 16), t = threadIdx.x;
  const unsigned short* qp = q + ((size_t)bh * SQ + rb * 16) * D_;
  const unsigned short* kp = k + (size_t)bh * SK * D_;
  const unsigned short* vp = v + (size_t)bh * SK * D_;
  for (int i = t; i < 16 * D_; i += 256) Q[i] = bf2f(qp[i]);
  __syncthreads();
  const float scale = 0.08838834764831845f;
  for (int key = t; key < SK; key += 256) {
    float acc[16];
    for (int r = 0; r < 16; ++r) acc[r] = 0.f;
    for (int d = 0; d < D_; d += 8) {
      uint4 kk = *(const uint4*)(kp + (size_t)key * D_ + d);
      float kf[8] = {__uint_as_float(kk.x << 16), __uint_as_float(kk.x & 0xffff0000u), __uint_as_float(kk.y << 16), __uint_as_float(kk.y & 0xffff0000u),
                     __uint_as_float(kk.z << 16), __uint_as_float(kk.z & 0xffff0000u), __uint_as_float(kk.w << 16), __uint_as_float(kk.w & 0xffff0000u)};
#pragma unroll
      for (int r = 0; r < 16; ++r)
#pragma unroll
        for (int j = 0; j < 8; ++j) acc[r] = fmaf(Q[r * D_ + d + j], kf[j], acc[r]);
    }
    for (int r = 0; r < 16; ++r) S[r * SK + key] = acc[r] * scale;
  }
  __syncthreads();
  {
    const int w = t >> 6, l = t & 63;
    for (int r = w * 4; r < w * 4 + 4; ++r) {
      float m = -INFINITY;
      for (int i = l; i < SK; i += 64) m = fmaxf(m, S[r * SK + i]);
      for (int s = 32; s; s >>= 1) m = fmaxf(m, __shfl_xor(m, s));
      float sum = 0.f;
      for (int i = l; i < SK; i += 64) { float p = expf(S[r * SK + i] - m); S[r * SK + i] = p; sum += p; }
      for (int s = 32; s; s >>= 1) sum += __shfl_xor(sum, s);
      if (l == 0) red[r] = 1.f / sum;
    }
  }
  __syncthreads();
  {
    const int d = t & 127, r0 = (t >> 7) * 8;
    float acc[8];
    for (int r = 0; r < 8; ++r) acc[r] = 0.f;
    for (int key = 0; key < SK; ++key) {
      float vv = bf2f(vp[(size_t)key * D_ + d]);
#pragma unroll
      for (int r = 0; r < 8; ++r) acc[r] = fmaf(S[(r0 + r) * SK + key], vv, acc[r]);
    }
    for (int r = 0; r < 8; ++r) {
      size_t idx = ((size_t)bh * SQ + rb * 16 + r0 + r) * D_ + d;
      float val = acc[r] * red[r0 + r];
      if (o) o[idx] = __float2bfloat16(val);
      if (of) of[idx] = val;
    }
  }
}
static void naive_launch(const void* q, const void* k, const void* v, void* o, float* of, hipStream_t stream) {
  size_t sh = (16 * SK + 16 * D_ + 16) * 4;
  hipFuncSetAttribute((const void*)naive_attn, hipFuncAttributeMaxDynamicSharedMemorySize, (int)sh);
  naive_attn<<<B_ * H_ * (SQ / 16), 256, sh, stream>>>((const unsigned short*)q, (const unsigned short*)k, (const unsigned short*)v, (__hip_bfloat16*)o, of);
}
extern "C" void kernel_launch(void* const* d_in, const int* in_sizes, int n_in, void* d_out, int out_size, void* d_ws, size_t ws_size, hipStream_t stream) {
  naive_launch(d_in[0], d_in[1], d_in[2], d_out, nullptr, stream);
}
```

```cpp
#include <hip/hip_runtime.h>
#include <hip/hip_bf16.h>


namespace {
typedef unsigned short u16;
constexpr int SQ = 288, SK = 2048, DH = 128;

__device__ __forceinline__ float lo16(unsigned x) { return __uint_as_float(x << 16); }
__device__ __forceinline__ float hi16(unsigned x) { return __uint_as_float(x & 0xffff0000u); }

__device__ __forceinline__ float dot128(const u16* __restrict__ a, const u16* __restrict__ b) {
  float s = 0.f;
#pragma unroll 2
  for (int d = 0; d < DH; d += 8) {
    const uint4 x = *(const uint4*)(a + d), y = *(const uint4*)(b + d);
    s = fmaf(lo16(x.x), lo16(y.x), s); s = fmaf(hi16(x.x), hi16(y.x), s);
    s = fmaf(lo16(x.y), lo16(y.y), s); s = fmaf(hi16(x.y), hi16(y.y), s);
    s = fmaf(lo16(x.z), lo16(y.z), s); s = fmaf(hi16(x.z), hi16(y.z), s);
    s = fmaf(lo16(x.w), lo16(y.w), s); s = fmaf(hi16(x.w), hi16(y.w), s);
  }
  return s;
}

__global__ __launch_bounds__(256, 1) void mha_fwd(const u16*, const u16*, const u16*, u16*, unsigned*) {
  typedef const u16* const __attribute__((address_space(4))) * kptr_t;
  kptr_t kp = (kptr_t)__builtin_amdgcn_kernarg_segment_ptr();
  unsigned flag, w, hd, tid = threadIdx.x;
  asm volatile(
      "s_load_dwordx8 s[24:31], %[kp], 0x0\n"
      "s_getpc_b64 s[66:67]\n"
      "v_and_b32 v112, 3, v0\n"
      "v_and_b32 v113, 0xc0, v0\n"
      "s_lshr_b32 s100, %[h], 3\n"
      "s_lshl_b32 s100, s100, 8\n"
      "v_lshl_add_u32 v112, v112, 13, v113\n"
      "v_add_u32 v112, s100, v112\n"
      "v_min_u32 v112, 40288, v112\n"
      "global_load_dword v127, v112, s[66:67]\n"
      "v_add_u32 v113, 0x8000, v112\n"
      "v_min_u32 v113, 40288, v113\n"
      "global_load_dword v126, v113, s[66:67]\n"
      "v_readfirstlane_b32 s64, v0\n"
      "v_and_b32 v88, 63, v0\n"
      "v_and_b32 v89, 15, v0\n"
      "v_lshrrev_b32 v90, 4, v88\n"
      "s_lshr_b32 s64, s64, 6\n"
      "s_lshl_b32 s53, s64, 12\n"
      "s_add_u32 m0, s53, 0x4000\n"
      "v_lshlrev_b32 v114, 15, v90\n"
      "s_lshl_b32 s100, s64, 12\n"
      "v_add_u32 v114, s100, v114\n"
      "v_lshlrev_b32 v116, 8, v90\n"
      "v_add_u32 v116, s53, v116\n"
      "v_add_u32 v115, 0, v90\n"
      "v_xor_b32 v115, v115, v89\n"
      "v_lshl_add_u32 v236, v115, 4, v114\n"
      "v_lshl_add_u32 v204, v115, 4, v116\n"
      "v_add_u32 v115, 4, v90\n"
      "v_xor_b32 v115, v115, v89\n"
      "v_lshl_add_u32 v237, v115, 4, v114\n"
      "v_lshl_add_u32 v205, v115, 4, v116\n"
      "v_add_u32 v115, 8, v90\n"
      "v_xor_b32 v115, v115, v89\n"
      "v_lshl_add_u32 v238, v115, 4, v114\n"
      "v_lshl_add_u32 v206, v115, 4, v116\n"
      "v_add_u32 v115, 12, v90\n"
      "v_xor_b32 v115, v115, v89\n"
      "v_lshl_add_u32 v239, v115, 4, v114\n"
      "v_lshl_add_u32 v207, v115, 4, v116\n"
      "s_and_b32 s101, %[h], 7\n"
      "s_lshr_b32 s100, %[h], 3\n"
      "s_lshl_b32 s101, s101, 5\n"
      "s_add_u32 %[hd], s101, s100\n"
      "s_lshl_b32 s62, %[hd], 19\n"
      "s_and_b32 s100, %[hd], 0x99\n"
      "s_bcnt1_i32_b32 s100, s100\n"
      "s_and_b32 s100, s100, 1\n"
      "s_and_b32 s101, %[hd], 0x31\n"
      "s_bcnt1_i32_b32 s101, s101\n"
      "s_and_b32 s101, s101, 1\n"
      "s_lshl1_add_u32 s100, s100, s101\n"
      "s_and_b32 s101, %[hd], 0xfb\n"
      "s_bcnt1_i32_b32 s101, s101\n"
      "s_and_b32 s101, s101, 1\n"
      "s_lshl1_add_u32 s100, s100, s101\n"
      "s_and_b32 s101, %[hd], 0x94\n"
      "s_bcnt1_i32_b32 s101, s101\n"
      "s_and_b32 s101, s101, 1\n"
      "s_lshl1_add_u32 s100, s100, s101\n"
      "s_and_b32 s101, %[hd], 0x3a\n"
      "s_bcnt1_i32_b32 s101, s101\n"
      "s_and_b32 s101, s101, 1\n"
      "s_lshl1_add_u32 s100, s100, s101\n"
      "s_bfe_u32 s69, s100, 0x20000\n"
      "s_lshl_b32 s69, s69, 8\n"
      "s_bfe_u32 s101, s100, 0x10002\n"
      "s_lshl_b32 s101, s101, 14\n"
      "s_or_b32 s69, s69, s101\n"
      "s_bfe_u32 s101, s100, 0x20003\n"
      "s_lshl_b32 s101, s101, 17\n"
      "s_or_b32 s69, s69, s101\n"
      "s_or_b32 s100, s69, 0x1bc00\n"
      "s_mul_i32 s52, %[hd], 0x12000\n"
      "s_mov_b32 s38, 524288\n"
      "s_mov_b32 s39, 0x20000\n"
      "s_mov_b32 s46, 73728\n"
      "s_mov_b32 s47, 0x20000\n"
      "s_lshr_b32 s65, s64, 1\n"
      "s_lshl_b32 s65, s65, 12\n"
      "s_add_u32 s65, s65, 0x10000\n"
      "s_sub_u32 s101, s65, s53\n"
      "s_mul_i32 s63, s53, 3\n"
      "s_getreg_b32 s66, hwreg(HW_REG_XCC_ID)\n"
      "s_bitcmp1_b32 s66, 0\n"
      "s_cbranch_scc1 L_odd%=\n"
      "s_sleep 60\n"
      "L_odd%=:\n"
      "s_waitcnt lgkmcnt(0)\n"
      "s_add_u32 s36, s26, s62\n"
      "s_addc_u32 s37, s27, 0\n"
      "s_and_b32 s37, s37, 0xffff\n"
      "buffer_load_dwordx4 v236, s[36:39], s69 offen offset:0 lds\n"
      "buffer_load_dwordx4 v237, s[36:39], s69 offen offset:1024 lds\n"
      "buffer_load_dwordx4 v238, s[36:39], s69 offen offset:2048 lds\n"
      "buffer_load_dwordx4 v239, s[36:39], s69 offen offset:3072 lds\n"
      "s_add_u32 s44, s24, s52\n"
      "s_addc_u32 s45, s25, 0\n"
      "s_and_b32 s45, s45, 0xffff\n"
      "s_add_u32 m0, s53, 0x20000\n"
      "s_nop 0\n"
      "buffer_load_dwordx4 v204, s[44:47], s101 offen offset:0 nt lds\n"
      "buffer_load_dwordx4 v205, s[44:47], s101 offen offset:1024 nt lds\n"
      "buffer_load_dwordx4 v206, s[44:47], s101 offen offset:2048 nt lds\n"
      "buffer_load_dwordx4 v207, s[44:47], s101 offen offset:3072 nt lds\n"
      "s_add_u32 m0, s53, 0x24000\n"
      "s_nop 0\n"
      "buffer_load_dwordx4 v204, s[44:47], s63 offen offset:0 nt lds\n"
      "buffer_load_dwordx4 v205, s[44:47], s63 offen offset:1024 nt lds\n"
      "s_add_u32 s40, s28, s62\n"
      "s_addc_u32 s41, s29, 0\n"
      "s_and_b32 s41, s41, 0xffff\n"
      "s_add_u32 s48, s30, s52\n"
      "s_addc_u32 s49, s31, 0\n"
      "s_and_b32 s49, s49, 0xffff\n"
      "s_mov_b32 s52, 0x1fd00\n"
      "s_mov_b32 s42, 524288\n"
      "s_mov_b32 s43, 0x20000\n"
      "s_mov_b32 s50, 73728\n"
      "s_mov_b32 s51, 0x20000\n"
      "s_and_b32 s62, s64, 1\n"
      "s_add_u32 s54, s53, 0\n"
      "s_add_u32 s55, s53, 16384\n"
      "s_add_u32 s56, s53, 32768\n"
      "s_add_u32 s57, s53, 49152\n"
      "s_add_u32 s58, s53, 65536\n"
      "s_add_u32 s59, s53, 81920\n"
      "s_add_u32 s60, s53, 98304\n"
      "s_add_u32 s61, s53, 114688\n"
      "v_add_u32 v115, 0, v90\n"
      "v_lshlrev_b32 v115, 1, v115\n"
      "v_xor_b32 v115, v115, v89\n"
      "v_lshl_add_u32 v240, v115, 4, v114\n"
      "v_add_u32 v115, 4, v90\n"
      "v_lshlrev_b32 v115, 1, v115\n"
      "v_xor_b32 v115, v115, v89\n"
      "v_lshl_add_u32 v241, v115, 4, v114\n"
      "s_lshl_b32 s66, s62, 13\n"
      "s_sub_u32 s67, s53, s66\n"
      "v_lshlrev_b32 v114, 8, v89\n"
      "v_add_u32 v114, s66, v114\n"
      "v_add_u32 v115, 0, v90\n"
      "v_xor_b32 v115, v115, v89\n"
      "v_lshl_add_u32 v212, v115, 4, v114\n"
      "v_xor_b32 v216, 0x2000, v212\n"
      "v_add_u32 v80, s67, v212\n"
      "v_add_u32 v84, 0x10000, v80\n"
      "v_add_u32 v92, 0x20000, v80\n"
      "v_add_u32 v115, 4, v90\n"
      "v_xor_b32 v115, v115, v89\n"
      "v_lshl_add_u32 v213, v115, 4, v114\n"
      "v_xor_b32 v217, 0x2000, v213\n"
      "v_add_u32 v81, s67, v213\n"
      "v_add_u32 v85, 0x10000, v81\n"
      "v_add_u32 v93, 0x20000, v81\n"
      "v_add_u32 v115, 8, v90\n"
      "v_xor_b32 v115, v115, v89\n"
      "v_lshl_add_u32 v214, v115, 4, v114\n"
      "v_xor_b32 v218, 0x2000, v214\n"
      "v_add_u32 v82, s67, v214\n"
      "v_add_u32 v86, 0x10000, v82\n"
      "v_add_u32 v94, 0x20000, v82\n"
      "v_add_u32 v115, 12, v90\n"
      "v_xor_b32 v115, v115, v89\n"
      "v_lshl_add_u32 v215, v115, 4, v114\n"
      "v_xor_b32 v219, 0x2000, v215\n"
      "v_add_u32 v83, s67, v215\n"
      "v_add_u32 v87, 0x10000, v83\n"
      "v_add_u32 v95, 0x20000, v83\n"
      "s_mov_b32 s68, 0x3e0293ee\n"
      "v_lshrrev_b32 v115, 2, v89\n"
      "v_lshl_add_u32 v115, v90, 2, v115\n"
      "v_and_b32 v116, 7, v115\n"
      "s_lshl_b32 s67, s62, 2\n"
      "v_xor_b32 v116, s67, v116\n"
      "v_lshlrev_b32 v115, 8, v115\n"
      "v_and_b32 v117, 3, v89\n"
      "v_lshl_add_u32 v115, v117, 3, v115\n"
      "s_add_u32 s66, s66, 0x10000\n"
      "v_add_u32 v115, s66, v115\n"
      "v_xor_b32 v117, 0, v116\n"
      "v_lshl_add_u32 v220, v117, 5, v115\n"
      "v_xor_b32 v228, 0x2000, v220\n"
      "v_xor_b32 v117, 1, v116\n"
      "v_lshl_add_u32 v221, v117, 5, v115\n"
      "v_xor_b32 v229, 0x2000, v221\n"
      "v_xor_b32 v117, 2, v116\n"
      "v_lshl_add_u32 v222, v117, 5, v115\n"
      "v_xor_b32 v230, 0x2000, v222\n"
      "v_xor_b32 v117, 3, v116\n"
      "v_lshl_add_u32 v223, v117, 5, v115\n"
      "v_xor_b32 v231, 0x2000, v223\n"
      "v_xor_b32 v117, 4, v116\n"
      "v_lshl_add_u32 v224, v117, 5, v115\n"
      "v_xor_b32 v232, 0x2000, v224\n"
      "v_xor_b32 v117, 5, v116\n"
      "v_lshl_add_u32 v225, v117, 5, v115\n"
      "v_xor_b32 v233, 0x2000, v225\n"
      "v_xor_b32 v117, 6, v116\n"
      "v_lshl_add_u32 v226, v117, 5, v115\n"
      "v_xor_b32 v234, 0x2000, v226\n"
      "v_xor_b32 v117, 7, v116\n"
      "v_lshl_add_u32 v227, v117, 5, v115\n"
      "v_xor_b32 v235, 0x2000, v227\n"
      "v_and_b32 v114, 3, v88\n"
      "v_cmp_eq_u32 vcc, 0, v114\n"
      "v_mov_b32 v115, 0x3f803f80\n"
      "s_nop 0\n"
      "v_cndmask_b32 v114, 0, v115, vcc\n"
      "v_accvgpr_write_b32 a228, v114\n"
      "v_accvgpr_write_b32 a229, v114\n"
      "v_accvgpr_write_b32 a230, v114\n"
      "v_accvgpr_write_b32 a231, v114\n"
      "v_accvgpr_write_b32 a232, 0\n"
      "v_accvgpr_write_b32 a233, 0\n"
      "v_accvgpr_write_b32 a234, 0\n"
      "v_accvgpr_write_b32 a235, 0\n"
      "v_lshrrev_b32 v114, 2, v89\n"
      "v_and_b32 v115, 3, v89\n"
      "v_xor_b32 v114, v114, v90\n"
      "v_lshl_add_u32 v114, v114, 2, v115\n"
      "v_and_b32 v115, 1, v114\n"
      "v_lshlrev_b32 v115, 4, v115\n"
      "v_lshrrev_b32 v114, 1, v114\n"
      "v_mov_b32 v116, 0x3e03\n"
      "v_mov_b32 v117, 0xb9d8\n"
      "v_lshlrev_b32 v116, v115, v116\n"
      "v_lshlrev_b32 v117, v115, v117\n"
      "v_cmp_eq_u32 vcc, 0, v114\n"
      "s_nop 1\n"
      "v_cndmask_b32 v118, 0, v116, vcc\n"
      "v_accvgpr_write_b32 a238, v118\n"
      "v_cndmask_b32 v118, 0, v117, vcc\n"
      "v_accvgpr_write_b32 a242, v118\n"
      "v_cmp_eq_u32 vcc, 1, v114\n"
      "s_nop 1\n"
      "v_cndmask_b32 v118, 0, v116, vcc\n"
      "v_accvgpr_write_b32 a239, v118\n"
      "v_cndmask_b32 v118, 0, v117, vcc\n"
      "v_accvgpr_write_b32 a243, v118\n"
      "v_accvgpr_write_b32 a236, 0\n"
      "v_accvgpr_write_b32 a237, 0\n"
      "v_accvgpr_write_b32 a240, 0\n"
      "v_accvgpr_write_b32 a241, 0\n"
      "v_accvgpr_write_b32 a244, 0\n"
      "v_accvgpr_write_b32 a245, 0\n"
      "v_mov_b32 v242, 0\n"
      "v_mov_b32 v243, 0\n"
      "v_mov_b32 v244, 0\n"
      "v_mov_b32 v245, 0\n"
      "v_mov_b32 v246, 0\n"
      "v_mov_b32 v247, 0\n"
      "s_lshl_b32 s66, s64, 13\n"
      "s_add_u32 s66, s66, 131072\n"
      "v_lshl_add_u32 v249, v88, 4, s66\n"
      "s_add_u32 s66, s66, 5120\n"
      "v_lshl_add_u32 v114, v90, 3, s66\n"
      "v_mov_b32 v115, 144\n"
      "v_mad_u32_u24 v250, v89, v115, v114\n"
      "v_and_b32 v114, 7, v88\n"
      "v_lshlrev_b32 v114, 4, v114\n"
      "v_lshrrev_b32 v116, 3, v88\n"
      "v_mad_u32_u24 v251, v116, v115, v114\n"
      "v_add_u32 v251, s66, v251\n"
      "v_lshl_add_u32 v114, v116, 8, v114\n"
      "s_lshl_b32 s66, s62, 7\n"
      "v_add_u32 v248, s66, v114\n"
      "v_xor_b32 v252, 0x80, v248\n"
      "v_mov_b32 v255, 0\n"
      "s_lshl_b32 s24, s64, 14\n"
      "s_add_u32 s25, s24, 0x1000\n"
      "s_add_u32 s26, s24, 0x2000\n"
      "s_add_u32 s27, s24, 0x3000\n"
      "buffer_load_dwordx4 v206, s[44:47], s63 offen offset:2048 nt lds\n"
      "buffer_load_dwordx4 v207, s[44:47], s63 offen offset:3072 nt lds\n"
      "s_add_u32 s63, s63, 0x1000\n"
      "s_add_u32 m0, s53, 0x0\n"
      "s_waitcnt vmcnt(8)\n"
      "s_barrier\n"
      "ds_read_b128 a[80:83], v212 offset:16384\n"
      "ds_read_b128 a[96:99], v212 offset:20480\n"
      "ds_read_b128 a[84:87], v213 offset:16384\n"
      "ds_read_b128 a[100:103], v213 offset:20480\n"
      "ds_read_b128 a[88:91], v214 offset:16384\n"
      "ds_read_b128 a[104:107], v214 offset:20480\n"
      "ds_read_b128 a[92:95], v215 offset:16384\n"
      "ds_read_b128 a[108:111], v215 offset:20480\n"
      "ds_read_b128 a[112:115], v216 offset:16384\n"
      "buffer_load_dwordx4 v204, s[44:47], s63 offen offset:0 nt lds\n"
      "ds_read_b128 a[140:143], v219 offset:20480\n"
      "ds_read_b128 a[116:119], v217 offset:16384\n"
      "ds_read_b128 a[136:139], v218 offset:20480\n"
      "ds_read_b128 a[120:123], v218 offset:16384\n"
      "ds_read_b128 a[132:135], v217 offset:20480\n"
      "ds_read_b128 a[124:127], v219 offset:16384\n"
      "buffer_load_dwordx4 v205, s[44:47], s63 offen offset:1024 nt lds\n"
      "ds_read_b128 a[128:131], v216 offset:20480\n"
      "buffer_load_dwordx4 v206, s[44:47], s63 offen offset:2048 nt lds\n"
      "buffer_load_dwordx4 v207, s[44:47], s63 offen offset:3072 nt lds\n"
      "s_add_u32 s63, s63, 0x1000\n"
      "s_mov_b32 m0, s59\n"
      "s_waitcnt vmcnt(8)\n"
      "ds_read_b128 v[96:99], v92 offset:0\n"
      "ds_read_b128 v[100:103], v93 offset:0\n"
      "ds_read_b128 v[104:107], v94 offset:0\n"
      "ds_read_b128 v[108:111], v95 offset:0\n"
      "s_waitcnt lgkmcnt(3)\n"
      "v_mfma_f32_16x16x32_bf16 v[112:115], a[238:241], v[96:99], 0\n"
      "v_mfma_f32_16x16x32_bf16 v[116:119], a[236:239], v[96:99], 0\n"
      "s_waitcnt lgkmcnt(2)\n"
      "v_mfma_f32_16x16x32_bf16 v[120:123], a[238:241], v[100:103], 0\n"
      "v_mfma_f32_16x16x32_bf16 v[124:127], a[236:239], v[100:103], 0\n"
      "v_mfma_f32_16x16x32_bf16 v[112:115], a[242:245], v[96:99], v[112:115]\n"
      "v_mfma_f32_16x16x32_bf16 v[116:119], a[240:243], v[96:99], v[116:119]\n"
      "buffer_load_dwordx4 v240, s[40:43], s69 offen offset:0 lds\n"
      "v_mfma_f32_16x16x32_bf16 v[120:123], a[242:245], v[100:103], v[120:123]\n"
      "v_mfma_f32_16x16x32_bf16 v[124:127], a[240:243], v[100:103], v[124:127]\n"
      "s_waitcnt lgkmcnt(1)\n"
      "s_nop 2\n"
      "v_cvt_pk_bf16_f32 v96, v112, v113\n"
      "v_accvgpr_write_b32 a64, v96\n"
      "v_cvt_pk_bf16_f32 v97, v114, v115\n"
      "v_accvgpr_write_b32 a65, v97\n"
      "v_mfma_f32_16x16x32_bf16 v[112:115], a[238:241], v[104:107], 0\n"
      "v_cvt_pk_bf16_f32 v98, v116, v117\n"
      "v_cvt_pk_bf16_f32 v99, v118, v119\n"
      "v_mfma_f32_16x16x32_bf16 v[116:119], a[236:239], v[104:107], 0\n"
      "v_cvt_pk_bf16_f32 v100, v120, v121\n"
      "v_cvt_pk_bf16_f32 v101, v122, v123\n"
      "v_cvt_pk_bf16_f32 v102, v124, v125\n"
      "v_cvt_pk_bf16_f32 v103, v126, v127\n"
      "v_mfma_f32_16x16x32_bf16 v[112:115], a[242:245], v[104:107], v[112:115]\n"
      "v_accvgpr_write_b32 a66, v98\n"
      "v_accvgpr_write_b32 a67, v99\n"
      "v_mfma_f32_16x16x32_bf16 v[116:119], a[240:243], v[104:107], v[116:119]\n"
      "s_waitcnt lgkmcnt(0)\n"
      "v_accvgpr_write_b32 a68, v100\n"
      "v_accvgpr_write_b32 a69, v101\n"
      "v_mfma_f32_16x16x32_bf16 v[120:123], a[238:241], v[108:111], 0\n"
      "v_accvgpr_write_b32 a70, v102\n"
      "v_accvgpr_write_b32 a71, v103\n"
      "v_mfma_f32_16x16x32_bf16 v[124:127], a[236:239], v[108:111], 0\n"
      "v_cvt_pk_bf16_f32 v104, v112, v113\n"
      "v_cvt_pk_bf16_f32 v105, v114, v115\n"
      "v_mfma_f32_16x16x32_bf16 v[120:123], a[242:245], v[108:111], v[120:123]\n"
      "v_cvt_pk_bf16_f32 v106, v116, v117\n"
      "v_cvt_pk_bf16_f32 v107, v118, v119\n"
      "v_mfma_f32_16x16x32_bf16 v[124:127], a[240:243], v[108:111], v[124:127]\n"
      "buffer_load_dwordx4 v241, s[40:43], s69 offen offset:1024 nt lds\n"
      "v_mfma_f32_16x16x32_bf16 v[160:163], a[80:83], v[96:99], 0\n"
      "v_accvgpr_write_b32 a72, v104\n"
      "v_accvgpr_write_b32 a73, v105\n"
      "v_mfma_f32_16x16x32_bf16 v[164:167], a[96:99], v[96:99], 0\n"
      "v_cvt_pk_bf16_f32 v108, v120, v121\n"
      "v_cvt_pk_bf16_f32 v109, v122, v123\n"
      "v_mfma_f32_16x16x32_bf16 v[160:163], a[84:87], v[100:103], v[160:163]\n"
      "v_cvt_pk_bf16_f32 v110, v124, v125\n"
      "v_cvt_pk_bf16_f32 v111, v126, v127\n"
      "v_mfma_f32_16x16x32_bf16 v[164:167], a[100:103], v[100:103], v[164:167]\n"
      "v_accvgpr_write_b32 a74, v106\n"
      "v_accvgpr_write_b32 a75, v107\n"
      "v_mfma_f32_16x16x32_bf16 v[160:163], a[88:91], v[104:107], v[160:163]\n"
      "v_accvgpr_write_b32 a76, v108\n"
      "v_accvgpr_write_b32 a77, v109\n"
      "v_mfma_f32_16x16x32_bf16 v[164:167], a[104:107], v[104:107], v[164:167]\n"
      "v_accvgpr_write_b32 a78, v110\n"
      "v_accvgpr_write_b32 a79, v111\n"
      "v_mfma_f32_16x16x32_bf16 v[160:163], a[92:95], v[108:111], v[160:163]\n"
      "v_mfma_f32_16x16x32_bf16 v[164:167], a[108:111], v[108:111], v[164:167]\n"
      "s_nop 6\n"
      "v_max3_f32 v88, v160, v161, v162\n"
      "v_max3_f32 v89, v164, v165, v166\n"
      "buffer_load_dwordx4 v240, s[40:43], s69 offen offset:2048 lds\n"
      "v_max3_f32 v88, v88, v163, v167\n"
      "v_max_f32 v88, v88, v89\n"
      "v_mov_b32 v89, v88\n"
      "s_nop 1\n"
      "v_permlane16_swap_b32 v88, v89\n"
      "v_max_f32 v88, v88, v89\n"
      "v_mov_b32 v89, v88\n"
      "s_nop 1\n"
      "v_permlane32_swap_b32 v88, v89\n"
      "v_max_f32 v88, v88, v89\n"
      "v_sub_f32 v208, 0, v88\n"
      "v_sub_f32 v209, 0, v88\n"
      "v_sub_f32 v210, 0, v88\n"
      "v_sub_f32 v211, 0, v88\n"
      "v_sub_f32 v160, v160, v88\n"
      "v_sub_f32 v161, v161, v88\n"
      "v_sub_f32 v162, v162, v88\n"
      "v_sub_f32 v163, v163, v88\n"
      "v_sub_f32 v164, v164, v88\n"
      "v_sub_f32 v165, v165, v88\n"
      "v_sub_f32 v166, v166, v88\n"
      "v_sub_f32 v167, v167, v88\n"
      "v_exp_f32 v160, v160\n"
      "v_exp_f32 v161, v161\n"
      "v_exp_f32 v162, v162\n"
      "v_exp_f32 v163, v163\n"
      "v_exp_f32 v164, v164\n"
      "v_exp_f32 v165, v165\n"
      "v_exp_f32 v166, v166\n"
      "v_exp_f32 v167, v167\n"
      "v_cvt_pk_bf16_f32 v176, v160, v161\n"
      "buffer_load_dwordx4 v241, s[40:43], s69 offen offset:3072 nt lds\n"
      "s_add_u32 m0, s53, 0x10000\n"
      "s_waitcnt vmcnt(8)\n"
      "ds_read_b128 v[64:67], v92 offset:16384\n"
      "ds_read_b128 v[68:71], v93 offset:16384\n"
      "ds_read_b128 v[72:75], v94 offset:16384\n"
      "ds_read_b128 v[76:79], v95 offset:16384\n"
      "v_cvt_pk_bf16_f32 v177, v162, v163\n"
      "v_cvt_pk_bf16_f32 v178, v164, v165\n"
      "v_cvt_pk_bf16_f32 v179, v166, v167\n"
      "s_nop 1\n"
      "v_mfma_f32_16x16x32_bf16 a[224:227], a[228:231], v[176:179], 0\n"
      "s_waitcnt lgkmcnt(3)\n"
      "v_mfma_f32_16x16x32_bf16 v[112:115], a[238:241], v[64:67], 0\n"
      "v_mfma_f32_16x16x32_bf16 v[116:119], a[236:239], v[64:67], 0\n"
      "s_waitcnt lgkmcnt(2)\n"
      "v_mfma_f32_16x16x32_bf16 v[120:123], a[238:241], v[68:71], 0\n"
      "v_mfma_f32_16x16x32_bf16 v[124:127], a[236:239], v[68:71], 0\n"
      "v_mfma_f32_16x16x32_bf16 v[112:115], a[242:245], v[64:67], v[112:115]\n"
      "v_mfma_f32_16x16x32_bf16 v[116:119], a[240:243], v[64:67], v[116:119]\n"
      "buffer_load_dwordx4 v204, s[44:47], s63 offen offset:0 nt lds\n"
      "v_mfma_f32_16x16x32_bf16 v[120:123], a[242:245], v[68:71], v[120:123]\n"
      "v_mfma_f32_16x16x32_bf16 v[124:127], a[240:243], v[68:71], v[124:127]\n"
      "s_waitcnt lgkmcnt(1)\n"
      "s_nop 2\n"
      "v_cvt_pk_bf16_f32 v64, v112, v113\n"
      "v_accvgpr_write_b32 a0, v64\n"
      "v_cvt_pk_bf16_f32 v65, v114, v115\n"
      "v_accvgpr_write_b32 a1, v65\n"
      "v_mfma_f32_16x16x32_bf16 v[112:115], a[238:241], v[72:75], 0\n"
      "v_cvt_pk_bf16_f32 v66, v116, v117\n"
      "v_cvt_pk_bf16_f32 v67, v118, v119\n"
      "v_mfma_f32_16x16x32_bf16 v[116:119], a[236:239], v[72:75], 0\n"
      "v_cvt_pk_bf16_f32 v68, v120, v121\n"
      "v_cvt_pk_bf16_f32 v69, v122, v123\n"
      "v_cvt_pk_bf16_f32 v70, v124, v125\n"
      "v_cvt_pk_bf16_f32 v71, v126, v127\n"
      "v_mfma_f32_16x16x32_bf16 v[112:115], a[242:245], v[72:75], v[112:115]\n"
      "v_accvgpr_write_b32 a2, v66\n"
      "v_accvgpr_write_b32 a3, v67\n"
      "v_mfma_f32_16x16x32_bf16 v[116:119], a[240:243], v[72:75], v[116:119]\n"
      "s_waitcnt lgkmcnt(0)\n"
      "v_accvgpr_write_b32 a4, v68\n"
      "v_accvgpr_write_b32 a5, v69\n"
      "v_mfma_f32_16x16x32_bf16 v[120:123], a[238:241], v[76:79], 0\n"
      "v_accvgpr_write_b32 a6, v70\n"
      "v_accvgpr_write_b32 a7, v71\n"
      "v_mfma_f32_16x16x32_bf16 v[124:127], a[236:239], v[76:79], 0\n"
      "v_cvt_pk_bf16_f32 v72, v112, v113\n"
      "v_cvt_pk_bf16_f32 v73, v114, v115\n"
      "v_mfma_f32_16x16x32_bf16 v[120:123], a[242:245], v[76:79], v[120:123]\n"
      "v_cvt_pk_bf16_f32 v74, v116, v117\n"
      "v_cvt_pk_bf16_f32 v75, v118, v119\n"
      "v_mfma_f32_16x16x32_bf16 v[124:127], a[240:243], v[76:79], v[124:127]\n"
      "buffer_load_dwordx4 v205, s[44:47], s63 offen offset:1024 nt lds\n"
      "v_mfma_f32_16x16x32_bf16 v[168:171], a[80:83], v[64:67], 0\n"
      "v_accvgpr_write_b32 a8, v72\n"
      "v_accvgpr_write_b32 a9, v73\n"
      "v_mfma_f32_16x16x32_bf16 v[172:175], a[96:99], v[64:67], 0\n"
      "v_cvt_pk_bf16_f32 v76, v120, v121\n"
      "v_cvt_pk_bf16_f32 v77, v122, v123\n"
      "v_mfma_f32_16x16x32_bf16 v[168:171], a[84:87], v[68:71], v[168:171]\n"
      "v_cvt_pk_bf16_f32 v78, v124, v125\n"
      "v_cvt_pk_bf16_f32 v79, v126, v127\n"
      "v_mfma_f32_16x16x32_bf16 v[172:175], a[100:103], v[68:71], v[172:175]\n"
      "v_accvgpr_write_b32 a10, v74\n"
      "v_accvgpr_write_b32 a11, v75\n"
      "v_mfma_f32_16x16x32_bf16 v[168:171], a[88:91], v[72:75], v[168:171]\n"
      "v_accvgpr_write_b32 a12, v76\n"
      "v_accvgpr_write_b32 a13, v77\n"
      "v_mfma_f32_16x16x32_bf16 v[172:175], a[104:107], v[72:75], v[172:175]\n"
      "v_accvgpr_write_b32 a14, v78\n"
      "v_accvgpr_write_b32 a15, v79\n"
      "v_mfma_f32_16x16x32_bf16 v[168:171], a[92:95], v[76:79], v[168:171]\n"
      "v_mfma_f32_16x16x32_bf16 v[172:175], a[108:111], v[76:79], v[172:175]\n"
      "s_nop 6\n"
      "v_max3_f32 v88, v168, v169, v170\n"
      "v_max3_f32 v89, v172, v173, v174\n"
      "buffer_load_dwordx4 v206, s[44:47], s63 offen offset:2048 nt lds\n"
      "v_max3_f32 v88, v88, v171, v175\n"
      "v_max_f32 v88, v88, v89\n"
      "v_mov_b32 v89, v88\n"
      "s_nop 1\n"
      "v_permlane16_swap_b32 v88, v89\n"
      "v_max_f32 v88, v88, v89\n"
      "v_mov_b32 v89, v88\n"
      "s_nop 1\n"
      "v_permlane32_swap_b32 v88, v89\n"
      "v_max_f32 v88, v88, v89\n"
      "v_sub_f32 v168, v168, v88\n"
      "v_sub_f32 v169, v169, v88\n"
      "v_sub_f32 v170, v170, v88\n"
      "v_sub_f32 v171, v171, v88\n"
      "v_sub_f32 v172, v172, v88\n"
      "v_sub_f32 v173, v173, v88\n"
      "v_sub_f32 v174, v174, v88\n"
      "v_sub_f32 v175, v175, v88\n"
      "v_exp_f32 v168, v168\n"
      "v_exp_f32 v169, v169\n"
      "v_exp_f32 v170, v170\n"
      "v_exp_f32 v171, v171\n"
      "v_exp_f32 v172, v172\n"
      "v_exp_f32 v173, v173\n"
      "v_exp_f32 v174, v174\n"
      "v_exp_f32 v175, v175\n"
      "v_cvt_pk_bf16_f32 v180, v168, v169\n"
      "v_cvt_pk_bf16_f32 v181, v170, v171\n"
      "v_cvt_pk_bf16_f32 v182, v172, v173\n"
      "v_cvt_pk_bf16_f32 v183, v174, v175\n"
      "v_sub_f32 v192, 0, v88\n"
      "buffer_load_dwordx4 v207, s[44:47], s63 offen offset:3072 nt lds\n"
      "s_add_u32 s63, s63, 0x1000\n"
      "s_add_u32 m0, s53, 0x1c000\n"
      "s_waitcnt vmcnt(8)\n"
      "ds_read_b128 v[96:99], v80 offset:0\n"
      "ds_read_b128 v[100:103], v81 offset:0\n"
      "ds_read_b128 v[104:107], v82 offset:0\n"
      "ds_read_b128 v[108:111], v83 offset:0\n"
      "v_sub_f32 v193, 0, v88\n"
      "v_sub_f32 v194, 0, v88\n"
      "v_sub_f32 v195, 0, v88\n"
      "v_mfma_f32_16x16x32_bf16 a[208:211], a[228:231], v[180:183], 0\n"
      "s_nop 0\n"
      "v_mfma_f32_16x16x32_bf16 v[160:163], a[112:115], a[0:3], v[192:195]\n"
      "v_mfma_f32_16x16x32_bf16 v[164:167], a[140:143], a[12:15], v[192:195]\n"
      "v_mfma_f32_16x16x32_bf16 v[160:163], a[116:119], a[4:7], v[160:163]\n"
      "v_mfma_f32_16x16x32_bf16 v[164:167], a[136:139], a[8:11], v[164:167]\n"
      "s_waitcnt lgkmcnt(3)\n"
      "v_mfma_f32_16x16x32_bf16 v[112:115], a[238:241], v[96:99], 0\n"
      "v_mfma_f32_16x16x32_bf16 v[116:119], a[236:239], v[96:99], 0\n"
      "s_waitcnt lgkmcnt(2)\n"
      "v_mfma_f32_16x16x32_bf16 v[120:123], a[238:241], v[100:103], 0\n"
      "v_mfma_f32_16x16x32_bf16 v[124:127], a[236:239], v[100:103], 0\n"
      "v_mfma_f32_16x16x32_bf16 v[112:115], a[242:245], v[96:99], v[112:115]\n"
      "buffer_load_dwordx4 v204, s[44:47], s63 offen offset:0 nt lds\n"
      "v_mfma_f32_16x16x32_bf16 v[116:119], a[240:243], v[96:99], v[116:119]\n"
      "v_mfma_f32_16x16x32_bf16 v[120:123], a[242:245], v[100:103], v[120:123]\n"
      "v_mfma_f32_16x16x32_bf16 v[124:127], a[240:243], v[100:103], v[124:127]\n"
      "v_mfma_f32_16x16x32_bf16 v[160:163], a[120:123], a[8:11], v[160:163]\n"
      "v_mfma_f32_16x16x32_bf16 v[164:167], a[132:135], a[4:7], v[164:167]\n"
      "s_waitcnt lgkmcnt(1)\n"
      "s_nop 0\n"
      "v_cvt_pk_bf16_f32 v96, v112, v113\n"
      "v_accvgpr_write_b32 a16, v96\n"
      "v_cvt_pk_bf16_f32 v97, v114, v115\n"
      "v_mfma_f32_16x16x32_bf16 v[112:115], a[238:241], v[104:107], 0\n"
      "v_cvt_pk_bf16_f32 v98, v116, v117\n"
      "v_cvt_pk_bf16_f32 v99, v118, v119\n"
      "v_mfma_f32_16x16x32_bf16 v[116:119], a[236:239], v[104:107], 0\n"
      "v_cvt_pk_bf16_f32 v100, v120, v121\n"
      "v_cvt_pk_bf16_f32 v101, v122, v123\n"
      "v_cvt_pk_bf16_f32 v102, v124, v125\n"
      "v_cvt_pk_bf16_f32 v103, v126, v127\n"
      "v_mfma_f32_16x16x32_bf16 v[112:115], a[242:245], v[104:107], v[112:115]\n"
      "v_accvgpr_write_b32 a17, v97\n"
      "v_accvgpr_write_b32 a18, v98\n"
      "v_mfma_f32_16x16x32_bf16 v[116:119], a[240:243], v[104:107], v[116:119]\n"
      "s_waitcnt lgkmcnt(0)\n"
      "v_accvgpr_write_b32 a19, v99\n"
      "v_accvgpr_write_b32 a20, v100\n"
      "v_mfma_f32_16x16x32_bf16 v[120:123], a[238:241], v[108:111], 0\n"
      "v_accvgpr_write_b32 a21, v101\n"
      "v_accvgpr_write_b32 a22, v102\n"
      "v_mfma_f32_16x16x32_bf16 v[124:127], a[236:239], v[108:111], 0\n"
      "v_cvt_pk_bf16_f32 v104, v112, v113\n"
      "buffer_load_dwordx4 v205, s[44:47], s63 offen offset:1024 nt lds\n"
      "v_mfma_f32_16x16x32_bf16 v[120:123], a[242:245], v[108:111], v[120:123]\n"
      "v_cvt_pk_bf16_f32 v105, v114, v115\n"
      "v_cvt_pk_bf16_f32 v106, v116, v117\n"
      "v_mfma_f32_16x16x32_bf16 v[124:127], a[240:243], v[108:111], v[124:127]\n"
      "v_cvt_pk_bf16_f32 v107, v118, v119\n"
      "v_accvgpr_write_b32 a23, v103\n"
      "v_mfma_f32_16x16x32_bf16 v[168:171], a[80:83], v[96:99], 0\n"
      "v_accvgpr_write_b32 a24, v104\n"
      "v_accvgpr_write_b32 a25, v105\n"
      "v_mfma_f32_16x16x32_bf16 v[172:175], a[96:99], v[96:99], 0\n"
      "v_cvt_pk_bf16_f32 v108, v120, v121\n"
      "v_cvt_pk_bf16_f32 v109, v122, v123\n"
      "v_mfma_f32_16x16x32_bf16 v[168:171], a[84:87], v[100:103], v[168:171]\n"
      "v_cvt_pk_bf16_f32 v110, v124, v125\n"
      "v_cvt_pk_bf16_f32 v111, v126, v127\n"
      "v_mfma_f32_16x16x32_bf16 v[172:175], a[100:103], v[100:103], v[172:175]\n"
      "v_accvgpr_write_b32 a26, v106\n"
      "v_accvgpr_write_b32 a27, v107\n"
      "v_mfma_f32_16x16x32_bf16 v[168:171], a[88:91], v[104:107], v[168:171]\n"
      "v_accvgpr_write_b32 a28, v108\n"
      "v_accvgpr_write_b32 a29, v109\n"
      "v_mfma_f32_16x16x32_bf16 v[172:175], a[104:107], v[104:107], v[172:175]\n"
      "v_accvgpr_write_b32 a30, v110\n"
      "v_accvgpr_write_b32 a31, v111\n"
      "v_mfma_f32_16x16x32_bf16 v[168:171], a[92:95], v[108:111], v[168:171]\n"
      "v_mfma_f32_16x16x32_bf16 v[172:175], a[108:111], v[108:111], v[172:175]\n"
      "v_mfma_f32_16x16x32_bf16 v[160:163], a[124:127], a[12:15], v[160:163]\n"
      "v_mfma_f32_16x16x32_bf16 v[164:167], a[128:131], a[0:3], v[164:167]\n"
      "buffer_load_dwordx4 v206, s[44:47], s63 offen offset:2048 nt lds\n"
      "s_nop 3\n"
      "v_max3_f32 v88, v168, v169, v170\n"
      "v_max3_f32 v89, v172, v173, v174\n"
      "v_max3_f32 v88, v88, v171, v175\n"
      "v_max_f32 v88, v88, v89\n"
      "v_mov_b32 v89, v88\n"
      "v_exp_f32 v160, v160\n"
      "v_exp_f32 v161, v161\n"
      "v_permlane16_swap_b32 v88, v89\n"
      "v_max_f32 v88, v88, v89\n"
      "v_mov_b32 v89, v88\n"
      "v_exp_f32 v162, v162\n"
      "v_exp_f32 v163, v163\n"
      "v_permlane32_swap_b32 v88, v89\n"
      "v_max_f32 v88, v88, v89\n"
      "v_sub_f32 v168, v168, v88\n"
      "v_sub_f32 v169, v169, v88\n"
      "v_sub_f32 v170, v170, v88\n"
      "v_sub_f32 v171, v171, v88\n"
      "v_sub_f32 v172, v172, v88\n"
      "v_sub_f32 v173, v173, v88\n"
      "v_sub_f32 v174, v174, v88\n"
      "v_sub_f32 v175, v175, v88\n"
      "v_exp_f32 v168, v168\n"
      "v_exp_f32 v169, v169\n"
      "v_exp_f32 v170, v170\n"
      "v_exp_f32 v171, v171\n"
      "buffer_load_dwordx4 v207, s[44:47], s63 offen offset:3072 nt lds\n"
      "s_mov_b32 m0, s56\n"
      "s_xor_b32 s101, 0x1bd00, s100\n"
      "s_waitcnt vmcnt(8)\n"
      "s_barrier\n"
      "v_exp_f32 v172, v172\n"
      "v_exp_f32 v173, v173\n"
      "v_exp_f32 v174, v174\n"
      "v_exp_f32 v175, v175\n"
      "v_cvt_pk_bf16_f32 v188, v168, v169\n"
      "v_cvt_pk_bf16_f32 v189, v170, v171\n"
      "v_cvt_pk_bf16_f32 v190, v172, v173\n"
      "v_cvt_pk_bf16_f32 v191, v174, v175\n"
      "ds_read_b64_tr_b16 a[180:181], v229 offset:16384\n"
      "ds_read_b64_tr_b16 a[182:183], v229 offset:20480\n"
      "ds_read_b64_tr_b16 a[188:189], v231 offset:16384\n"
      "ds_read_b64_tr_b16 a[190:191], v231 offset:20480\n"
      "ds_read_b64_tr_b16 a[196:197], v233 offset:16384\n"
      "ds_read_b64_tr_b16 a[198:199], v233 offset:20480\n"
      "ds_read_b64_tr_b16 a[204:205], v235 offset:16384\n"
      "ds_read_b64_tr_b16 a[206:207], v235 offset:20480\n"
      "v_sub_f32 v196, 0, v88\n"
      "v_sub_f32 v197, 0, v88\n"
      "v_sub_f32 v198, 0, v88\n"
      "v_sub_f32 v199, 0, v88\n"
      "ds_read_b64_tr_b16 a[144:145], v220 offset:16384\n"
      "ds_read_b64_tr_b16 a[146:147], v220 offset:20480\n"
      "v_exp_f32 v164, v164\n"
      "ds_read_b64_tr_b16 a[148:149], v221 offset:16384\n"
      "v_exp_f32 v165, v165\n"
      "v_exp_f32 v166, v166\n"
      "ds_read_b64_tr_b16 a[150:151], v221 offset:20480\n"
      "v_exp_f32 v167, v167\n"
      "ds_read_b64_tr_b16 a[152:153], v222 offset:16384\n"
      "v_cvt_pk_bf16_f32 v184, v160, v161\n"
      "v_cvt_pk_bf16_f32 v185, v162, v163\n"
      "v_mfma_f32_16x16x32_bf16 v[160:163], a[112:115], a[16:19], v[196:199]\n"
      "ds_read_b64_tr_b16 a[154:155], v222 offset:20480\n"
      "v_cvt_pk_bf16_f32 v186, v164, v165\n"
      "v_cvt_pk_bf16_f32 v187, v166, v167\n"
      "ds_read_b64_tr_b16 a[156:157], v223 offset:16384\n"
      "buffer_load_dwordx4 v236, s[36:39], s101 offen offset:0 lds\n"
      "v_mfma_f32_16x16x32_bf16 v[160:163], a[116:119], a[20:23], v[160:163]\n"
      "s_waitcnt lgkmcnt(13)\n"
      "ds_read_b64_tr_b16 a[158:159], v223 offset:20480\n"
      "v_mfma_f32_16x16x32_bf16 v[4:7], a[180:183], v[184:187], 0\n"
      "s_waitcnt lgkmcnt(12)\n"
      "ds_read_b64_tr_b16 a[160:161], v224 offset:16384\n"
      "v_mfma_f32_16x16x32_bf16 v[12:15], a[188:191], v[184:187], 0\n"
      "s_waitcnt lgkmcnt(11)\n"
      "ds_read_b64_tr_b16 a[162:163], v224 offset:20480\n"
      "v_mfma_f32_16x16x32_bf16 v[20:23], a[196:199], v[184:187], 0\n"
      "s_waitcnt lgkmcnt(10)\n"
      "ds_read_b64_tr_b16 a[164:165], v225 offset:16384\n"
      "v_mfma_f32_16x16x32_bf16 v[28:31], a[204:207], v[184:187], 0\n"
      "ds_read_b64_tr_b16 a[166:167], v225 offset:20480\n"
      "v_mfma_f32_16x16x32_bf16 v[164:167], a[140:143], a[28:31], v[196:199]\n"
      "ds_read_b64_tr_b16 a[168:169], v226 offset:16384\n"
      "v_mfma_f32_16x16x32_bf16 v[160:163], a[120:123], a[24:27], v[160:163]\n"
      "s_waitcnt lgkmcnt(11)\n"
      "ds_read_b64_tr_b16 a[170:171], v226 offset:20480\n"
      "v_mfma_f32_16x16x32_bf16 v[0:3], a[144:147], v[180:183], 0\n"
      "s_waitcnt lgkmcnt(10)\n"
      "ds_read_b64_tr_b16 a[172:173], v227 offset:16384\n"
      "v_mfma_f32_16x16x32_bf16 v[4:7], a[148:151], v[180:183], v[4:7]\n"
      "ds_read_b64_tr_b16 a[174:175], v227 offset:20480\n"
      "v_mfma_f32_16x16x32_bf16 v[164:167], a[136:139], a[24:27], v[164:167]\n"
      "s_waitcnt lgkmcnt(10)\n"
      "ds_read_b64_tr_b16 a[176:177], v228 offset:16384\n"
      "v_mfma_f32_16x16x32_bf16 v[8:11], a[152:155], v[180:183], 0\n"
      "ds_read_b64_tr_b16 a[178:179], v228 offset:20480\n"
      "v_mfma_f32_16x16x32_bf16 v[160:163], a[124:127], a[28:31], v[160:163]\n"
      "s_waitcnt lgkmcnt(10)\n"
      "ds_read_b64_tr_b16 a[184:185], v230 offset:16384\n"
      "buffer_load_dwordx4 v237, s[36:39], s101 offen offset:1024 lds\n"
      "v_mfma_f32_16x16x32_bf16 v[12:15], a[156:159], v[180:183], v[12:15]\n"
      "s_waitcnt lgkmcnt(9)\n"
      "ds_read_b64_tr_b16 a[186:187], v230 offset:20480\n"
      "ds_read_b64_tr_b16 a[192:193], v232 offset:16384\n"
      "v_mfma_f32_16x16x32_bf16 v[16:19], a[160:163], v[180:183], 0\n"
      "ds_read_b64_tr_b16 a[194:195], v232 offset:20480\n"
      "v_mfma_f32_16x16x32_bf16 v[164:167], a[132:135], a[20:23], v[164:167]\n"
      "s_waitcnt lgkmcnt(10)\n"
      "v_exp_f32 v160, v160\n"
      "v_mfma_f32_16x16x32_bf16 v[20:23], a[164:167], v[180:183], v[20:23]\n"
      "v_exp_f32 v161, v161\n"
      "v_mfma_f32_16x16x32_bf16 v[164:167], a[128:131], a[16:19], v[164:167]\n"
      "s_waitcnt lgkmcnt(8)\n"
      "v_exp_f32 v162, v162\n"
      "v_mfma_f32_16x16x32_bf16 v[24:27], a[168:171], v[180:183], 0\n"
      "v_exp_f32 v163, v163\n"
      "v_mfma_f32_16x16x32_bf16 v[128:131], a[144:147], v[176:179], 0\n"
      "s_waitcnt lgkmcnt(6)\n"
      "ds_read_b64_tr_b16 a[200:201], v234 offset:16384\n"
      "ds_read_b64_tr_b16 a[202:203], v234 offset:20480\n"
      "v_mfma_f32_16x16x32_bf16 v[28:31], a[172:175], v[180:183], v[28:31]\n"
      "v_exp_f32 v164, v164\n"
      "v_mfma_f32_16x16x32_bf16 v[132:135], a[148:151], v[176:179], 0\n"
      "v_exp_f32 v165, v165\n"
      "v_mfma_f32_16x16x32_bf16 v[136:139], a[152:155], v[176:179], 0\n"
      "v_exp_f32 v166, v166\n"
      "v_mfma_f32_16x16x32_bf16 v[140:143], a[156:159], v[176:179], 0\n"
      "v_exp_f32 v167, v167\n"
      "v_mfma_f32_16x16x32_bf16 v[144:147], a[160:163], v[176:179], 0\n"
      "buffer_load_dwordx4 v238, s[36:39], s101 offen offset:2048 lds\n"
      "v_mfma_f32_16x16x32_bf16 v[148:151], a[164:167], v[176:179], 0\n"
      "v_mfma_f32_16x16x32_bf16 v[152:155], a[168:171], v[176:179], 0\n"
      "v_mfma_f32_16x16x32_bf16 v[156:159], a[172:175], v[176:179], 0\n"
      "v_cvt_pk_bf16_f32 v176, v160, v161\n"
      "v_cvt_pk_bf16_f32 v177, v162, v163\n"
      "v_cvt_pk_bf16_f32 v178, v164, v165\n"
      "v_cvt_pk_bf16_f32 v179, v166, v167\n"
      "v_mfma_f32_16x16x32_bf16 a[208:211], a[228:231], v[184:187], a[208:211]\n"
      "s_waitcnt lgkmcnt(6)\n"
      "v_mfma_f32_16x16x32_bf16 v[0:3], a[176:179], v[184:187], v[0:3]\n"
      "s_waitcnt lgkmcnt(4)\n"
      "v_mfma_f32_16x16x32_bf16 v[8:11], a[184:187], v[184:187], v[8:11]\n"
      "s_waitcnt lgkmcnt(2)\n"
      "v_mfma_f32_16x16x32_bf16 v[16:19], a[192:195], v[184:187], v[16:19]\n"
      "s_waitcnt lgkmcnt(0)\n"
      "v_mfma_f32_16x16x32_bf16 v[24:27], a[200:203], v[184:187], v[24:27]\n"
      "v_mfma_f32_16x16x32_bf16 v[36:39], a[180:183], v[176:179], 0\n"
      "v_mfma_f32_16x16x32_bf16 v[44:47], a[188:191], v[176:179], 0\n"
      "v_mfma_f32_16x16x32_bf16 v[52:55], a[196:199], v[176:179], 0\n"
      "v_mfma_f32_16x16x32_bf16 v[60:63], a[204:207], v[176:179], 0\n"
      "buffer_load_dwordx4 v239, s[36:39], s101 offen offset:3072 lds\n"
      "s_mov_b32 m0, s60\n"
      "s_xor_b32 s101, 0x1bd00, s100\n"
      "s_waitcnt vmcnt(8)\n"
      "ds_read_b128 v[64:67], v84 offset:0\n"
      "ds_read_b128 v[68:71], v85 offset:0\n"
      "ds_read_b128 v[72:75], v86 offset:0\n"
      "ds_read_b128 v[76:79], v87 offset:0\n"
      "v_mfma_f32_16x16x32_bf16 a[212:215], a[228:231], v[188:191], 0\n"
      "v_mfma_f32_16x16x32_bf16 v[32:35], a[144:147], v[188:191], 0\n"
      "v_mfma_f32_16x16x32_bf16 v[40:43], a[152:155], v[188:191], 0\n"
      "v_mfma_f32_16x16x32_bf16 v[48:51], a[160:163], v[188:191], 0\n"
      "v_mfma_f32_16x16x32_bf16 v[56:59], a[168:171], v[188:191], 0\n"
      "s_waitcnt lgkmcnt(3)\n"
      "v_mfma_f32_16x16x32_bf16 v[112:115], a[238:241], v[64:67], 0\n"
      "v_mfma_f32_16x16x32_bf16 v[116:119], a[236:239], v[64:67], 0\n"
      "s_waitcnt lgkmcnt(2)\n"
      "v_mfma_f32_16x16x32_bf16 v[120:123], a[238:241], v[68:71], 0\n"
      "v_mfma_f32_16x16x32_bf16 v[124:127], a[236:239], v[68:71], 0\n"
      "v_mfma_f32_16x16x32_bf16 v[112:115], a[242:245], v[64:67], v[112:115]\n"
      "v_mfma_f32_16x16x32_bf16 v[116:119], a[240:243], v[64:67], v[116:119]\n"
      "buffer_load_dwordx4 v240, s[40:43], s101 offen offset:0 lds\n"
      "v_mfma_f32_16x16x32_bf16 v[120:123], a[242:245], v[68:71], v[120:123]\n"
      "v_mfma_f32_16x16x32_bf16 v[124:127], a[240:243], v[68:71], v[124:127]\n"
      "v_mfma_f32_16x16x32_bf16 v[36:39], a[148:151], v[188:191], v[36:39]\n"
      "v_mfma_f32_16x16x32_bf16 v[44:47], a[156:159], v[188:191], v[44:47]\n"
      "s_waitcnt lgkmcnt(1)\n"
      "s_nop 0\n"
      "v_cvt_pk_bf16_f32 v64, v112, v113\n"
      "v_accvgpr_write_b32 a32, v64\n"
      "v_cvt_pk_bf16_f32 v65, v114, v115\n"
      "v_cvt_pk_bf16_f32 v66, v116, v117\n"
      "v_cvt_pk_bf16_f32 v67, v118, v119\n"
      "v_mfma_f32_16x16x32_bf16 v[112:115], a[238:241], v[72:75], 0\n"
      "v_cvt_pk_bf16_f32 v68, v120, v121\n"
      "v_cvt_pk_bf16_f32 v69, v122, v123\n"
      "v_mfma_f32_16x16x32_bf16 v[116:119], a[236:239], v[72:75], 0\n"
      "v_cvt_pk_bf16_f32 v70, v124, v125\n"
      "v_cvt_pk_bf16_f32 v71, v126, v127\n"
      "v_mfma_f32_16x16x32_bf16 v[112:115], a[242:245], v[72:75], v[112:115]\n"
      "s_waitcnt lgkmcnt(0)\n"
      "v_accvgpr_write_b32 a33, v65\n"
      "v_accvgpr_write_b32 a34, v66\n"
      "v_mfma_f32_16x16x32_bf16 v[120:123], a[238:241], v[76:79], 0\n"
      "v_accvgpr_write_b32 a35, v67\n"
      "v_accvgpr_write_b32 a36, v68\n"
      "v_mfma_f32_16x16x32_bf16 v[116:119], a[240:243], v[72:75], v[116:119]\n"
      "v_accvgpr_write_b32 a37, v69\n"
      "v_accvgpr_write_b32 a38, v70\n"
      "v_mfma_f32_16x16x32_bf16 v[124:127], a[236:239], v[76:79], 0\n"
      "v_cvt_pk_bf16_f32 v72, v112, v113\n"
      "v_cvt_pk_bf16_f32 v73, v114, v115\n"
      "v_mfma_f32_16x16x32_bf16 v[120:123], a[242:245], v[76:79], v[120:123]\n"
      "v_accvgpr_write_b32 a39, v71\n"
      "buffer_load_dwordx4 v241, s[40:43], s101 offen offset:1024 nt lds\n"
      "v_mfma_f32_16x16x32_bf16 v[124:127], a[240:243], v[76:79], v[124:127]\n"
      "v_cvt_pk_bf16_f32 v74, v116, v117\n"
      "v_cvt_pk_bf16_f32 v75, v118, v119\n"
      "v_mfma_f32_16x16x32_bf16 v[168:171], a[80:83], v[64:67], 0\n"
      "v_accvgpr_write_b32 a40, v72\n"
      "v_accvgpr_write_b32 a41, v73\n"
      "v_mfma_f32_16x16x32_bf16 v[172:175], a[96:99], v[64:67], 0\n"
      "v_cvt_pk_bf16_f32 v76, v120, v121\n"
      "v_cvt_pk_bf16_f32 v77, v122, v123\n"
      "v_mfma_f32_16x16x32_bf16 v[168:171], a[84:87], v[68:71], v[168:171]\n"
      "v_cvt_pk_bf16_f32 v78, v124, v125\n"
      "v_cvt_pk_bf16_f32 v79, v126, v127\n"
      "v_mfma_f32_16x16x32_bf16 v[172:175], a[100:103], v[68:71], v[172:175]\n"
      "v_accvgpr_write_b32 a44, v76\n"
      "v_accvgpr_write_b32 a45, v77\n"
      "v_mfma_f32_16x16x32_bf16 v[168:171], a[88:91], v[72:75], v[168:171]\n"
      "v_accvgpr_write_b32 a46, v78\n"
      "v_accvgpr_write_b32 a47, v79\n"
      "v_mfma_f32_16x16x32_bf16 v[172:175], a[104:107], v[72:75], v[172:175]\n"
      "v_accvgpr_write_b32 a42, v74\n"
      "v_accvgpr_write_b32 a43, v75\n"
      "v_mfma_f32_16x16x32_bf16 v[168:171], a[92:95], v[76:79], v[168:171]\n"
      "v_mfma_f32_16x16x32_bf16 v[172:175], a[108:111], v[76:79], v[172:175]\n"
      "v_mfma_f32_16x16x32_bf16 v[52:55], a[164:167], v[188:191], v[52:55]\n"
      "v_mfma_f32_16x16x32_bf16 v[60:63], a[172:175], v[188:191], v[60:63]\n"
      "v_mfma_f32_16x16x32_bf16 a[212:215], a[228:231], v[176:179], a[212:215]\n"
      "buffer_load_dwordx4 v240, s[40:43], s101 offen offset:2048 lds\n"
      "v_mfma_f32_16x16x32_bf16 v[32:35], a[176:179], v[176:179], v[32:35]\n"
      "s_nop 1\n"
      "v_max3_f32 v88, v168, v169, v170\n"
      "v_mfma_f32_16x16x32_bf16 v[40:43], a[184:187], v[176:179], v[40:43]\n"
      "v_max3_f32 v89, v172, v173, v174\n"
      "v_max3_f32 v88, v88, v171, v175\n"
      "v_max_f32 v88, v88, v89\n"
      "v_mov_b32 v89, v88\n"
      "v_mfma_f32_16x16x32_bf16 v[48:51], a[192:195], v[176:179], v[48:51]\n"
      "s_nop 0\n"
      "v_permlane16_swap_b32 v88, v89\n"
      "v_max_f32 v88, v88, v89\n"
      "v_mov_b32 v89, v88\n"
      "v_mfma_f32_16x16x32_bf16 v[56:59], a[200:203], v[176:179], v[56:59]\n"
      "s_nop 0\n"
      "v_permlane32_swap_b32 v88, v89\n"
      "v_max_f32 v88, v88, v89\n"
      "v_sub_f32 v168, v168, v88\n"
      "v_sub_f32 v169, v169, v88\n"
      "v_sub_f32 v170, v170, v88\n"
      "v_sub_f32 v171, v171, v88\n"
      "v_sub_f32 v172, v172, v88\n"
      "v_sub_f32 v173, v173, v88\n"
      "v_sub_f32 v174, v174, v88\n"
      "v_sub_f32 v175, v175, v88\n"
      "v_exp_f32 v168, v168\n"
      "v_exp_f32 v169, v169\n"
      "v_exp_f32 v170, v170\n"
      "v_exp_f32 v171, v171\n"
      "v_exp_f32 v172, v172\n"
      "v_exp_f32 v173, v173\n"
      "v_exp_f32 v174, v174\n"
      "v_exp_f32 v175, v175\n"
      "buffer_load_dwordx4 v241, s[40:43], s101 offen offset:3072 nt lds\n"
      "s_mov_b32 m0, s57\n"
      "s_xor_b32 s101, 0x1be00, s100\n"
      "s_waitcnt vmcnt(8)\n"
      "ds_read_b128 v[96:99], v84 offset:49152\n"
      "ds_read_b128 v[100:103], v85 offset:49152\n"
      "ds_read_b128 v[104:107], v86 offset:49152\n"
      "ds_read_b128 v[108:111], v87 offset:49152\n"
      "v_cvt_pk_bf16_f32 v180, v168, v169\n"
      "v_cvt_pk_bf16_f32 v181, v170, v171\n"
      "v_cvt_pk_bf16_f32 v182, v172, v173\n"
      "v_cvt_pk_bf16_f32 v183, v174, v175\n"
      "v_sub_f32 v200, 0, v88\n"
      "v_sub_f32 v201, 0, v88\n"
      "v_sub_f32 v202, 0, v88\n"
      "v_sub_f32 v203, 0, v88\n"
      "v_mfma_f32_16x16x32_bf16 v[64:67], a[144:147], v[180:183], 0\n"
      "s_nop 0\n"
      "v_mfma_f32_16x16x32_bf16 v[160:163], a[112:115], a[32:35], v[200:203]\n"
      "v_mfma_f32_16x16x32_bf16 v[160:163], a[116:119], a[36:39], v[160:163]\n"
      "s_waitcnt lgkmcnt(3)\n"
      "v_mfma_f32_16x16x32_bf16 v[112:115], a[238:241], v[96:99], 0\n"
      "v_mfma_f32_16x16x32_bf16 v[116:119], a[236:239], v[96:99], 0\n"
      "s_waitcnt lgkmcnt(2)\n"
      "v_mfma_f32_16x16x32_bf16 v[120:123], a[238:241], v[100:103], 0\n"
      "v_mfma_f32_16x16x32_bf16 v[124:127], a[236:239], v[100:103], 0\n"
      "v_mfma_f32_16x16x32_bf16 v[112:115], a[242:245], v[96:99], v[112:115]\n"
      "v_mfma_f32_16x16x32_bf16 v[116:119], a[240:243], v[96:99], v[116:119]\n"
      "buffer_load_dwordx4 v236, s[36:39], s101 offen offset:0 lds\n"
      "v_mfma_f32_16x16x32_bf16 v[120:123], a[242:245], v[100:103], v[120:123]\n"
      "v_mfma_f32_16x16x32_bf16 v[124:127], a[240:243], v[100:103], v[124:127]\n"
      "v_mfma_f32_16x16x32_bf16 v[164:167], a[140:143], a[44:47], v[200:203]\n"
      "v_mfma_f32_16x16x32_bf16 v[160:163], a[120:123], a[40:43], v[160:163]\n"
      "s_nop 1\n"
      "v_cvt_pk_bf16_f32 v96, v112, v113\n"
      "v_mfma_f32_16x16x32_bf16 v[164:167], a[136:139], a[40:43], v[164:167]\n"
      "s_waitcnt lgkmcnt(1)\n"
      "v_cvt_pk_bf16_f32 v97, v114, v115\n"
      "v_cvt_pk_bf16_f32 v98, v116, v117\n"
      "v_cvt_pk_bf16_f32 v99, v118, v119\n"
      "v_mfma_f32_16x16x32_bf16 v[112:115], a[238:241], v[104:107], 0\n"
      "v_cvt_pk_bf16_f32 v100, v120, v121\n"
      "v_cvt_pk_bf16_f32 v101, v122, v123\n"
      "v_mfma_f32_16x16x32_bf16 v[116:119], a[236:239], v[104:107], 0\n"
      "v_cvt_pk_bf16_f32 v102, v124, v125\n"
      "v_cvt_pk_bf16_f32 v103, v126, v127\n"
      "v_mfma_f32_16x16x32_bf16 v[112:115], a[242:245], v[104:107], v[112:115]\n"
      "s_waitcnt lgkmcnt(0)\n"
      "v_accvgpr_write_b32 a48, v96\n"
      "v_accvgpr_write_b32 a49, v97\n"
      "v_mfma_f32_16x16x32_bf16 v[120:123], a[238:241], v[108:111], 0\n"
      "v_accvgpr_write_b32 a50, v98\n"
      "v_accvgpr_write_b32 a51, v99\n"
      "v_mfma_f32_16x16x32_bf16 v[116:119], a[240:243], v[104:107], v[116:119]\n"
      "v_accvgpr_write_b32 a52, v100\n"
      "v_accvgpr_write_b32 a53, v101\n"
      "v_mfma_f32_16x16x32_bf16 v[124:127], a[236:239], v[108:111], 0\n"
      "v_cvt_pk_bf16_f32 v104, v112, v113\n"
      "v_cvt_pk_bf16_f32 v105, v114, v115\n"
      "v_mfma_f32_16x16x32_bf16 v[120:123], a[242:245], v[108:111], v[120:123]\n"
      "buffer_load_dwordx4 v237, s[36:39], s101 offen offset:1024 lds\n"
      "v_mfma_f32_16x16x32_bf16 v[124:127], a[240:243], v[108:111], v[124:127]\n"
      "v_cvt_pk_bf16_f32 v106, v116, v117\n"
      "v_cvt_pk_bf16_f32 v107, v118, v119\n"
      "v_mfma_f32_16x16x32_bf16 v[168:171], a[80:83], v[96:99], 0\n"
      "v_accvgpr_write_b32 a54, v102\n"
      "v_accvgpr_write_b32 a55, v103\n"
      "v_mfma_f32_16x16x32_bf16 v[172:175], a[96:99], v[96:99], 0\n"
      "v_cvt_pk_bf16_f32 v108, v120, v121\n"
      "v_cvt_pk_bf16_f32 v109, v122, v123\n"
      "v_mfma_f32_16x16x32_bf16 v[168:171], a[84:87], v[100:103], v[168:171]\n"
      "v_cvt_pk_bf16_f32 v110, v124, v125\n"
      "v_cvt_pk_bf16_f32 v111, v126, v127\n"
      "v_mfma_f32_16x16x32_bf16 v[172:175], a[100:103], v[100:103], v[172:175]\n"
      "v_accvgpr_write_b32 a60, v108\n"
      "v_accvgpr_write_b32 a61, v109\n"
      "v_mfma_f32_16x16x32_bf16 v[168:171], a[88:91], v[104:107], v[168:171]\n"
      "v_accvgpr_write_b32 a62, v110\n"
      "v_accvgpr_write_b32 a63, v111\n"
      "v_mfma_f32_16x16x32_bf16 v[172:175], a[104:107], v[104:107], v[172:175]\n"
      "v_accvgpr_write_b32 a56, v104\n"
      "v_accvgpr_write_b32 a57, v105\n"
      "v_mfma_f32_16x16x32_bf16 v[168:171], a[92:95], v[108:111], v[168:171]\n"
      "v_accvgpr_write_b32 a58, v106\n"
      "v_accvgpr_write_b32 a59, v107\n"
      "v_mfma_f32_16x16x32_bf16 v[172:175], a[108:111], v[108:111], v[172:175]\n"
      "v_mfma_f32_16x16x32_bf16 v[160:163], a[124:127], a[44:47], v[160:163]\n"
      "v_mfma_f32_16x16x32_bf16 v[164:167], a[132:135], a[36:39], v[164:167]\n"
      "v_mfma_f32_16x16x32_bf16 v[164:167], a[128:131], a[32:35], v[164:167]\n"
      "buffer_load_dwordx4 v238, s[36:39], s101 offen offset:2048 lds\n"
      "v_mfma_f32_16x16x32_bf16 v[72:75], a[152:155], v[180:183], 0\n"
      "v_max3_f32 v88, v168, v169, v170\n"
      "v_mfma_f32_16x16x32_bf16 a[216:219], a[228:231], v[180:183], 0\n"
      "s_nop 0\n"
      "v_max3_f32 v89, v172, v173, v174\n"
      "v_max3_f32 v88, v88, v171, v175\n"
      "v_max_f32 v88, v88, v89\n"
      "v_mov_b32 v89, v88\n"
      "v_exp_f32 v160, v160\n"
      "v_exp_f32 v161, v161\n"
      "v_permlane16_swap_b32 v88, v89\n"
      "v_max_f32 v88, v88, v89\n"
      "v_mov_b32 v89, v88\n"
      "v_exp_f32 v162, v162\n"
      "v_exp_f32 v163, v163\n"
      "v_permlane32_swap_b32 v88, v89\n"
      "v_max_f32 v88, v88, v89\n"
      "v_sub_f32 v204, 0, v88\n"
      "v_sub_f32 v205, 0, v88\n"
      "v_sub_f32 v206, 0, v88\n"
      "v_sub_f32 v207, 0, v88\n"
      "v_cvt_pk_bf16_f32 v184, v160, v161\n"
      "v_cvt_pk_bf16_f32 v185, v162, v163\n"
      "v_mfma_f32_16x16x32_bf16 v[160:163], a[112:115], a[48:51], v[204:207]\n"
      "v_exp_f32 v164, v164\n"
      "v_exp_f32 v165, v165\n"
      "v_exp_f32 v166, v166\n"
      "v_exp_f32 v167, v167\n"
      "v_mfma_f32_16x16x32_bf16 v[160:163], a[116:119], a[52:55], v[160:163]\n"
      "v_cvt_pk_bf16_f32 v186, v164, v165\n"
      "v_cvt_pk_bf16_f32 v187, v166, v167\n"
      "v_mfma_f32_16x16x32_bf16 v[164:167], a[140:143], a[60:63], v[204:207]\n"
      "buffer_load_dwordx4 v239, s[36:39], s101 offen offset:3072 lds\n"
      "s_waitcnt vmcnt(8) lgkmcnt(0)\n"
      "s_barrier\n"
      "v_mfma_f32_16x16x32_bf16 v[160:163], a[120:123], a[56:59], v[160:163]\n"
      "ds_read_b128 a[80:83], v212 offset:32768\n"
      "ds_read_b128 a[108:111], v215 offset:36864\n"
      "ds_read_b128 a[88:91], v214 offset:32768\n"
      "ds_read_b128 a[104:107], v214 offset:36864\n"
      "v_mfma_f32_16x16x32_bf16 v[160:163], a[124:127], a[60:63], v[160:163]\n"
      "v_sub_f32 v168, v168, v88\n"
      "v_sub_f32 v169, v169, v88\n"
      "ds_read_b128 a[92:95], v215 offset:32768\n"
      "v_mfma_f32_16x16x32_bf16 v[164:167], a[136:139], a[56:59], v[164:167]\n"
      "v_sub_f32 v170, v170, v88\n"
      "v_sub_f32 v171, v171, v88\n"
      "v_mfma_f32_16x16x32_bf16 v[68:71], a[180:183], v[184:187], 0\n"
      "v_sub_f32 v172, v172, v88\n"
      "ds_read_b128 a[84:87], v213 offset:32768\n"
      "v_mfma_f32_16x16x32_bf16 v[76:79], a[188:191], v[184:187], 0\n"
      "v_sub_f32 v173, v173, v88\n"
      "v_sub_f32 v174, v174, v88\n"
      "v_mfma_f32_16x16x32_bf16 v[164:167], a[132:135], a[52:55], v[164:167]\n"
      "v_sub_f32 v175, v175, v88\n"
      "ds_read_b128 a[116:119], v217 offset:32768\n"
      "v_exp_f32 v168, v168\n"
      "v_exp_f32 v169, v169\n"
      "v_exp_f32 v170, v170\n"
      "v_exp_f32 v171, v171\n"
      "ds_read_b128 a[140:143], v219 offset:36864\n"
      "v_exp_f32 v172, v172\n"
      "v_exp_f32 v173, v173\n"
      "v_exp_f32 v174, v174\n"
      "v_exp_f32 v175, v175\n"
      "v_mfma_f32_16x16x32_bf16 v[64:67], a[176:179], v[184:187], v[64:67]\n"
      "v_exp_f32 v160, v160\n"
      "v_mfma_f32_16x16x32_bf16 v[72:75], a[184:187], v[184:187], v[72:75]\n"
      "v_exp_f32 v161, v161\n"
      "v_mfma_f32_16x16x32_bf16 a[216:219], a[228:231], v[184:187], a[216:219]\n"
      "ds_read_b128 a[100:103], v213 offset:36864\n"
      "ds_read_b128 a[96:99], v212 offset:36864\n"
      "v_mfma_f32_16x16x32_bf16 v[68:71], a[148:151], v[180:183], v[68:71]\n"
      "ds_read_b128 a[136:139], v218 offset:36864\n"
      "v_cvt_pk_bf16_f32 v188, v168, v169\n"
      "v_mfma_f32_16x16x32_bf16 v[76:79], a[156:159], v[180:183], v[76:79]\n"
      "v_cvt_pk_bf16_f32 v189, v170, v171\n"
      "v_cvt_pk_bf16_f32 v190, v172, v173\n"
      "v_mfma_f32_16x16x32_bf16 v[164:167], a[128:131], a[48:51], v[164:167]\n"
      "s_waitcnt lgkmcnt(10)\n"
      "ds_read_b128 a[132:135], v217 offset:36864\n"
      "v_cvt_pk_bf16_f32 v191, v174, v175\n"
      "s_nop 1\n"
      "v_mfma_f32_16x16x32_bf16 v[168:171], a[80:83], a[64:67], v[208:211]\n"
      "s_waitcnt lgkmcnt(6)\n"
      "ds_read_b128 a[124:127], v219 offset:32768\n"
      "v_cvt_pk_bf16_f32 v176, v160, v161\n"
      "s_nop 1\n"
      "v_mfma_f32_16x16x32_bf16 v[168:171], a[84:87], a[68:71], v[168:171]\n"
      "v_exp_f32 v162, v162\n"
      "s_nop 1\n"
      "v_mfma_f32_16x16x32_bf16 v[168:171], a[88:91], a[72:75], v[168:171]\n"
      "ds_read_b128 a[128:131], v216 offset:36864\n"
      "s_nop 1\n"
      "v_mfma_f32_16x16x32_bf16 v[168:171], a[92:95], a[76:79], v[168:171]\n"
      "v_exp_f32 v163, v163\n"
      "s_nop 1\n"
      "v_mfma_f32_16x16x32_bf16 v[172:175], a[108:111], a[76:79], v[208:211]\n"
      "ds_read_b128 a[120:123], v218 offset:32768\n"
      "v_cvt_pk_bf16_f32 v177, v162, v163\n"
      "s_nop 1\n"
      "v_mfma_f32_16x16x32_bf16 v[172:175], a[104:107], a[72:75], v[172:175]\n"
      "s_waitcnt lgkmcnt(6)\n"
      "v_exp_f32 v164, v164\n"
      "s_nop 1\n"
      "v_mfma_f32_16x16x32_bf16 v[172:175], a[100:103], a[68:71], v[172:175]\n"
      "s_waitcnt lgkmcnt(5)\n"
      "ds_read_b128 a[112:115], v216 offset:32768\n"
      "s_nop 1\n"
      "v_mfma_f32_16x16x32_bf16 v[172:175], a[96:99], a[64:67], v[172:175]\n"
      "v_exp_f32 v165, v165\n"
      "s_nop 1\n"
      "v_mfma_f32_16x16x32_bf16 v[80:83], a[160:163], v[180:183], 0\n"
      "v_cvt_pk_bf16_f32 v178, v164, v165\n"
      "s_nop 1\n"
      "v_mfma_f32_16x16x32_bf16 v[80:83], a[192:195], v[184:187], v[80:83]\n"
      "v_exp_f32 v166, v166\n"
      "s_nop 1\n"
      "v_mfma_f32_16x16x32_bf16 v[84:87], a[196:199], v[184:187], 0\n"
      "s_mov_b32 m0, s61\n"
      "s_xor_b32 s101, 0x1be00, s100\n"
      "buffer_load_dwordx4 v240, s[40:43], s101 offen offset:0 lds\n"
      "s_nop 1\n"
      "v_mfma_f32_16x16x32_bf16 v[84:87], a[164:167], v[180:183], v[84:87]\n"
      "v_exp_f32 v167, v167\n"
      "s_nop 1\n"
      "v_mfma_f32_16x16x32_bf16 v[88:91], a[168:171], v[180:183], 0\n"
      "v_cvt_pk_bf16_f32 v179, v166, v167\n"
      "s_nop 1\n"
      "v_mfma_f32_16x16x32_bf16 v[88:91], a[200:203], v[184:187], v[88:91]\n"
      "v_exp_f32 v168, v168\n"
      "s_nop 1\n"
      "v_mfma_f32_16x16x32_bf16 v[92:95], a[204:207], v[184:187], 0\n"
      "s_nop 0\n"
      "s_nop 0\n"
      "v_mfma_f32_16x16x32_bf16 v[92:95], a[172:175], v[180:183], v[92:95]\n"
      "v_exp_f32 v169, v169\n"
      "s_nop 1\n"
      "v_mfma_f32_16x16x32_bf16 v[160:163], a[80:83], a[0:3], v[192:195]\n"
      "v_add_f32 v246, v246, v169\n"
      "v_cvt_pk_bf16_f32 v180, v168, v169\n"
      "s_nop 1\n"
      "v_mfma_f32_16x16x32_bf16 v[160:163], a[84:87], a[4:7], v[160:163]\n"
      "v_exp_f32 v170, v170\n"
      "s_nop 1\n"
      "v_mfma_f32_16x16x32_bf16 v[160:163], a[88:91], a[8:11], v[160:163]\n"
      "v_add_f32 v247, v247, v168\n"
      "v_add_f32 v246, v246, v170\n"
      "s_nop 1\n"
      "v_mfma_f32_16x16x32_bf16 v[160:163], a[92:95], a[12:15], v[160:163]\n"
      "v_exp_f32 v171, v171\n"
      "s_nop 1\n"
      "v_mfma_f32_16x16x32_bf16 v[164:167], a[108:111], a[12:15], v[192:195]\n"
      "s_waitcnt vmcnt(5)\n"
      "s_barrier\n"
      "v_add_f32 v247, v247, v171\n"
      "v_cvt_pk_bf16_f32 v181, v170, v171\n"
      "s_nop 1\n"
      "v_mfma_f32_16x16x32_bf16 v[164:167], a[104:107], a[8:11], v[164:167]\n"
      "v_exp_f32 v172, v172\n"
      "s_nop 1\n"
      "v_mfma_f32_16x16x32_bf16 v[164:167], a[100:103], a[4:7], v[164:167]\n"
      "buffer_load_dwordx4 v241, s[40:43], s101 offen offset:1024 nt lds\n"
      "v_add_f32 v246, v246, v172\n"
      "s_nop 1\n"
      "v_mfma_f32_16x16x32_bf16 v[164:167], a[96:99], a[0:3], v[164:167]\n"
      "v_exp_f32 v173, v173\n"
      "s_nop 1\n"
      "v_mfma_f32_16x16x32_bf16 v[96:99], a[144:147], v[188:191], 0\n"
      "v_add_f32 v247, v247, v173\n"
      "v_cvt_pk_bf16_f32 v182, v172, v173\n"
      "s_nop 1\n"
      "v_mfma_f32_16x16x32_bf16 v[96:99], a[176:179], v[176:179], v[96:99]\n"
      "v_exp_f32 v174, v174\n"
      "s_nop 0\n"
      "v_mfma_f32_16x16x32_bf16 v[100:103], a[180:183], v[176:179], 0\n"
      "ds_read_b64_tr_b16 a[146:147], v220 offset:36864\n"
      "ds_read_b64_tr_b16 a[182:183], v229 offset:36864\n"
      "s_nop 1\n"
      "v_mfma_f32_16x16x32_bf16 v[100:103], a[148:151], v[188:191], v[100:103]\n"
      "v_exp_f32 v175, v175\n"
      "s_nop 1\n"
      "v_mfma_f32_16x16x32_bf16 v[104:107], a[152:155], v[188:191], 0\n"
      "ds_read_b64_tr_b16 a[150:151], v221 offset:36864\n"
      "v_add_f32 v246, v246, v175\n"
      "s_nop 1\n"
      "v_mfma_f32_16x16x32_bf16 v[104:107], a[184:187], v[176:179], v[104:107]\n"
      "v_exp_f32 v160, v160\n"
      "s_nop 1\n"
      "v_mfma_f32_16x16x32_bf16 v[108:111], a[188:191], v[176:179], 0\n"
      "ds_read_b64_tr_b16 a[144:145], v220 offset:32768\n"
      "v_cvt_pk_bf16_f32 v183, v174, v175\n"
      "s_nop 1\n"
      "v_mfma_f32_16x16x32_bf16 v[108:111], a[156:159], v[188:191], v[108:111]\n"
      "s_waitcnt lgkmcnt(4)\n"
      "v_exp_f32 v161, v161\n"
      "s_nop 1\n"
      "v_mfma_f32_16x16x32_bf16 v[168:171], a[112:115], a[0:3], v[192:195]\n"
      "s_nop 0\n"
      "s_nop 0\n"
      "v_mfma_f32_16x16x32_bf16 v[168:171], a[116:119], a[4:7], v[168:171]\n"
      "v_exp_f32 v162, v162\n"
      "s_nop 1\n"
      "v_mfma_f32_16x16x32_bf16 v[168:171], a[120:123], a[8:11], v[168:171]\n"
      "v_add_f32 v247, v247, v174\n"
      "v_cvt_pk_bf16_f32 v184, v160, v161\n"
      "s_nop 1\n"
      "v_mfma_f32_16x16x32_bf16 v[168:171], a[124:127], a[12:15], v[168:171]\n"
      "v_exp_f32 v163, v163\n"
      "s_nop 1\n"
      "v_mfma_f32_16x16x32_bf16 v[172:175], a[140:143], a[12:15], v[192:195]\n"
      "buffer_load_dwordx4 v240, s[40:43], s101 offen offset:2048 lds\n"
      "ds_read_b64_tr_b16 a[148:149], v221 offset:32768\n"
      "ds_read_b64_tr_b16 a[154:155], v222 offset:36864\n"
      "s_nop 1\n"
      "v_mfma_f32_16x16x32_bf16 v[172:175], a[136:139], a[8:11], v[172:175]\n"
      "v_exp_f32 v164, v164\n"
      "s_nop 1\n"
      "v_mfma_f32_16x16x32_bf16 v[172:175], a[132:135], a[4:7], v[172:175]\n"
      "ds_read_b64_tr_b16 a[156:157], v223 offset:32768\n"
      "ds_read_b64_tr_b16 a[158:159], v223 offset:36864\n"
      "s_nop 0\n"
      "v_mfma_f32_16x16x32_bf16 v[172:175], a[128:131], a[0:3], v[172:175]\n"
      "v_exp_f32 v165, v165\n"
      "s_nop 1\n"
      "v_mfma_f32_16x16x32_bf16 v[112:115], a[160:163], v[188:191], 0\n"
      "v_cvt_pk_bf16_f32 v185, v162, v163\n"
      "v_cvt_pk_bf16_f32 v186, v164, v165\n"
      "s_nop 1\n"
      "v_mfma_f32_16x16x32_bf16 v[112:115], a[192:195], v[176:179], v[112:115]\n"
      "v_exp_f32 v166, v166\n"
      "s_nop 1\n"
      "v_mfma_f32_16x16x32_bf16 v[116:119], a[196:199], v[176:179], 0\n"
      "ds_read_b64_tr_b16 a[152:153], v222 offset:32768\n"
      "ds_read_b64_tr_b16 a[160:161], v224 offset:32768\n"
      "s_nop 1\n"
      "v_mfma_f32_16x16x32_bf16 v[116:119], a[164:167], v[188:191], v[116:119]\n"
      "v_exp_f32 v167, v167\n"
      "s_nop 1\n"
      "v_mfma_f32_16x16x32_bf16 v[120:123], a[168:171], v[188:191], 0\n"
      "ds_read_b64_tr_b16 a[162:163], v224 offset:36864\n"
      "ds_read_b64_tr_b16 a[170:171], v226 offset:36864\n"
      "s_nop 0\n"
      "v_mfma_f32_16x16x32_bf16 v[120:123], a[200:203], v[176:179], v[120:123]\n"
      "ds_read_b64_tr_b16 a[196:197], v233 offset:32768\n"
      "ds_read_b64_tr_b16 a[198:199], v233 offset:36864\n"
      "s_nop 1\n"
      "v_mfma_f32_16x16x32_bf16 v[124:127], a[204:207], v[176:179], 0\n"
      "ds_read_b64_tr_b16 a[204:205], v235 offset:32768\n"
      "ds_read_b64_tr_b16 a[206:207], v235 offset:36864\n"
      "s_nop 1\n"
      "v_mfma_f32_16x16x32_bf16 v[124:127], a[172:175], v[188:191], v[124:127]\n"
      "v_exp_f32 v168, v168\n"
      "s_nop 1\n"
      "v_mfma_f32_16x16x32_bf16 a[220:223], a[228:231], v[188:191], 0\n"
      "ds_read_b64_tr_b16 a[166:167], v225 offset:36864\n"
      "ds_read_b64_tr_b16 a[168:169], v226 offset:32768\n"
      "s_nop 0\n"
      "v_mfma_f32_16x16x32_bf16 a[220:223], a[228:231], v[176:179], a[220:223]\n"
      "v_exp_f32 v169, v169\n"
      "s_nop 1\n"
      "v_mfma_f32_16x16x32_bf16 v[160:163], a[80:83], a[16:19], v[196:199]\n"
      "buffer_load_dwordx4 v241, s[40:43], s101 offen offset:3072 nt lds\n"
      "s_nop 1\n"
      "v_mfma_f32_16x16x32_bf16 v[160:163], a[84:87], a[20:23], v[160:163]\n"
      "v_exp_f32 v170, v170\n"
      "s_nop 1\n"
      "v_mfma_f32_16x16x32_bf16 v[160:163], a[88:91], a[24:27], v[160:163]\n"
      "ds_read_b64_tr_b16 a[164:165], v225 offset:32768\n"
      "v_cvt_pk_bf16_f32 v187, v166, v167\n"
      "s_nop 1\n"
      "v_mfma_f32_16x16x32_bf16 v[160:163], a[92:95], a[28:31], v[160:163]\n"
      "v_exp_f32 v171, v171\n"
      "s_nop 0\n"
      "v_mfma_f32_16x16x32_bf16 v[164:167], a[108:111], a[28:31], v[196:199]\n"
      "ds_read_b64_tr_b16 a[172:173], v227 offset:32768\n"
      "ds_read_b64_tr_b16 a[174:175], v227 offset:36864\n"
      "s_nop 1\n"
      "v_mfma_f32_16x16x32_bf16 v[164:167], a[104:107], a[24:27], v[164:167]\n"
      "v_exp_f32 v172, v172\n"
      "s_nop 1\n"
      "v_mfma_f32_16x16x32_bf16 v[164:167], a[100:103], a[20:23], v[164:167]\n"
      "ds_read_b64_tr_b16 a[200:201], v234 offset:32768\n"
      "ds_read_b64_tr_b16 a[202:203], v234 offset:36864\n"
      "s_nop 1\n"
      "v_mfma_f32_16x16x32_bf16 v[164:167], a[96:99], a[16:19], v[164:167]\n"
      "s_waitcnt lgkmcnt(15)\n"
      "v_exp_f32 v173, v173\n"
      "s_nop 0\n"
      "v_mfma_f32_16x16x32_bf16 v[128:131], a[144:147], v[180:183], v[128:131]\n"
      "s_waitcnt lgkmcnt(15)\n"
      "ds_read_b64_tr_b16 a[186:187], v230 offset:36864\n"
      "ds_read_b64_tr_b16 a[194:195], v232 offset:36864\n"
      "s_nop 1\n"
      "v_mfma_f32_16x16x32_bf16 v[132:135], a[148:151], v[180:183], v[132:135]\n"
      "s_waitcnt lgkmcnt(15)\n"
      "v_exp_f32 v174, v174\n"
      "s_nop 1\n"
      "v_mfma_f32_16x16x32_bf16 v[136:139], a[152:155], v[180:183], v[136:139]\n"
      "ds_read_b64_tr_b16 a[176:177], v228 offset:32768\n"
      "ds_read_b64_tr_b16 a[184:185], v230 offset:32768\n"
      "s_nop 0\n"
      "v_mfma_f32_16x16x32_bf16 v[140:143], a[156:159], v[180:183], v[140:143]\n"
      "s_waitcnt lgkmcnt(15)\n"
      "v_exp_f32 v175, v175\n"
      "s_nop 1\n"
      "v_mfma_f32_16x16x32_bf16 v[144:147], a[160:163], v[180:183], v[144:147]\n"
      "s_waitcnt lgkmcnt(8)\n"
      "ds_read_b64_tr_b16 a[178:179], v228 offset:36864\n"
      "ds_read_b64_tr_b16 a[180:181], v229 offset:32768\n"
      "s_nop 1\n"
      "v_mfma_f32_16x16x32_bf16 v[148:151], a[164:167], v[180:183], v[148:151]\n"
      "v_exp_f32 v160, v160\n"
      "s_nop 1\n"
      "v_mfma_f32_16x16x32_bf16 v[152:155], a[168:171], v[180:183], v[152:155]\n"
      "s_waitcnt lgkmcnt(8)\n"
      "s_mov_b32 m0, s54\n"
      "s_xor_b32 s101, 0x1bf00, s100\n"
      "buffer_load_dwordx4 v236, s[36:39], s101 offen offset:0 lds\n"
      "v_cvt_pk_bf16_f32 v188, v168, v169\n"
      "v_cvt_pk_bf16_f32 v189, v170, v171\n"
      "s_nop 0\n"
      "v_mfma_f32_16x16x32_bf16 v[156:159], a[172:175], v[180:183], v[156:159]\n"
      "v_exp_f32 v161, v161\n"
      "s_nop 1\n"
      "v_mfma_f32_16x16x32_bf16 v[168:171], a[112:115], a[16:19], v[196:199]\n"
      "v_cvt_pk_bf16_f32 v190, v172, v173\n"
      "v_cvt_pk_bf16_f32 v191, v174, v175\n"
      "s_nop 1\n"
      "v_mfma_f32_16x16x32_bf16 v[168:171], a[116:119], a[20:23], v[168:171]\n"
      "v_exp_f32 v162, v162\n"
      "s_nop 0\n"
      "v_mfma_f32_16x16x32_bf16 v[168:171], a[120:123], a[24:27], v[168:171]\n"
      "s_nop 0\n"
      "s_nop 1\n"
      "v_mfma_f32_16x16x32_bf16 v[168:171], a[124:127], a[28:31], v[168:171]\n"
      "v_exp_f32 v163, v163\n"
      "s_nop 1\n"
      "v_mfma_f32_16x16x32_bf16 v[172:175], a[140:143], a[28:31], v[196:199]\n"
      "ds_read_b64_tr_b16 a[188:189], v231 offset:32768\n"
      "ds_read_b64_tr_b16 a[190:191], v231 offset:36864\n"
      "s_nop 0\n"
      "v_mfma_f32_16x16x32_bf16 v[172:175], a[136:139], a[24:27], v[172:175]\n"
      "v_exp_f32 v164, v164\n"
      "s_nop 1\n"
      "v_mfma_f32_16x16x32_bf16 v[172:175], a[132:135], a[20:23], v[172:175]\n"
      "ds_read_b64_tr_b16 a[192:193], v232 offset:32768\n"
      "v_add_f32 v242, v242, v160\n"
      "s_nop 1\n"
      "v_mfma_f32_16x16x32_bf16 v[172:175], a[128:131], a[16:19], v[172:175]\n"
      "v_exp_f32 v165, v165\n"
      "s_nop 0\n"
      "v_mfma_f32_16x16x32_bf16 v[0:3], a[144:147], v[184:187], v[0:3]\n"
      "s_waitcnt lgkmcnt(4)\n"
      "v_add_f32 v243, v243, v161\n"
      "v_cvt_pk_bf16_f32 v176, v160, v161\n"
      "s_nop 1\n"
      "v_mfma_f32_16x16x32_bf16 v[0:3], a[176:179], v[188:191], v[0:3]\n"
      "s_waitcnt lgkmcnt(3)\n"
      "v_exp_f32 v166, v166\n"
      "s_nop 1\n"
      "v_mfma_f32_16x16x32_bf16 v[4:7], a[180:183], v[188:191], v[4:7]\n"
      "v_add_f32 v242, v242, v163\n"
      "v_cvt_pk_bf16_f32 v177, v162, v163\n"
      "s_nop 0\n"
      "v_mfma_f32_16x16x32_bf16 v[4:7], a[148:151], v[184:187], v[4:7]\n"
      "v_exp_f32 v167, v167\n"
      "s_nop 1\n"
      "v_mfma_f32_16x16x32_bf16 v[8:11], a[152:155], v[184:187], v[8:11]\n"
      "buffer_load_dwordx4 v237, s[36:39], s101 offen offset:1024 lds\n"
      "v_add_f32 v243, v243, v162\n"
      "v_add_f32 v242, v242, v164\n"
      "s_nop 1\n"
      "v_mfma_f32_16x16x32_bf16 v[8:11], a[184:187], v[188:191], v[8:11]\n"
      "s_waitcnt lgkmcnt(1)\n"
      "v_exp_f32 v168, v168\n"
      "s_nop 0\n"
      "v_mfma_f32_16x16x32_bf16 v[12:15], a[188:191], v[188:191], v[12:15]\n"
      "v_add_f32 v243, v243, v165\n"
      "v_cvt_pk_bf16_f32 v178, v164, v165\n"
      "s_nop 1\n"
      "v_mfma_f32_16x16x32_bf16 v[12:15], a[156:159], v[184:187], v[12:15]\n"
      "v_exp_f32 v169, v169\n"
      "s_nop 1\n"
      "v_mfma_f32_16x16x32_bf16 v[160:163], a[80:83], a[32:35], v[200:203]\n"
      "v_add_f32 v242, v242, v166\n"
      "v_cvt_pk_bf16_f32 v179, v166, v167\n"
      "s_nop 0\n"
      "v_mfma_f32_16x16x32_bf16 v[160:163], a[84:87], a[36:39], v[160:163]\n"
      "v_exp_f32 v170, v170\n"
      "s_nop 1\n"
      "v_mfma_f32_16x16x32_bf16 v[160:163], a[88:91], a[40:43], v[160:163]\n"
      "v_add_f32 v243, v243, v167\n"
      "v_add_f32 v242, v242, v168\n"
      "s_nop 1\n"
      "v_mfma_f32_16x16x32_bf16 v[160:163], a[92:95], a[44:47], v[160:163]\n"
      "v_exp_f32 v171, v171\n"
      "s_nop 0\n"
      "v_mfma_f32_16x16x32_bf16 v[164:167], a[108:111], a[44:47], v[200:203]\n"
      "s_nop 0\n"
      "s_nop 1\n"
      "v_mfma_f32_16x16x32_bf16 v[164:167], a[104:107], a[40:43], v[164:167]\n"
      "v_exp_f32 v172, v172\n"
      "s_nop 0\n"
      "v_mfma_f32_16x16x32_bf16 v[164:167], a[100:103], a[36:39], v[164:167]\n"
      "v_add_f32 v243, v243, v169\n"
      "v_cvt_pk_bf16_f32 v180, v168, v169\n"
      "s_nop 1\n"
      "v_mfma_f32_16x16x32_bf16 v[164:167], a[96:99], a[32:35], v[164:167]\n"
      "v_exp_f32 v173, v173\n"
      "s_nop 1\n"
      "v_mfma_f32_16x16x32_bf16 v[16:19], a[160:163], v[184:187], v[16:19]\n"
      "s_waitcnt lgkmcnt(0)\n"
      "v_add_f32 v242, v242, v170\n"
      "v_add_f32 v243, v243, v171\n"
      "s_nop 0\n"
      "v_mfma_f32_16x16x32_bf16 v[16:19], a[192:195], v[188:191], v[16:19]\n"
      "v_exp_f32 v174, v174\n"
      "s_nop 1\n"
      "v_mfma_f32_16x16x32_bf16 v[20:23], a[196:199], v[188:191], v[20:23]\n"
      "buffer_load_dwordx4 v238, s[36:39], s101 offen offset:2048 lds\n"
      "v_cvt_pk_bf16_f32 v181, v170, v171\n"
      "v_cvt_pk_bf16_f32 v182, v172, v173\n"
      "s_nop 0\n"
      "v_mfma_f32_16x16x32_bf16 v[20:23], a[164:167], v[184:187], v[20:23]\n"
      "v_exp_f32 v175, v175\n"
      "s_nop 1\n"
      "v_mfma_f32_16x16x32_bf16 v[24:27], a[168:171], v[184:187], v[24:27]\n"
      "v_add_f32 v242, v242, v172\n"
      "v_add_f32 v243, v243, v173\n"
      "s_nop 0\n"
      "v_mfma_f32_16x16x32_bf16 v[24:27], a[200:203], v[188:191], v[24:27]\n"
      "v_add_f32 v242, v242, v174\n"
      "v_cvt_pk_bf16_f32 v183, v174, v175\n"
      "s_nop 1\n"
      "v_mfma_f32_16x16x32_bf16 v[28:31], a[204:207], v[188:191], v[28:31]\n"
      "v_add_f32 v243, v243, v175\n"
      "s_nop 1\n"
      "v_mfma_f32_16x16x32_bf16 v[28:31], a[172:175], v[184:187], v[28:31]\n"
      "v_exp_f32 v160, v160\n"
      "s_nop 0\n"
      "v_mfma_f32_16x16x32_bf16 a[208:211], a[228:231], v[184:187], a[208:211]\n"
      "v_add_f32 v244, v244, v160\n"
      "s_nop 1\n"
      "v_mfma_f32_16x16x32_bf16 a[208:211], a[228:231], v[188:191], a[208:211]\n"
      "v_exp_f32 v161, v161\n"
      "s_nop 0\n"
      "v_mfma_f32_16x16x32_bf16 v[168:171], a[112:115], a[32:35], v[200:203]\n"
      "v_add_f32 v245, v245, v161\n"
      "v_cvt_pk_bf16_f32 v184, v160, v161\n"
      "s_nop 1\n"
      "v_mfma_f32_16x16x32_bf16 v[168:171], a[116:119], a[36:39], v[168:171]\n"
      "v_exp_f32 v162, v162\n"
      "s_nop 0\n"
      "v_mfma_f32_16x16x32_bf16 v[168:171], a[120:123], a[40:43], v[168:171]\n"
      "v_add_f32 v244, v244, v162\n"
      "s_nop 1\n"
      "v_mfma_f32_16x16x32_bf16 v[168:171], a[124:127], a[44:47], v[168:171]\n"
      "v_exp_f32 v163, v163\n"
      "s_nop 0\n"
      "v_mfma_f32_16x16x32_bf16 v[172:175], a[140:143], a[44:47], v[200:203]\n"
      "buffer_load_dwordx4 v239, s[36:39], s101 offen offset:3072 lds\n"
      "s_nop 1\n"
      "v_mfma_f32_16x16x32_bf16 v[172:175], a[136:139], a[40:43], v[172:175]\n"
      "v_exp_f32 v164, v164\n"
      "s_nop 0\n"
      "v_mfma_f32_16x16x32_bf16 v[172:175], a[132:135], a[36:39], v[172:175]\n"
      "v_add_f32 v245, v245, v163\n"
      "v_cvt_pk_bf16_f32 v185, v162, v163\n"
      "s_nop 1\n"
      "v_mfma_f32_16x16x32_bf16 v[172:175], a[128:131], a[32:35], v[172:175]\n"
      "v_exp_f32 v165, v165\n"
      "s_nop 0\n"
      "v_mfma_f32_16x16x32_bf16 v[32:35], a[144:147], v[176:179], v[32:35]\n"
      "v_add_f32 v244, v244, v164\n"
      "v_cvt_pk_bf16_f32 v186, v164, v165\n"
      "s_nop 1\n"
      "v_mfma_f32_16x16x32_bf16 v[32:35], a[176:179], v[180:183], v[32:35]\n"
      "v_exp_f32 v166, v166\n"
      "s_nop 0\n"
      "v_mfma_f32_16x16x32_bf16 v[36:39], a[180:183], v[180:183], v[36:39]\n"
      "v_add_f32 v245, v245, v165\n"
      "v_add_f32 v244, v244, v166\n"
      "s_nop 1\n"
      "v_mfma_f32_16x16x32_bf16 v[36:39], a[148:151], v[176:179], v[36:39]\n"
      "v_exp_f32 v167, v167\n"
      "s_nop 0\n"
      "v_mfma_f32_16x16x32_bf16 v[40:43], a[152:155], v[176:179], v[40:43]\n"
      "v_add_f32 v245, v245, v167\n"
      "v_cvt_pk_bf16_f32 v187, v166, v167\n"
      "s_nop 1\n"
      "v_mfma_f32_16x16x32_bf16 v[40:43], a[184:187], v[180:183], v[40:43]\n"
      "v_exp_f32 v168, v168\n"
      "s_nop 0\n"
      "v_mfma_f32_16x16x32_bf16 v[44:47], a[188:191], v[180:183], v[44:47]\n"
      "v_add_f32 v244, v244, v168\n"
      "s_nop 1\n"
      "v_mfma_f32_16x16x32_bf16 v[44:47], a[156:159], v[176:179], v[44:47]\n"
      "s_waitcnt vmcnt(4) lgkmcnt(0)\n"
      "s_barrier\n"
      "v_exp_f32 v169, v169\n"
      "s_nop 0\n"
      "v_mfma_f32_16x16x32_bf16 v[160:163], a[80:83], a[48:51], v[204:207]\n"
      "ds_read_b128 a[80:83], v212 offset:49152\n"
      "v_add_f32 v245, v245, v169\n"
      "s_nop 1\n"
      "v_mfma_f32_16x16x32_bf16 v[160:163], a[84:87], a[52:55], v[160:163]\n"
      "v_exp_f32 v170, v170\n"
      "s_nop 0\n"
      "v_mfma_f32_16x16x32_bf16 v[160:163], a[88:91], a[56:59], v[160:163]\n"
      "v_add_f32 v244, v244, v170\n"
      "v_cvt_pk_bf16_f32 v188, v168, v169\n"
      "s_nop 1\n"
      "v_mfma_f32_16x16x32_bf16 v[160:163], a[92:95], a[60:63], v[160:163]\n"
      "v_exp_f32 v171, v171\n"
      "s_nop 0\n"
      "v_mfma_f32_16x16x32_bf16 v[164:167], a[108:111], a[60:63], v[204:207]\n"
      "ds_read_b128 a[108:111], v215 offset:53248\n"
      "v_add_f32 v245, v245, v171\n"
      "s_nop 1\n"
      "v_mfma_f32_16x16x32_bf16 v[164:167], a[104:107], a[56:59], v[164:167]\n"
      "v_exp_f32 v172, v172\n"
      "s_nop 0\n"
      "v_mfma_f32_16x16x32_bf16 v[164:167], a[100:103], a[52:55], v[164:167]\n"
      "ds_read_b128 a[88:91], v214 offset:49152\n"
      "v_cvt_pk_bf16_f32 v189, v170, v171\n"
      "s_nop 1\n"
      "v_mfma_f32_16x16x32_bf16 v[164:167], a[96:99], a[48:51], v[164:167]\n"
      "v_exp_f32 v173, v173\n"
      "s_nop 0\n"
      "v_mfma_f32_16x16x32_bf16 v[48:51], a[160:163], v[176:179], v[48:51]\n"
      "ds_read_b128 a[104:107], v214 offset:53248\n"
      "v_add_f32 v244, v244, v173\n"
      "s_nop 1\n"
      "v_mfma_f32_16x16x32_bf16 v[48:51], a[192:195], v[180:183], v[48:51]\n"
      "v_exp_f32 v174, v174\n"
      "s_nop 0\n"
      "v_mfma_f32_16x16x32_bf16 v[52:55], a[196:199], v[180:183], v[52:55]\n"
      "s_mov_b32 m0, s58\n"
      "s_xor_b32 s101, 0x1bf00, s100\n"
      "buffer_load_dwordx4 v240, s[40:43], s101 offen offset:0 lds\n"
      "s_nop 0\n"
      "v_mfma_f32_16x16x32_bf16 v[52:55], a[164:167], v[176:179], v[52:55]\n"
      "v_exp_f32 v175, v175\n"
      "s_nop 1\n"
      "v_mfma_f32_16x16x32_bf16 v[56:59], a[168:171], v[176:179], v[56:59]\n"
      "ds_read_b128 a[92:95], v215 offset:49152\n"
      "v_add_f32 v245, v245, v172\n"
      "s_nop 0\n"
      "v_mfma_f32_16x16x32_bf16 v[56:59], a[200:203], v[180:183], v[56:59]\n"
      "v_exp_f32 v160, v160\n"
      "s_nop 1\n"
      "v_mfma_f32_16x16x32_bf16 v[60:63], a[204:207], v[180:183], v[60:63]\n"
      "ds_read_b128 a[84:87], v213 offset:49152\n"
      "v_cvt_pk_bf16_f32 v190, v172, v173\n"
      "s_nop 0\n"
      "v_mfma_f32_16x16x32_bf16 v[60:63], a[172:175], v[176:179], v[60:63]\n"
      "v_exp_f32 v161, v161\n"
      "s_nop 0\n"
      "v_mfma_f32_16x16x32_bf16 v[168:171], a[112:115], a[48:51], v[204:207]\n"
      "v_add_f32 v244, v244, v175\n"
      "v_cvt_pk_bf16_f32 v191, v174, v175\n"
      "s_nop 1\n"
      "v_mfma_f32_16x16x32_bf16 v[168:171], a[116:119], a[52:55], v[168:171]\n"
      "v_exp_f32 v162, v162\n"
      "s_nop 0\n"
      "v_mfma_f32_16x16x32_bf16 v[168:171], a[120:123], a[56:59], v[168:171]\n"
      "ds_read_b128 a[116:119], v217 offset:49152\n"
      "v_add_f32 v245, v245, v174\n"
      "s_nop 1\n"
      "v_mfma_f32_16x16x32_bf16 v[168:171], a[124:127], a[60:63], v[168:171]\n"
      "buffer_load_dwordx4 v241, s[40:43], s101 offen offset:1024 nt lds\n"
      "v_exp_f32 v163, v163\n"
      "s_nop 0\n"
      "v_mfma_f32_16x16x32_bf16 v[172:175], a[140:143], a[60:63], v[204:207]\n"
      "ds_read_b128 a[140:143], v219 offset:53248\n"
      "v_cvt_pk_bf16_f32 v177, v162, v163\n"
      "s_nop 0\n"
      "v_mfma_f32_16x16x32_bf16 v[172:175], a[136:139], a[56:59], v[172:175]\n"
      "v_exp_f32 v164, v164\n"
      "s_nop 1\n"
      "v_mfma_f32_16x16x32_bf16 v[172:175], a[132:135], a[52:55], v[172:175]\n"
      "ds_read_b128 a[100:103], v213 offset:53248\n"
      "v_cvt_pk_bf16_f32 v176, v160, v161\n"
      "s_nop 0\n"
      "v_mfma_f32_16x16x32_bf16 v[172:175], a[128:131], a[48:51], v[172:175]\n"
      "v_exp_f32 v165, v165\n"
      "s_nop 1\n"
      "v_mfma_f32_16x16x32_bf16 v[64:67], a[144:147], v[184:187], v[64:67]\n"
      "ds_read_b128 a[136:139], v218 offset:53248\n"
      "v_cvt_pk_bf16_f32 v178, v164, v165\n"
      "s_nop 0\n"
      "v_mfma_f32_16x16x32_bf16 v[64:67], a[176:179], v[188:191], v[64:67]\n"
      "v_exp_f32 v166, v166\n"
      "s_nop 0\n"
      "v_mfma_f32_16x16x32_bf16 v[68:71], a[180:183], v[188:191], v[68:71]\n"
      "ds_read_b128 a[96:99], v212 offset:53248\n"
      "s_nop 1\n"
      "v_mfma_f32_16x16x32_bf16 v[68:71], a[148:151], v[184:187], v[68:71]\n"
      "v_exp_f32 v167, v167\n"
      "s_nop 0\n"
      "v_mfma_f32_16x16x32_bf16 v[72:75], a[152:155], v[184:187], v[72:75]\n"
      "buffer_load_dwordx4 v240, s[40:43], s101 offen offset:2048 lds\n"
      "s_nop 0\n"
      "v_mfma_f32_16x16x32_bf16 v[72:75], a[184:187], v[188:191], v[72:75]\n"
      "v_exp_f32 v168, v168\n"
      "s_nop 1\n"
      "v_mfma_f32_16x16x32_bf16 v[76:79], a[188:191], v[188:191], v[76:79]\n"
      "ds_read_b128 a[132:135], v217 offset:53248\n"
      "v_cvt_pk_bf16_f32 v179, v166, v167\n"
      "s_nop 0\n"
      "v_mfma_f32_16x16x32_bf16 v[76:79], a[156:159], v[184:187], v[76:79]\n"
      "s_waitcnt lgkmcnt(11)\n"
      "v_exp_f32 v169, v169\n"
      "s_nop 0\n"
      "v_mfma_f32_16x16x32_bf16 v[160:163], a[80:83], a[64:67], v[208:211]\n"
      "s_waitcnt lgkmcnt(6)\n"
      "ds_read_b128 a[124:127], v219 offset:49152\n"
      "v_cvt_pk_bf16_f32 v180, v168, v169\n"
      "s_nop 1\n"
      "v_mfma_f32_16x16x32_bf16 v[160:163], a[84:87], a[68:71], v[160:163]\n"
      "v_exp_f32 v170, v170\n"
      "s_nop 0\n"
      "v_mfma_f32_16x16x32_bf16 v[160:163], a[88:91], a[72:75], v[160:163]\n"
      "ds_read_b128 a[128:131], v216 offset:53248\n"
      "s_nop 0\n"
      "v_mfma_f32_16x16x32_bf16 v[160:163], a[92:95], a[76:79], v[160:163]\n"
      "v_exp_f32 v171, v171\n"
      "s_nop 1\n"
      "v_mfma_f32_16x16x32_bf16 v[164:167], a[108:111], a[76:79], v[208:211]\n"
      "ds_read_b128 a[120:123], v218 offset:49152\n"
      "v_cvt_pk_bf16_f32 v181, v170, v171\n"
      "s_nop 0\n"
      "v_mfma_f32_16x16x32_bf16 v[164:167], a[104:107], a[72:75], v[164:167]\n"
      "s_waitcnt lgkmcnt(6)\n"
      "v_exp_f32 v172, v172\n"
      "s_nop 0\n"
      "v_mfma_f32_16x16x32_bf16 v[164:167], a[100:103], a[68:71], v[164:167]\n"
      "s_waitcnt lgkmcnt(4)\n"
      "ds_read_b128 a[112:115], v216 offset:49152\n"
      "s_nop 1\n"
      "v_mfma_f32_16x16x32_bf16 v[164:167], a[96:99], a[64:67], v[164:167]\n"
      "v_exp_f32 v173, v173\n"
      "s_nop 0\n"
      "v_mfma_f32_16x16x32_bf16 v[80:83], a[160:163], v[184:187], v[80:83]\n"
      "v_cvt_pk_bf16_f32 v182, v172, v173\n"
      "s_nop 0\n"
      "v_mfma_f32_16x16x32_bf16 v[80:83], a[192:195], v[188:191], v[80:83]\n"
      "v_exp_f32 v174, v174\n"
      "s_nop 0\n"
      "v_mfma_f32_16x16x32_bf16 v[84:87], a[196:199], v[188:191], v[84:87]\n"
      "buffer_load_dwordx4 v241, s[40:43], s101 offen offset:3072 nt lds\n"
      "s_nop 1\n"
      "v_mfma_f32_16x16x32_bf16 v[84:87], a[164:167], v[184:187], v[84:87]\n"
      "v_exp_f32 v175, v175\n"
      "s_nop 0\n"
      "v_mfma_f32_16x16x32_bf16 v[88:91], a[168:171], v[184:187], v[88:91]\n"
      "v_cvt_pk_bf16_f32 v183, v174, v175\n"
      "s_nop 0\n"
      "v_mfma_f32_16x16x32_bf16 v[88:91], a[200:203], v[188:191], v[88:91]\n"
      "v_exp_f32 v160, v160\n"
      "s_nop 1\n"
      "v_mfma_f32_16x16x32_bf16 v[92:95], a[204:207], v[188:191], v[92:95]\n"
      "s_nop 0\n"
      "s_nop 0\n"
      "v_mfma_f32_16x16x32_bf16 v[92:95], a[172:175], v[184:187], v[92:95]\n"
      "v_exp_f32 v161, v161\n"
      "s_nop 0\n"
      "v_mfma_f32_16x16x32_bf16 v[168:171], a[80:83], a[0:3], v[192:195]\n"
      "v_add_f32 v246, v246, v161\n"
      "v_cvt_pk_bf16_f32 v184, v160, v161\n"
      "s_nop 0\n"
      "v_mfma_f32_16x16x32_bf16 v[168:171], a[84:87], a[4:7], v[168:171]\n"
      "v_exp_f32 v162, v162\n"
      "s_nop 1\n"
      "v_mfma_f32_16x16x32_bf16 v[168:171], a[88:91], a[8:11], v[168:171]\n"
      "v_add_f32 v247, v247, v160\n"
      "v_add_f32 v246, v246, v162\n"
      "s_nop 0\n"
      "v_mfma_f32_16x16x32_bf16 v[168:171], a[92:95], a[12:15], v[168:171]\n"
      "v_exp_f32 v163, v163\n"
      "s_nop 0\n"
      "v_mfma_f32_16x16x32_bf16 v[172:175], a[108:111], a[12:15], v[192:195]\n"
      "v_add_f32 v247, v247, v163\n"
      "v_cvt_pk_bf16_f32 v185, v162, v163\n"
      "s_nop 0\n"
      "v_mfma_f32_16x16x32_bf16 v[172:175], a[104:107], a[8:11], v[172:175]\n"
      "v_exp_f32 v164, v164\n"
      "s_nop 1\n"
      "v_mfma_f32_16x16x32_bf16 v[172:175], a[100:103], a[4:7], v[172:175]\n"
      "s_mov_b32 m0, s55\n"
      "s_xor_b32 s101, 0x1fc00, s100\n"
      "buffer_load_dwordx4 v236, s[36:39], s101 offen offset:0 lds\n"
      "v_add_f32 v246, v246, v164\n"
      "s_nop 0\n"
      "v_mfma_f32_16x16x32_bf16 v[172:175], a[96:99], a[0:3], v[172:175]\n"
      "v_exp_f32 v165, v165\n"
      "s_nop 0\n"
      "v_mfma_f32_16x16x32_bf16 v[96:99], a[144:147], v[176:179], v[96:99]\n"
      "v_add_f32 v247, v247, v165\n"
      "v_cvt_pk_bf16_f32 v186, v164, v165\n"
      "s_nop 0\n"
      "v_mfma_f32_16x16x32_bf16 v[96:99], a[176:179], v[180:183], v[96:99]\n"
      "v_exp_f32 v166, v166\n"
      "s_nop 0\n"
      "v_mfma_f32_16x16x32_bf16 v[100:103], a[180:183], v[180:183], v[100:103]\n"
      "ds_read_b64_tr_b16 a[146:147], v220 offset:53248\n"
      "ds_read_b64_tr_b16 a[182:183], v229 offset:53248\n"
      "s_nop 1\n"
      "v_mfma_f32_16x16x32_bf16 v[100:103], a[148:151], v[176:179], v[100:103]\n"
      "v_exp_f32 v167, v167\n"
      "s_nop 0\n"
      "v_mfma_f32_16x16x32_bf16 v[104:107], a[152:155], v[176:179], v[104:107]\n"
      "ds_read_b64_tr_b16 a[150:151], v221 offset:53248\n"
      "v_add_f32 v246, v246, v167\n"
      "s_nop 0\n"
      "v_mfma_f32_16x16x32_bf16 v[104:107], a[184:187], v[180:183], v[104:107]\n"
      "v_exp_f32 v168, v168\n"
      "s_nop 0\n"
      "v_mfma_f32_16x16x32_bf16 v[108:111], a[188:191], v[180:183], v[108:111]\n"
      "ds_read_b64_tr_b16 a[144:145], v220 offset:49152\n"
      "v_cvt_pk_bf16_f32 v187, v166, v167\n"
      "s_nop 0\n"
      "v_mfma_f32_16x16x32_bf16 v[108:111], a[156:159], v[176:179], v[108:111]\n"
      "s_waitcnt lgkmcnt(4)\n"
      "v_exp_f32 v169, v169\n"
      "s_nop 1\n"
      "v_mfma_f32_16x16x32_bf16 v[160:163], a[112:115], a[0:3], v[192:195]\n"
      "buffer_load_dwordx4 v237, s[36:39], s101 offen offset:1024 lds\n"
      "s_nop 0\n"
      "v_mfma_f32_16x16x32_bf16 v[160:163], a[116:119], a[4:7], v[160:163]\n"
      "v_exp_f32 v170, v170\n"
      "s_nop 0\n"
      "v_mfma_f32_16x16x32_bf16 v[160:163], a[120:123], a[8:11], v[160:163]\n"
      "v_add_f32 v247, v247, v166\n"
      "v_cvt_pk_bf16_f32 v188, v168, v169\n"
      "s_nop 0\n"
      "v_mfma_f32_16x16x32_bf16 v[160:163], a[124:127], a[12:15], v[160:163]\n"
      "v_exp_f32 v171, v171\n"
      "s_nop 0\n"
      "v_mfma_f32_16x16x32_bf16 v[164:167], a[140:143], a[12:15], v[192:195]\n"
      "ds_read_b64_tr_b16 a[148:149], v221 offset:49152\n"
      "ds_read_b64_tr_b16 a[154:155], v222 offset:53248\n"
      "s_nop 0\n"
      "v_mfma_f32_16x16x32_bf16 v[164:167], a[136:139], a[8:11], v[164:167]\n"
      "v_exp_f32 v172, v172\n"
      "s_nop 1\n"
      "v_mfma_f32_16x16x32_bf16 v[164:167], a[132:135], a[4:7], v[164:167]\n"
      "ds_read_b64_tr_b16 a[156:157], v223 offset:49152\n"
      "ds_read_b64_tr_b16 a[158:159], v223 offset:53248\n"
      "s_nop 0\n"
      "v_mfma_f32_16x16x32_bf16 v[164:167], a[128:131], a[0:3], v[164:167]\n"
      "v_exp_f32 v173, v173\n"
      "s_nop 0\n"
      "v_mfma_f32_16x16x32_bf16 v[112:115], a[160:163], v[176:179], v[112:115]\n"
      "v_cvt_pk_bf16_f32 v189, v170, v171\n"
      "v_cvt_pk_bf16_f32 v190, v172, v173\n"
      "s_nop 0\n"
      "v_mfma_f32_16x16x32_bf16 v[112:115], a[192:195], v[180:183], v[112:115]\n"
      "v_exp_f32 v174, v174\n"
      "s_nop 0\n"
      "v_mfma_f32_16x16x32_bf16 v[116:119], a[196:199], v[180:183], v[116:119]\n"
      "ds_read_b64_tr_b16 a[152:153], v222 offset:49152\n"
      "ds_read_b64_tr_b16 a[160:161], v224 offset:49152\n"
      "s_nop 0\n"
      "v_mfma_f32_16x16x32_bf16 v[116:119], a[164:167], v[176:179], v[116:119]\n"
      "v_exp_f32 v175, v175\n"
      "s_nop 1\n"
      "v_mfma_f32_16x16x32_bf16 v[120:123], a[168:171], v[176:179], v[120:123]\n"
      "ds_read_b64_tr_b16 a[162:163], v224 offset:53248\n"
      "ds_read_b64_tr_b16 a[170:171], v226 offset:53248\n"
      "s_nop 0\n"
      "v_mfma_f32_16x16x32_bf16 v[120:123], a[200:203], v[180:183], v[120:123]\n"
      "ds_read_b64_tr_b16 a[196:197], v233 offset:49152\n"
      "ds_read_b64_tr_b16 a[198:199], v233 offset:53248\n"
      "s_nop 0\n"
      "v_mfma_f32_16x16x32_bf16 v[124:127], a[204:207], v[180:183], v[124:127]\n"
      "ds_read_b64_tr_b16 a[204:205], v235 offset:49152\n"
      "ds_read_b64_tr_b16 a[206:207], v235 offset:53248\n"
      "s_nop 0\n"
      "v_mfma_f32_16x16x32_bf16 v[124:127], a[172:175], v[176:179], v[124:127]\n"
      "v_exp_f32 v160, v160\n"
      "s_nop 0\n"
      "v_mfma_f32_16x16x32_bf16 a[220:223], a[228:231], v[176:179], a[220:223]\n"
      "ds_read_b64_tr_b16 a[166:167], v225 offset:53248\n"
      "ds_read_b64_tr_b16 a[168:169], v226 offset:49152\n"
      "s_nop 0\n"
      "v_mfma_f32_16x16x32_bf16 a[220:223], a[228:231], v[180:183], a[220:223]\n"
      "v_exp_f32 v161, v161\n"
      "s_nop 0\n"
      "v_mfma_f32_16x16x32_bf16 v[168:171], a[80:83], a[16:19], v[196:199]\n"
      "buffer_load_dwordx4 v238, s[36:39], s101 offen offset:2048 lds\n"
      "s_nop 0\n"
      "v_mfma_f32_16x16x32_bf16 v[168:171], a[84:87], a[20:23], v[168:171]\n"
      "v_exp_f32 v162, v162\n"
      "s_nop 0\n"
      "v_mfma_f32_16x16x32_bf16 v[168:171], a[88:91], a[24:27], v[168:171]\n"
      "ds_read_b64_tr_b16 a[164:165], v225 offset:49152\n"
      "v_cvt_pk_bf16_f32 v191, v174, v175\n"
      "s_nop 1\n"
      "v_mfma_f32_16x16x32_bf16 v[168:171], a[92:95], a[28:31], v[168:171]\n"
      "v_exp_f32 v163, v163\n"
      "s_nop 0\n"
      "v_mfma_f32_16x16x32_bf16 v[172:175], a[108:111], a[28:31], v[196:199]\n"
      "ds_read_b64_tr_b16 a[172:173], v227 offset:49152\n"
      "ds_read_b64_tr_b16 a[174:175], v227 offset:53248\n"
      "s_nop 0\n"
      "v_mfma_f32_16x16x32_bf16 v[172:175], a[104:107], a[24:27], v[172:175]\n"
      "v_exp_f32 v164, v164\n"
      "s_nop 0\n"
      "v_mfma_f32_16x16x32_bf16 v[172:175], a[100:103], a[20:23], v[172:175]\n"
      "ds_read_b64_tr_b16 a[200:201], v234 offset:49152\n"
      "ds_read_b64_tr_b16 a[202:203], v234 offset:53248\n"
      "s_nop 0\n"
      "v_mfma_f32_16x16x32_bf16 v[172:175], a[96:99], a[16:19], v[172:175]\n"
      "s_waitcnt lgkmcnt(15)\n"
      "v_exp_f32 v165, v165\n"
      "s_nop 0\n"
      "v_mfma_f32_16x16x32_bf16 v[128:131], a[144:147], v[184:187], v[128:131]\n"
      "s_waitcnt lgkmcnt(15)\n"
      "ds_read_b64_tr_b16 a[186:187], v230 offset:53248\n"
      "ds_read_b64_tr_b16 a[194:195], v232 offset:53248\n"
      "s_nop 0\n"
      "v_mfma_f32_16x16x32_bf16 v[132:135], a[148:151], v[184:187], v[132:135]\n"
      "s_waitcnt lgkmcnt(15)\n"
      "v_exp_f32 v166, v166\n"
      "s_nop 0\n"
      "v_mfma_f32_16x16x32_bf16 v[136:139], a[152:155], v[184:187], v[136:139]\n"
      "buffer_load_dwordx4 v239, s[36:39], s101 offen offset:3072 lds\n"
      "ds_read_b64_tr_b16 a[176:177], v228 offset:49152\n"
      "ds_read_b64_tr_b16 a[184:185], v230 offset:49152\n"
      "s_nop 0\n"
      "v_mfma_f32_16x16x32_bf16 v[140:143], a[156:159], v[184:187], v[140:143]\n"
      "s_waitcnt lgkmcnt(15)\n"
      "v_exp_f32 v167, v167\n"
      "s_nop 0\n"
      "v_mfma_f32_16x16x32_bf16 v[144:147], a[160:163], v[184:187], v[144:147]\n"
      "s_waitcnt lgkmcnt(8)\n"
      "ds_read_b64_tr_b16 a[178:179], v228 offset:53248\n"
      "ds_read_b64_tr_b16 a[180:181], v229 offset:49152\n"
      "s_nop 0\n"
      "v_mfma_f32_16x16x32_bf16 v[148:151], a[164:167], v[184:187], v[148:151]\n"
      "v_exp_f32 v168, v168\n"
      "s_nop 0\n"
      "v_mfma_f32_16x16x32_bf16 v[152:155], a[168:171], v[184:187], v[152:155]\n"
      "s_waitcnt lgkmcnt(8)\n"
      "v_cvt_pk_bf16_f32 v176, v160, v161\n"
      "v_cvt_pk_bf16_f32 v177, v162, v163\n"
      "s_nop 0\n"
      "v_mfma_f32_16x16x32_bf16 v[156:159], a[172:175], v[184:187], v[156:159]\n"
      "v_exp_f32 v169, v169\n"
      "s_nop 0\n"
      "v_mfma_f32_16x16x32_bf16 v[160:163], a[112:115], a[16:19], v[196:199]\n"
      "v_cvt_pk_bf16_f32 v178, v164, v165\n"
      "v_cvt_pk_bf16_f32 v179, v166, v167\n"
      "s_nop 0\n"
      "v_mfma_f32_16x16x32_bf16 v[160:163], a[116:119], a[20:23], v[160:163]\n"
      "v_exp_f32 v170, v170\n"
      "s_nop 0\n"
      "v_mfma_f32_16x16x32_bf16 v[160:163], a[120:123], a[24:27], v[160:163]\n"
      "s_nop 0\n"
      "s_nop 0\n"
      "v_mfma_f32_16x16x32_bf16 v[160:163], a[124:127], a[28:31], v[160:163]\n"
      "v_exp_f32 v171, v171\n"
      "s_nop 0\n"
      "v_mfma_f32_16x16x32_bf16 v[164:167], a[140:143], a[28:31], v[196:199]\n"
      "ds_read_b64_tr_b16 a[188:189], v231 offset:49152\n"
      "ds_read_b64_tr_b16 a[190:191], v231 offset:53248\n"
      "s_nop 0\n"
      "v_mfma_f32_16x16x32_bf16 v[164:167], a[136:139], a[24:27], v[164:167]\n"
      "v_exp_f32 v172, v172\n"
      "s_nop 0\n"
      "v_mfma_f32_16x16x32_bf16 v[164:167], a[132:135], a[20:23], v[164:167]\n"
      "s_mov_b32 m0, s59\n"
      "s_xor_b32 s101, 0x1fc00, s100\n"
      "buffer_load_dwordx4 v240, s[40:43], s101 offen offset:0 lds\n"
      "ds_read_b64_tr_b16 a[192:193], v232 offset:49152\n"
      "v_add_f32 v242, v242, v168\n"
      "s_nop 0\n"
      "v_mfma_f32_16x16x32_bf16 v[164:167], a[128:131], a[16:19], v[164:167]\n"
      "v_exp_f32 v173, v173\n"
      "s_nop 0\n"
      "v_mfma_f32_16x16x32_bf16 v[0:3], a[144:147], v[188:191], v[0:3]\n"
      "s_waitcnt lgkmcnt(4)\n"
      "v_add_f32 v243, v243, v169\n"
      "v_cvt_pk_bf16_f32 v180, v168, v169\n"
      "s_nop 0\n"
      "v_mfma_f32_16x16x32_bf16 v[0:3], a[176:179], v[176:179], v[0:3]\n"
      "s_waitcnt lgkmcnt(3)\n"
      "v_exp_f32 v174, v174\n"
      "s_nop 0\n"
      "v_mfma_f32_16x16x32_bf16 v[4:7], a[180:183], v[176:179], v[4:7]\n"
      "v_add_f32 v242, v242, v171\n"
      "v_cvt_pk_bf16_f32 v181, v170, v171\n"
      "s_nop 0\n"
      "v_mfma_f32_16x16x32_bf16 v[4:7], a[148:151], v[188:191], v[4:7]\n"
      "v_exp_f32 v175, v175\n"
      "s_nop 0\n"
      "v_mfma_f32_16x16x32_bf16 v[8:11], a[152:155], v[188:191], v[8:11]\n"
      "v_add_f32 v243, v243, v170\n"
      "v_add_f32 v242, v242, v172\n"
      "s_nop 0\n"
      "v_mfma_f32_16x16x32_bf16 v[8:11], a[184:187], v[176:179], v[8:11]\n"
      "s_waitcnt lgkmcnt(1)\n"
      "v_exp_f32 v160, v160\n"
      "s_nop 0\n"
      "v_mfma_f32_16x16x32_bf16 v[12:15], a[188:191], v[176:179], v[12:15]\n"
      "v_add_f32 v243, v243, v173\n"
      "v_cvt_pk_bf16_f32 v182, v172, v173\n"
      "s_nop 0\n"
      "v_mfma_f32_16x16x32_bf16 v[12:15], a[156:159], v[188:191], v[12:15]\n"
      "v_exp_f32 v161, v161\n"
      "s_nop 0\n"
      "v_mfma_f32_16x16x32_bf16 v[168:171], a[80:83], a[32:35], v[200:203]\n"
      "v_add_f32 v242, v242, v174\n"
      "v_cvt_pk_bf16_f32 v183, v174, v175\n"
      "s_nop 0\n"
      "v_mfma_f32_16x16x32_bf16 v[168:171], a[84:87], a[36:39], v[168:171]\n"
      "v_exp_f32 v162, v162\n"
      "s_nop 0\n"
      "v_mfma_f32_16x16x32_bf16 v[168:171], a[88:91], a[40:43], v[168:171]\n"
      "v_add_f32 v243, v243, v175\n"
      "v_add_f32 v242, v242, v160\n"
      "s_nop 0\n"
      "v_mfma_f32_16x16x32_bf16 v[168:171], a[92:95], a[44:47], v[168:171]\n"
      "v_exp_f32 v163, v163\n"
      "s_nop 0\n"
      "v_mfma_f32_16x16x32_bf16 v[172:175], a[108:111], a[44:47], v[200:203]\n"
      "buffer_load_dwordx4 v241, s[40:43], s101 offen offset:1024 nt lds\n"
      "s_nop 0\n"
      "v_mfma_f32_16x16x32_bf16 v[172:175], a[104:107], a[40:43], v[172:175]\n"
      "v_exp_f32 v164, v164\n"
      "s_nop 0\n"
      "v_mfma_f32_16x16x32_bf16 v[172:175], a[100:103], a[36:39], v[172:175]\n"
      "v_add_f32 v243, v243, v161\n"
      "v_cvt_pk_bf16_f32 v184, v160, v161\n"
      "s_nop 0\n"
      "v_mfma_f32_16x16x32_bf16 v[172:175], a[96:99], a[32:35], v[172:175]\n"
      "v_exp_f32 v165, v165\n"
      "s_nop 0\n"
      "v_mfma_f32_16x16x32_bf16 v[16:19], a[160:163], v[188:191], v[16:19]\n"
      "s_waitcnt lgkmcnt(0)\n"
      "v_add_f32 v242, v242, v162\n"
      "v_add_f32 v243, v243, v163\n"
      "s_nop 0\n"
      "v_mfma_f32_16x16x32_bf16 v[16:19], a[192:195], v[176:179], v[16:19]\n"
      "v_exp_f32 v166, v166\n"
      "s_nop 0\n"
      "v_mfma_f32_16x16x32_bf16 v[20:23], a[196:199], v[176:179], v[20:23]\n"
      "v_cvt_pk_bf16_f32 v185, v162, v163\n"
      "v_cvt_pk_bf16_f32 v186, v164, v165\n"
      "s_nop 0\n"
      "v_mfma_f32_16x16x32_bf16 v[20:23], a[164:167], v[188:191], v[20:23]\n"
      "v_exp_f32 v167, v167\n"
      "s_nop 0\n"
      "v_mfma_f32_16x16x32_bf16 v[24:27], a[168:171], v[188:191], v[24:27]\n"
      "v_add_f32 v242, v242, v164\n"
      "v_add_f32 v243, v243, v165\n"
      "v_mfma_f32_16x16x32_bf16 v[24:27], a[200:203], v[176:179], v[24:27]\n"
      "v_add_f32 v242, v242, v166\n"
      "v_cvt_pk_bf16_f32 v187, v166, v167\n"
      "s_nop 0\n"
      "v_mfma_f32_16x16x32_bf16 v[28:31], a[204:207], v[176:179], v[28:31]\n"
      "buffer_load_dwordx4 v240, s[40:43], s101 offen offset:2048 lds\n"
      "v_add_f32 v243, v243, v167\n"
      "s_nop 0\n"
      "v_mfma_f32_16x16x32_bf16 v[28:31], a[172:175], v[188:191], v[28:31]\n"
      "v_exp_f32 v168, v168\n"
      "s_nop 0\n"
      "v_mfma_f32_16x16x32_bf16 a[208:211], a[228:231], v[188:191], a[208:211]\n"
      "v_add_f32 v244, v244, v168\n"
      "s_nop 0\n"
      "v_mfma_f32_16x16x32_bf16 a[208:211], a[228:231], v[176:179], a[208:211]\n"
      "v_exp_f32 v169, v169\n"
      "s_nop 0\n"
      "v_mfma_f32_16x16x32_bf16 v[160:163], a[112:115], a[32:35], v[200:203]\n"
      "v_add_f32 v245, v245, v169\n"
      "v_cvt_pk_bf16_f32 v188, v168, v169\n"
      "s_nop 0\n"
      "v_mfma_f32_16x16x32_bf16 v[160:163], a[116:119], a[36:39], v[160:163]\n"
      "v_exp_f32 v170, v170\n"
      "s_nop 0\n"
      "v_mfma_f32_16x16x32_bf16 v[160:163], a[120:123], a[40:43], v[160:163]\n"
      "v_add_f32 v244, v244, v170\n"
      "s_nop 0\n"
      "v_mfma_f32_16x16x32_bf16 v[160:163], a[124:127], a[44:47], v[160:163]\n"
      "v_exp_f32 v171, v171\n"
      "v_mfma_f32_16x16x32_bf16 v[164:167], a[140:143], a[44:47], v[200:203]\n"
      "s_nop 0\n"
      "s_nop 0\n"
      "v_mfma_f32_16x16x32_bf16 v[164:167], a[136:139], a[40:43], v[164:167]\n"
      "v_exp_f32 v172, v172\n"
      "s_nop 0\n"
      "v_mfma_f32_16x16x32_bf16 v[164:167], a[132:135], a[36:39], v[164:167]\n"
      "v_add_f32 v245, v245, v171\n"
      "v_cvt_pk_bf16_f32 v189, v170, v171\n"
      "s_nop 0\n"
      "v_mfma_f32_16x16x32_bf16 v[164:167], a[128:131], a[32:35], v[164:167]\n"
      "v_exp_f32 v173, v173\n"
      "s_nop 0\n"
      "v_mfma_f32_16x16x32_bf16 v[32:35], a[144:147], v[180:183], v[32:35]\n"
      "buffer_load_dwordx4 v241, s[40:43], s101 offen offset:3072 nt lds\n"
      "v_add_f32 v244, v244, v172\n"
      "v_cvt_pk_bf16_f32 v190, v172, v173\n"
      "s_nop 0\n"
      "v_mfma_f32_16x16x32_bf16 v[32:35], a[176:179], v[184:187], v[32:35]\n"
      "v_exp_f32 v174, v174\n"
      "v_mfma_f32_16x16x32_bf16 v[36:39], a[180:183], v[184:187], v[36:39]\n"
      "v_add_f32 v245, v245, v173\n"
      "v_add_f32 v244, v244, v174\n"
      "s_nop 0\n"
      "v_mfma_f32_16x16x32_bf16 v[36:39], a[148:151], v[180:183], v[36:39]\n"
      "v_exp_f32 v175, v175\n"
      "s_nop 0\n"
      "v_mfma_f32_16x16x32_bf16 v[40:43], a[152:155], v[180:183], v[40:43]\n"
      "v_add_f32 v245, v245, v175\n"
      "v_cvt_pk_bf16_f32 v191, v174, v175\n"
      "s_nop 0\n"
      "v_mfma_f32_16x16x32_bf16 v[40:43], a[184:187], v[184:187], v[40:43]\n"
      "v_exp_f32 v160, v160\n"
      "s_nop 0\n"
      "v_mfma_f32_16x16x32_bf16 v[44:47], a[188:191], v[184:187], v[44:47]\n"
      "s_mov_b32 s69, 0\n"
      "v_add_f32 v244, v244, v160\n"
      "s_nop 0\n"
      "v_mfma_f32_16x16x32_bf16 v[44:47], a[156:159], v[180:183], v[44:47]\n"
      "s_waitcnt vmcnt(8) lgkmcnt(0)\n"
      "s_barrier\n"
      "v_exp_f32 v161, v161\n"
      "v_mfma_f32_16x16x32_bf16 v[168:171], a[80:83], a[48:51], v[204:207]\n"
      "ds_read_b128 a[80:83], v212 offset:0\n"
      "v_add_f32 v245, v245, v161\n"
      "s_nop 0\n"
      "v_mfma_f32_16x16x32_bf16 v[168:171], a[84:87], a[52:55], v[168:171]\n"
      "v_exp_f32 v162, v162\n"
      "s_nop 0\n"
      "v_mfma_f32_16x16x32_bf16 v[168:171], a[88:91], a[56:59], v[168:171]\n"
      "v_add_f32 v244, v244, v162\n"
      "v_cvt_pk_bf16_f32 v176, v160, v161\n"
      "s_nop 0\n"
      "v_mfma_f32_16x16x32_bf16 v[168:171], a[92:95], a[60:63], v[168:171]\n"
      "v_exp_f32 v163, v163\n"
      "s_nop 0\n"
      "v_mfma_f32_16x16x32_bf16 v[172:175], a[108:111], a[60:63], v[204:207]\n"
      "ds_read_b128 a[108:111], v215 offset:4096\n"
      "v_add_f32 v245, v245, v163\n"
      "v_mfma_f32_16x16x32_bf16 v[172:175], a[104:107], a[56:59], v[172:175]\n"
      "v_exp_f32 v164, v164\n"
      "s_nop 0\n"
      "v_mfma_f32_16x16x32_bf16 v[172:175], a[100:103], a[52:55], v[172:175]\n"
      "ds_read_b128 a[88:91], v214 offset:0\n"
      "v_cvt_pk_bf16_f32 v177, v162, v163\n"
      "s_nop 0\n"
      "v_mfma_f32_16x16x32_bf16 v[172:175], a[96:99], a[48:51], v[172:175]\n"
      "v_exp_f32 v165, v165\n"
      "s_nop 0\n"
      "v_mfma_f32_16x16x32_bf16 v[48:51], a[160:163], v[180:183], v[48:51]\n"
      "ds_read_b128 a[104:107], v214 offset:4096\n"
      "v_add_f32 v244, v244, v165\n"
      "s_nop 0\n"
      "v_mfma_f32_16x16x32_bf16 v[48:51], a[192:195], v[184:187], v[48:51]\n"
      "s_mov_b32 m0, s56\n"
      "s_xor_b32 s101, 0x1fd00, s100\n"
      "v_exp_f32 v166, v166\n"
      "v_mfma_f32_16x16x32_bf16 v[52:55], a[196:199], v[184:187], v[52:55]\n"
      "buffer_load_dwordx4 v236, s[36:39], s101 offen offset:0 lds\n"
      "s_nop 0\n"
      "v_mfma_f32_16x16x32_bf16 v[52:55], a[164:167], v[180:183], v[52:55]\n"
      "v_exp_f32 v167, v167\n"
      "s_nop 0\n"
      "v_mfma_f32_16x16x32_bf16 v[56:59], a[168:171], v[180:183], v[56:59]\n"
      "ds_read_b128 a[92:95], v215 offset:0\n"
      "v_add_f32 v245, v245, v164\n"
      "s_nop 0\n"
      "v_mfma_f32_16x16x32_bf16 v[56:59], a[200:203], v[184:187], v[56:59]\n"
      "v_exp_f32 v168, v168\n"
      "v_mfma_f32_16x16x32_bf16 v[60:63], a[204:207], v[184:187], v[60:63]\n"
      "ds_read_b128 a[84:87], v213 offset:0\n"
      "v_cvt_pk_bf16_f32 v178, v164, v165\n"
      "s_nop 0\n"
      "v_mfma_f32_16x16x32_bf16 v[60:63], a[172:175], v[180:183], v[60:63]\n"
      "v_exp_f32 v169, v169\n"
      "s_nop 0\n"
      "v_mfma_f32_16x16x32_bf16 v[160:163], a[112:115], a[48:51], v[204:207]\n"
      "v_add_f32 v244, v244, v167\n"
      "v_cvt_pk_bf16_f32 v179, v166, v167\n"
      "s_nop 0\n"
      "v_mfma_f32_16x16x32_bf16 v[160:163], a[116:119], a[52:55], v[160:163]\n"
      "v_exp_f32 v170, v170\n"
      "v_mfma_f32_16x16x32_bf16 v[160:163], a[120:123], a[56:59], v[160:163]\n"
      "ds_read_b128 a[116:119], v217 offset:0\n"
      "v_add_f32 v245, v245, v166\n"
      "s_nop 0\n"
      "v_mfma_f32_16x16x32_bf16 v[160:163], a[124:127], a[60:63], v[160:163]\n"
      "v_exp_f32 v171, v171\n"
      "s_nop 0\n"
      "v_mfma_f32_16x16x32_bf16 v[164:167], a[140:143], a[60:63], v[204:207]\n"
      "ds_read_b128 a[140:143], v219 offset:4096\n"
      "v_cvt_pk_bf16_f32 v181, v170, v171\n"
      "v_mfma_f32_16x16x32_bf16 v[164:167], a[136:139], a[56:59], v[164:167]\n"
      "v_exp_f32 v172, v172\n"
      "s_nop 0\n"
      "v_mfma_f32_16x16x32_bf16 v[164:167], a[132:135], a[52:55], v[164:167]\n"
      "ds_read_b128 a[100:103], v213 offset:4096\n"
      "v_cvt_pk_bf16_f32 v180, v168, v169\n"
      "s_nop 0\n"
      "v_mfma_f32_16x16x32_bf16 v[164:167], a[128:131], a[48:51], v[164:167]\n"
      "v_exp_f32 v173, v173\n"
      "s_nop 0\n"
      "v_mfma_f32_16x16x32_bf16 v[64:67], a[144:147], v[188:191], v[64:67]\n"
      "ds_read_b128 a[136:139], v218 offset:4096\n"
      "v_cvt_pk_bf16_f32 v182, v172, v173\n"
      "v_mfma_f32_16x16x32_bf16 v[64:67], a[176:179], v[176:179], v[64:67]\n"
      "v_exp_f32 v174, v174\n"
      "s_nop 0\n"
      "v_mfma_f32_16x16x32_bf16 v[68:71], a[180:183], v[176:179], v[68:71]\n"
      "ds_read_b128 a[96:99], v212 offset:4096\n"
      "s_nop 0\n"
      "v_mfma_f32_16x16x32_bf16 v[68:71], a[148:151], v[188:191], v[68:71]\n"
      "v_exp_f32 v175, v175\n"
      "v_mfma_f32_16x16x32_bf16 v[72:75], a[152:155], v[188:191], v[72:75]\n"
      "buffer_load_dwordx4 v237, s[36:39], s101 offen offset:1024 lds\n"
      "s_nop 0\n"
      "v_mfma_f32_16x16x32_bf16 v[72:75], a[184:187], v[176:179], v[72:75]\n"
      "v_exp_f32 v160, v160\n"
      "s_nop 0\n"
      "v_mfma_f32_16x16x32_bf16 v[76:79], a[188:191], v[176:179], v[76:79]\n"
      "ds_read_b128 a[132:135], v217 offset:4096\n"
      "v_cvt_pk_bf16_f32 v183, v174, v175\n"
      "v_mfma_f32_16x16x32_bf16 v[76:79], a[156:159], v[188:191], v[76:79]\n"
      "s_waitcnt lgkmcnt(11)\n"
      "v_exp_f32 v161, v161\n"
      "s_nop 0\n"
      "v_mfma_f32_16x16x32_bf16 v[168:171], a[80:83], a[64:67], v[208:211]\n"
      "s_waitcnt lgkmcnt(6)\n"
      "ds_read_b128 a[124:127], v219 offset:0\n"
      "v_cvt_pk_bf16_f32 v184, v160, v161\n"
      "s_nop 0\n"
      "v_mfma_f32_16x16x32_bf16 v[168:171], a[84:87], a[68:71], v[168:171]\n"
      "v_exp_f32 v162, v162\n"
      "v_mfma_f32_16x16x32_bf16 v[168:171], a[88:91], a[72:75], v[168:171]\n"
      "ds_read_b128 a[128:131], v216 offset:4096\n"
      "s_nop 0\n"
      "v_mfma_f32_16x16x32_bf16 v[168:171], a[92:95], a[76:79], v[168:171]\n"
      "v_exp_f32 v163, v163\n"
      "s_nop 0\n"
      "v_mfma_f32_16x16x32_bf16 v[172:175], a[108:111], a[76:79], v[208:211]\n"
      "ds_read_b128 a[120:123], v218 offset:0\n"
      "v_cvt_pk_bf16_f32 v185, v162, v163\n"
      "v_mfma_f32_16x16x32_bf16 v[172:175], a[104:107], a[72:75], v[172:175]\n"
      "s_waitcnt lgkmcnt(6)\n"
      "v_exp_f32 v164, v164\n"
      "s_nop 0\n"
      "v_mfma_f32_16x16x32_bf16 v[172:175], a[100:103], a[68:71], v[172:175]\n"
      "s_waitcnt lgkmcnt(4)\n"
      "ds_read_b128 a[112:115], v216 offset:0\n"
      "s_nop 0\n"
      "v_mfma_f32_16x16x32_bf16 v[172:175], a[96:99], a[64:67], v[172:175]\n"
      "v_exp_f32 v165, v165\n"
      "v_mfma_f32_16x16x32_bf16 v[80:83], a[160:163], v[188:191], v[80:83]\n"
      "v_cvt_pk_bf16_f32 v186, v164, v165\n"
      "s_nop 0\n"
      "v_mfma_f32_16x16x32_bf16 v[80:83], a[192:195], v[176:179], v[80:83]\n"
      "v_exp_f32 v166, v166\n"
      "v_mfma_f32_16x16x32_bf16 v[84:87], a[196:199], v[176:179], v[84:87]\n"
      "s_cmp_gt_u32 s69, 5\n"
      "s_cbranch_scc1 L_ev3%=\n"
      "s_nop 0\n"
      "v_mfma_f32_16x16x32_bf16 v[84:87], a[164:167], v[188:191], v[84:87]\n"
      "v_exp_f32 v167, v167\n"
      "s_nop 0\n"
      "v_mfma_f32_16x16x32_bf16 v[88:91], a[168:171], v[188:191], v[88:91]\n"
      "v_cvt_pk_bf16_f32 v187, v166, v167\n"
      "v_mfma_f32_16x16x32_bf16 v[88:91], a[200:203], v[176:179], v[88:91]\n"
      "v_exp_f32 v168, v168\n"
      "s_nop 0\n"
      "v_mfma_f32_16x16x32_bf16 v[92:95], a[204:207], v[176:179], v[92:95]\n"
      "buffer_load_dwordx4 v238, s[36:39], s101 offen offset:2048 lds\n"
      "v_mfma_f32_16x16x32_bf16 v[92:95], a[172:175], v[188:191], v[92:95]\n"
      "v_exp_f32 v169, v169\n"
      "s_nop 0\n"
      "v_mfma_f32_16x16x32_bf16 v[160:163], a[80:83], a[0:3], v[192:195]\n"
      "v_add_f32 v246, v246, v169\n"
      "v_cvt_pk_bf16_f32 v188, v168, v169\n"
      "s_nop 0\n"
      "v_mfma_f32_16x16x32_bf16 v[160:163], a[84:87], a[4:7], v[160:163]\n"
      "v_exp_f32 v170, v170\n"
      "v_mfma_f32_16x16x32_bf16 v[160:163], a[88:91], a[8:11], v[160:163]\n"
      "v_add_f32 v247, v247, v168\n"
      "v_add_f32 v246, v246, v170\n"
      "s_nop 0\n"
      "v_mfma_f32_16x16x32_bf16 v[160:163], a[92:95], a[12:15], v[160:163]\n"
      "v_exp_f32 v171, v171\n"
      "v_mfma_f32_16x16x32_bf16 v[164:167], a[108:111], a[12:15], v[192:195]\n"
      "v_add_f32 v247, v247, v171\n"
      "v_cvt_pk_bf16_f32 v189, v170, v171\n"
      "s_nop 0\n"
      "v_mfma_f32_16x16x32_bf16 v[164:167], a[104:107], a[8:11], v[164:167]\n"
      "v_exp_f32 v172, v172\n"
      "s_nop 0\n"
      "v_mfma_f32_16x16x32_bf16 v[164:167], a[100:103], a[4:7], v[164:167]\n"
      "v_add_f32 v246, v246, v172\n"
      "v_mfma_f32_16x16x32_bf16 v[164:167], a[96:99], a[0:3], v[164:167]\n"
      "v_exp_f32 v173, v173\n"
      "s_nop 0\n"
      "v_mfma_f32_16x16x32_bf16 v[96:99], a[144:147], v[180:183], v[96:99]\n"
      "v_add_f32 v247, v247, v173\n"
      "v_cvt_pk_bf16_f32 v190, v172, v173\n"
      "v_mfma_f32_16x16x32_bf16 v[96:99], a[176:179], v[184:187], v[96:99]\n"
      "v_exp_f32 v174, v174\n"
      "s_nop 0\n"
      "v_mfma_f32_16x16x32_bf16 v[100:103], a[180:183], v[184:187], v[100:103]\n"
      "ds_read_b64_tr_b16 a[146:147], v220 offset:4096\n"
      "ds_read_b64_tr_b16 a[182:183], v229 offset:4096\n"
      "v_mfma_f32_16x16x32_bf16 v[100:103], a[148:151], v[180:183], v[100:103]\n"
      "v_exp_f32 v175, v175\n"
      "s_nop 0\n"
      "v_mfma_f32_16x16x32_bf16 v[104:107], a[152:155], v[180:183], v[104:107]\n"
      "ds_read_b64_tr_b16 a[150:151], v221 offset:4096\n"
      "v_add_f32 v246, v246, v175\n"
      "v_mfma_f32_16x16x32_bf16 v[104:107], a[184:187], v[184:187], v[104:107]\n"
      "v_exp_f32 v160, v160\n"
      "s_nop 0\n"
      "v_mfma_f32_16x16x32_bf16 v[108:111], a[188:191], v[184:187], v[108:111]\n"
      "ds_read_b64_tr_b16 a[144:145], v220 offset:0\n"
      "v_cvt_pk_bf16_f32 v191, v174, v175\n"
      "v_mfma_f32_16x16x32_bf16 v[108:111], a[156:159], v[180:183], v[108:111]\n"
      "s_waitcnt lgkmcnt(4)\n"
      "v_exp_f32 v161, v161\n"
      "s_nop 0\n"
      "v_mfma_f32_16x16x32_bf16 v[168:171], a[112:115], a[0:3], v[192:195]\n"
      "buffer_load_dwordx4 v239, s[36:39], s101 offen offset:3072 lds\n"
      "v_mfma_f32_16x16x32_bf16 v[168:171], a[116:119], a[4:7], v[168:171]\n"
      "v_exp_f32 v162, v162\n"
      "s_nop 0\n"
      "v_mfma_f32_16x16x32_bf16 v[168:171], a[120:123], a[8:11], v[168:171]\n"
      "v_add_f32 v247, v247, v174\n"
      "v_cvt_pk_bf16_f32 v176, v160, v161\n"
      "v_mfma_f32_16x16x32_bf16 v[168:171], a[124:127], a[12:15], v[168:171]\n"
      "v_exp_f32 v163, v163\n"
      "s_nop 0\n"
      "v_mfma_f32_16x16x32_bf16 v[172:175], a[140:143], a[12:15], v[192:195]\n"
      "ds_read_b64_tr_b16 a[148:149], v221 offset:0\n"
      "ds_read_b64_tr_b16 a[154:155], v222 offset:4096\n"
      "v_mfma_f32_16x16x32_bf16 v[172:175], a[136:139], a[8:11], v[172:175]\n"
      "v_exp_f32 v164, v164\n"
      "s_nop 0\n"
      "v_mfma_f32_16x16x32_bf16 v[172:175], a[132:135], a[4:7], v[172:175]\n"
      "ds_read_b64_tr_b16 a[156:157], v223 offset:0\n"
      "ds_read_b64_tr_b16 a[158:159], v223 offset:4096\n"
      "v_mfma_f32_16x16x32_bf16 v[172:175], a[128:131], a[0:3], v[172:175]\n"
      "v_exp_f32 v165, v165\n"
      "s_nop 0\n"
      "v_mfma_f32_16x16x32_bf16 v[112:115], a[160:163], v[180:183], v[112:115]\n"
      "v_cvt_pk_bf16_f32 v177, v162, v163\n"
      "v_cvt_pk_bf16_f32 v178, v164, v165\n"
      "v_mfma_f32_16x16x32_bf16 v[112:115], a[192:195], v[184:187], v[112:115]\n"
      "v_exp_f32 v166, v166\n"
      "s_nop 0\n"
      "v_mfma_f32_16x16x32_bf16 v[116:119], a[196:199], v[184:187], v[116:119]\n"
      "ds_read_b64_tr_b16 a[152:153], v222 offset:0\n"
      "ds_read_b64_tr_b16 a[160:161], v224 offset:0\n"
      "v_mfma_f32_16x16x32_bf16 v[116:119], a[164:167], v[180:183], v[116:119]\n"
      "v_exp_f32 v167, v167\n"
      "s_nop 0\n"
      "v_mfma_f32_16x16x32_bf16 v[120:123], a[168:171], v[180:183], v[120:123]\n"
      "ds_read_b64_tr_b16 a[162:163], v224 offset:4096\n"
      "ds_read_b64_tr_b16 a[170:171], v226 offset:4096\n"
      "v_mfma_f32_16x16x32_bf16 v[120:123], a[200:203], v[184:187], v[120:123]\n"
      "ds_read_b64_tr_b16 a[196:197], v233 offset:0\n"
      "ds_read_b64_tr_b16 a[198:199], v233 offset:4096\n"
      "s_nop 0\n"
      "v_mfma_f32_16x16x32_bf16 v[124:127], a[204:207], v[184:187], v[124:127]\n"
      "ds_read_b64_tr_b16 a[204:205], v235 offset:0\n"
      "ds_read_b64_tr_b16 a[206:207], v235 offset:4096\n"
      "v_mfma_f32_16x16x32_bf16 v[124:127], a[172:175], v[180:183], v[124:127]\n"
      "v_exp_f32 v168, v168\n"
      "s_nop 0\n"
      "v_mfma_f32_16x16x32_bf16 a[220:223], a[228:231], v[180:183], a[220:223]\n"
      "ds_read_b64_tr_b16 a[166:167], v225 offset:4096\n"
      "ds_read_b64_tr_b16 a[168:169], v226 offset:0\n"
      "v_mfma_f32_16x16x32_bf16 a[220:223], a[228:231], v[184:187], a[220:223]\n"
      "s_mov_b32 m0, s60\n"
      "v_exp_f32 v169, v169\n"
      "s_nop 0\n"
      "v_mfma_f32_16x16x32_bf16 v[160:163], a[80:83], a[16:19], v[196:199]\n"
      "buffer_load_dwordx4 v240, s[40:43], s101 offen offset:0 lds\n"
      "v_mfma_f32_16x16x32_bf16 v[160:163], a[84:87], a[20:23], v[160:163]\n"
      "v_exp_f32 v170, v170\n"
      "v_mfma_f32_16x16x32_bf16 v[160:163], a[88:91], a[24:27], v[160:163]\n"
      "ds_read_b64_tr_b16 a[164:165], v225 offset:0\n"
      "v_cvt_pk_bf16_f32 v179, v166, v167\n"
      "s_nop 0\n"
      "v_mfma_f32_16x16x32_bf16 v[160:163], a[92:95], a[28:31], v[160:163]\n"
      "v_exp_f32 v171, v171\n"
      "v_mfma_f32_16x16x32_bf16 v[164:167], a[108:111], a[28:31], v[196:199]\n"
      "ds_read_b64_tr_b16 a[172:173], v227 offset:0\n"
      "ds_read_b64_tr_b16 a[174:175], v227 offset:4096\n"
      "s_nop 0\n"
      "v_mfma_f32_16x16x32_bf16 v[164:167], a[104:107], a[24:27], v[164:167]\n"
      "v_exp_f32 v172, v172\n"
      "v_mfma_f32_16x16x32_bf16 v[164:167], a[100:103], a[20:23], v[164:167]\n"
      "ds_read_b64_tr_b16 a[200:201], v234 offset:0\n"
      "ds_read_b64_tr_b16 a[202:203], v234 offset:4096\n"
      "s_nop 0\n"
      "v_mfma_f32_16x16x32_bf16 v[164:167], a[96:99], a[16:19], v[164:167]\n"
      "s_waitcnt lgkmcnt(15)\n"
      "v_exp_f32 v173, v173\n"
      "v_mfma_f32_16x16x32_bf16 v[128:131], a[144:147], v[188:191], v[128:131]\n"
      "s_waitcnt lgkmcnt(15)\n"
      "ds_read_b64_tr_b16 a[186:187], v230 offset:4096\n"
      "ds_read_b64_tr_b16 a[194:195], v232 offset:4096\n"
      "v_mfma_f32_16x16x32_bf16 v[132:135], a[148:151], v[188:191], v[132:135]\n"
      "s_waitcnt lgkmcnt(15)\n"
      "v_exp_f32 v174, v174\n"
      "s_nop 0\n"
      "v_mfma_f32_16x16x32_bf16 v[136:139], a[152:155], v[188:191], v[136:139]\n"
      "ds_read_b64_tr_b16 a[176:177], v228 offset:0\n"
      "ds_read_b64_tr_b16 a[184:185], v230 offset:0\n"
      "v_mfma_f32_16x16x32_bf16 v[140:143], a[156:159], v[188:191], v[140:143]\n"
      "s_waitcnt lgkmcnt(15)\n"
      "v_exp_f32 v175, v175\n"
      "s_nop 0\n"
      "v_mfma_f32_16x16x32_bf16 v[144:147], a[160:163], v[188:191], v[144:147]\n"
      "s_waitcnt lgkmcnt(8)\n"
      "ds_read_b64_tr_b16 a[178:179], v228 offset:4096\n"
      "ds_read_b64_tr_b16 a[180:181], v229 offset:0\n"
      "v_mfma_f32_16x16x32_bf16 v[148:151], a[164:167], v[188:191], v[148:151]\n"
      "v_exp_f32 v160, v160\n"
      "v_mfma_f32_16x16x32_bf16 v[152:155], a[168:171], v[188:191], v[152:155]\n"
      "s_waitcnt lgkmcnt(8)\n"
      "v_cvt_pk_bf16_f32 v180, v168, v169\n"
      "v_cvt_pk_bf16_f32 v181, v170, v171\n"
      "s_nop 0\n"
      "v_mfma_f32_16x16x32_bf16 v[156:159], a[172:175], v[188:191], v[156:159]\n"
      "v_exp_f32 v161, v161\n"
      "v_mfma_f32_16x16x32_bf16 v[168:171], a[112:115], a[16:19], v[196:199]\n"
      "v_cvt_pk_bf16_f32 v182, v172, v173\n"
      "v_cvt_pk_bf16_f32 v183, v174, v175\n"
      "v_mfma_f32_16x16x32_bf16 v[168:171], a[116:119], a[20:23], v[168:171]\n"
      "v_exp_f32 v162, v162\n"
      "s_nop 0\n"
      "v_mfma_f32_16x16x32_bf16 v[168:171], a[120:123], a[24:27], v[168:171]\n"
      "buffer_load_dwordx4 v241, s[40:43], s101 offen offset:1024 nt lds\n"
      "v_mfma_f32_16x16x32_bf16 v[168:171], a[124:127], a[28:31], v[168:171]\n"
      "v_exp_f32 v163, v163\n"
      "v_mfma_f32_16x16x32_bf16 v[172:175], a[140:143], a[28:31], v[196:199]\n"
      "ds_read_b64_tr_b16 a[188:189], v231 offset:0\n"
      "ds_read_b64_tr_b16 a[190:191], v231 offset:4096\n"
      "s_nop 0\n"
      "v_mfma_f32_16x16x32_bf16 v[172:175], a[136:139], a[24:27], v[172:175]\n"
      "v_exp_f32 v164, v164\n"
      "v_mfma_f32_16x16x32_bf16 v[172:175], a[132:135], a[20:23], v[172:175]\n"
      "ds_read_b64_tr_b16 a[192:193], v232 offset:0\n"
      "v_add_f32 v242, v242, v160\n"
      "v_mfma_f32_16x16x32_bf16 v[172:175], a[128:131], a[16:19], v[172:175]\n"
      "v_exp_f32 v165, v165\n"
      "s_nop 0\n"
      "v_mfma_f32_16x16x32_bf16 v[0:3], a[144:147], v[176:179], v[0:3]\n"
      "s_waitcnt lgkmcnt(4)\n"
      "v_add_f32 v243, v243, v161\n"
      "v_cvt_pk_bf16_f32 v184, v160, v161\n"
      "v_mfma_f32_16x16x32_bf16 v[0:3], a[176:179], v[180:183], v[0:3]\n"
      "s_waitcnt lgkmcnt(3)\n"
      "v_exp_f32 v166, v166\n"
      "v_mfma_f32_16x16x32_bf16 v[4:7], a[180:183], v[180:183], v[4:7]\n"
      "v_add_f32 v242, v242, v163\n"
      "v_cvt_pk_bf16_f32 v185, v162, v163\n"
      "s_nop 0\n"
      "v_mfma_f32_16x16x32_bf16 v[4:7], a[148:151], v[176:179], v[4:7]\n"
      "v_exp_f32 v167, v167\n"
      "v_mfma_f32_16x16x32_bf16 v[8:11], a[152:155], v[176:179], v[8:11]\n"
      "v_add_f32 v243, v243, v162\n"
      "v_add_f32 v242, v242, v164\n"
      "v_mfma_f32_16x16x32_bf16 v[8:11], a[184:187], v[180:183], v[8:11]\n"
      "s_waitcnt lgkmcnt(1)\n"
      "v_exp_f32 v168, v168\n"
      "s_nop 0\n"
      "v_mfma_f32_16x16x32_bf16 v[12:15], a[188:191], v[180:183], v[12:15]\n"
      "v_add_f32 v243, v243, v165\n"
      "v_cvt_pk_bf16_f32 v186, v164, v165\n"
      "v_mfma_f32_16x16x32_bf16 v[12:15], a[156:159], v[176:179], v[12:15]\n"
      "v_exp_f32 v169, v169\n"
      "v_mfma_f32_16x16x32_bf16 v[160:163], a[80:83], a[32:35], v[200:203]\n"
      "v_add_f32 v242, v242, v166\n"
      "v_cvt_pk_bf16_f32 v187, v166, v167\n"
      "s_nop 0\n"
      "v_mfma_f32_16x16x32_bf16 v[160:163], a[84:87], a[36:39], v[160:163]\n"
      "v_exp_f32 v170, v170\n"
      "v_mfma_f32_16x16x32_bf16 v[160:163], a[88:91], a[40:43], v[160:163]\n"
      "v_add_f32 v243, v243, v167\n"
      "v_add_f32 v242, v242, v168\n"
      "v_mfma_f32_16x16x32_bf16 v[160:163], a[92:95], a[44:47], v[160:163]\n"
      "v_exp_f32 v171, v171\n"
      "v_mfma_f32_16x16x32_bf16 v[164:167], a[108:111], a[44:47], v[200:203]\n"
      "buffer_load_dwordx4 v240, s[40:43], s101 offen offset:2048 lds\n"
      "s_nop 0\n"
      "v_mfma_f32_16x16x32_bf16 v[164:167], a[104:107], a[40:43], v[164:167]\n"
      "v_exp_f32 v172, v172\n"
      "v_mfma_f32_16x16x32_bf16 v[164:167], a[100:103], a[36:39], v[164:167]\n"
      "v_add_f32 v243, v243, v169\n"
      "v_cvt_pk_bf16_f32 v188, v168, v169\n"
      "v_mfma_f32_16x16x32_bf16 v[164:167], a[96:99], a[32:35], v[164:167]\n"
      "v_exp_f32 v173, v173\n"
      "s_nop 0\n"
      "v_mfma_f32_16x16x32_bf16 v[16:19], a[160:163], v[176:179], v[16:19]\n"
      "s_waitcnt lgkmcnt(0)\n"
      "v_add_f32 v242, v242, v170\n"
      "v_add_f32 v243, v243, v171\n"
      "v_mfma_f32_16x16x32_bf16 v[16:19], a[192:195], v[180:183], v[16:19]\n"
      "v_exp_f32 v174, v174\n"
      "v_mfma_f32_16x16x32_bf16 v[20:23], a[196:199], v[180:183], v[20:23]\n"
      "v_cvt_pk_bf16_f32 v189, v170, v171\n"
      "v_cvt_pk_bf16_f32 v190, v172, v173\n"
      "v_mfma_f32_16x16x32_bf16 v[20:23], a[164:167], v[176:179], v[20:23]\n"
      "v_exp_f32 v175, v175\n"
      "s_nop 0\n"
      "v_mfma_f32_16x16x32_bf16 v[24:27], a[168:171], v[176:179], v[24:27]\n"
      "v_add_f32 v242, v242, v172\n"
      "v_add_f32 v243, v243, v173\n"
      "v_mfma_f32_16x16x32_bf16 v[24:27], a[200:203], v[180:183], v[24:27]\n"
      "v_add_f32 v242, v242, v174\n"
      "v_cvt_pk_bf16_f32 v191, v174, v175\n"
      "v_mfma_f32_16x16x32_bf16 v[28:31], a[204:207], v[180:183], v[28:31]\n"
      "v_add_f32 v243, v243, v175\n"
      "v_mfma_f32_16x16x32_bf16 v[28:31], a[172:175], v[176:179], v[28:31]\n"
      "v_exp_f32 v160, v160\n"
      "v_mfma_f32_16x16x32_bf16 a[208:211], a[228:231], v[176:179], a[208:211]\n"
      "v_add_f32 v244, v244, v160\n"
      "s_nop 0\n"
      "v_mfma_f32_16x16x32_bf16 a[208:211], a[228:231], v[180:183], a[208:211]\n"
      "v_exp_f32 v161, v161\n"
      "v_mfma_f32_16x16x32_bf16 v[168:171], a[112:115], a[32:35], v[200:203]\n"
      "v_add_f32 v245, v245, v161\n"
      "v_cvt_pk_bf16_f32 v176, v160, v161\n"
      "v_mfma_f32_16x16x32_bf16 v[168:171], a[116:119], a[36:39], v[168:171]\n"
      "v_exp_f32 v162, v162\n"
      "v_mfma_f32_16x16x32_bf16 v[168:171], a[120:123], a[40:43], v[168:171]\n"
      "v_add_f32 v244, v244, v162\n"
      "s_nop 0\n"
      "v_mfma_f32_16x16x32_bf16 v[168:171], a[124:127], a[44:47], v[168:171]\n"
      "v_exp_f32 v163, v163\n"
      "v_mfma_f32_16x16x32_bf16 v[172:175], a[140:143], a[44:47], v[200:203]\n"
      "buffer_load_dwordx4 v241, s[40:43], s101 offen offset:3072 nt lds\n"
      "v_mfma_f32_16x16x32_bf16 v[172:175], a[136:139], a[40:43], v[172:175]\n"
      "s_add_u32 s52, s52, 0x100\n"
      "v_exp_f32 v164, v164\n"
      "v_mfma_f32_16x16x32_bf16 v[172:175], a[132:135], a[36:39], v[172:175]\n"
      "s_or_b32 s52, s52, 0x1bc00\n"
      "v_add_f32 v245, v245, v163\n"
      "v_cvt_pk_bf16_f32 v177, v162, v163\n"
      "v_mfma_f32_16x16x32_bf16 v[172:175], a[128:131], a[32:35], v[172:175]\n"
      "v_exp_f32 v165, v165\n"
      "v_mfma_f32_16x16x32_bf16 v[32:35], a[144:147], v[184:187], v[32:35]\n"
      "s_xor_b32 s101, s52, s100\n"
      "v_add_f32 v244, v244, v164\n"
      "v_cvt_pk_bf16_f32 v178, v164, v165\n"
      "s_nop 0\n"
      "v_mfma_f32_16x16x32_bf16 v[32:35], a[176:179], v[188:191], v[32:35]\n"
      "v_exp_f32 v166, v166\n"
      "v_mfma_f32_16x16x32_bf16 v[36:39], a[180:183], v[188:191], v[36:39]\n"
      "v_add_f32 v245, v245, v165\n"
      "v_add_f32 v244, v244, v166\n"
      "v_mfma_f32_16x16x32_bf16 v[36:39], a[148:151], v[184:187], v[36:39]\n"
      "v_exp_f32 v167, v167\n"
      "v_mfma_f32_16x16x32_bf16 v[40:43], a[152:155], v[184:187], v[40:43]\n"
      "v_add_f32 v245, v245, v167\n"
      "v_cvt_pk_bf16_f32 v179, v166, v167\n"
      "v_mfma_f32_16x16x32_bf16 v[40:43], a[184:187], v[188:191], v[40:43]\n"
      "v_exp_f32 v168, v168\n"
      "v_mfma_f32_16x16x32_bf16 v[44:47], a[188:191], v[188:191], v[44:47]\n"
      "s_getreg_b32 s69, hwreg(7, 0, 4)\n"
      "v_add_f32 v244, v244, v168\n"
      "s_nop 0\n"
      "v_mfma_f32_16x16x32_bf16 v[44:47], a[156:159], v[184:187], v[44:47]\n"
      "s_waitcnt vmcnt(8) lgkmcnt(0)\n"
      "s_barrier\n"
      "v_exp_f32 v169, v169\n"
      "v_mfma_f32_16x16x32_bf16 v[160:163], a[80:83], a[48:51], v[204:207]\n"
      "ds_read_b128 a[80:83], v212 offset:16384\n"
      "v_add_f32 v245, v245, v169\n"
      "v_mfma_f32_16x16x32_bf16 v[160:163], a[84:87], a[52:55], v[160:163]\n"
      "v_exp_f32 v170, v170\n"
      "v_mfma_f32_16x16x32_bf16 v[160:163], a[88:91], a[56:59], v[160:163]\n"
      "v_add_f32 v244, v244, v170\n"
      "v_cvt_pk_bf16_f32 v180, v168, v169\n"
      "v_mfma_f32_16x16x32_bf16 v[160:163], a[92:95], a[60:63], v[160:163]\n"
      "v_exp_f32 v171, v171\n"
      "v_mfma_f32_16x16x32_bf16 v[164:167], a[108:111], a[60:63], v[204:207]\n"
      "ds_read_b128 a[108:111], v215 offset:20480\n"
      "v_add_f32 v245, v245, v171\n"
      "v_mfma_f32_16x16x32_bf16 v[164:167], a[104:107], a[56:59], v[164:167]\n"
      "v_exp_f32 v172, v172\n"
      "s_nop 0\n"
      "v_mfma_f32_16x16x32_bf16 v[164:167], a[100:103], a[52:55], v[164:167]\n"
      "ds_read_b128 a[88:91], v214 offset:16384\n"
      "v_cvt_pk_bf16_f32 v181, v170, v171\n"
      "v_mfma_f32_16x16x32_bf16 v[164:167], a[96:99], a[48:51], v[164:167]\n"
      "v_exp_f32 v173, v173\n"
      "v_mfma_f32_16x16x32_bf16 v[48:51], a[160:163], v[184:187], v[48:51]\n"
      "ds_read_b128 a[104:107], v214 offset:20480\n"
      "v_add_f32 v244, v244, v173\n"
      "v_mfma_f32_16x16x32_bf16 v[48:51], a[192:195], v[188:191], v[48:51]\n"
      "s_mov_b32 m0, s57\n"
      "v_exp_f32 v174, v174\n"
      "v_mfma_f32_16x16x32_bf16 v[52:55], a[196:199], v[188:191], v[52:55]\n"
      "buffer_load_dwordx4 v236, s[36:39], s101 offen offset:0 lds\n"
      "v_mfma_f32_16x16x32_bf16 v[52:55], a[164:167], v[184:187], v[52:55]\n"
      "v_exp_f32 v175, v175\n"
      "v_mfma_f32_16x16x32_bf16 v[56:59], a[168:171], v[184:187], v[56:59]\n"
      "ds_read_b128 a[92:95], v215 offset:16384\n"
      "v_add_f32 v245, v245, v172\n"
      "v_mfma_f32_16x16x32_bf16 v[56:59], a[200:203], v[188:191], v[56:59]\n"
      "v_exp_f32 v160, v160\n"
      "v_mfma_f32_16x16x32_bf16 v[60:63], a[204:207], v[188:191], v[60:63]\n"
      "ds_read_b128 a[84:87], v213 offset:16384\n"
      "v_cvt_pk_bf16_f32 v182, v172, v173\n"
      "v_mfma_f32_16x16x32_bf16 v[60:63], a[172:175], v[184:187], v[60:63]\n"
      "v_exp_f32 v161, v161\n"
      "v_mfma_f32_16x16x32_bf16 v[168:171], a[112:115], a[48:51], v[204:207]\n"
      "v_add_f32 v244, v244, v175\n"
      "v_cvt_pk_bf16_f32 v183, v174, v175\n"
      "s_nop 0\n"
      "v_mfma_f32_16x16x32_bf16 v[168:171], a[116:119], a[52:55], v[168:171]\n"
      "v_exp_f32 v162, v162\n"
      "v_mfma_f32_16x16x32_bf16 v[168:171], a[120:123], a[56:59], v[168:171]\n"
      "ds_read_b128 a[116:119], v217 offset:16384\n"
      "v_add_f32 v245, v245, v174\n"
      "v_mfma_f32_16x16x32_bf16 v[168:171], a[124:127], a[60:63], v[168:171]\n"
      "v_exp_f32 v163, v163\n"
      "v_mfma_f32_16x16x32_bf16 v[172:175], a[140:143], a[60:63], v[204:207]\n"
      "ds_read_b128 a[140:143], v219 offset:20480\n"
      "v_cvt_pk_bf16_f32 v185, v162, v163\n"
      "v_mfma_f32_16x16x32_bf16 v[172:175], a[136:139], a[56:59], v[172:175]\n"
      "v_exp_f32 v164, v164\n"
      "v_mfma_f32_16x16x32_bf16 v[172:175], a[132:135], a[52:55], v[172:175]\n"
      "ds_read_b128 a[100:103], v213 offset:20480\n"
      "v_cvt_pk_bf16_f32 v184, v160, v161\n"
      "v_mfma_f32_16x16x32_bf16 v[172:175], a[128:131], a[48:51], v[172:175]\n"
      "v_exp_f32 v165, v165\n"
      "v_mfma_f32_16x16x32_bf16 v[64:67], a[144:147], v[176:179], v[64:67]\n"
      "ds_read_b128 a[136:139], v218 offset:20480\n"
      "v_cvt_pk_bf16_f32 v186, v164, v165\n"
      "v_mfma_f32_16x16x32_bf16 v[64:67], a[176:179], v[180:183], v[64:67]\n"
      "v_exp_f32 v166, v166\n"
      "v_mfma_f32_16x16x32_bf16 v[68:71], a[180:183], v[180:183], v[68:71]\n"
      "ds_read_b128 a[96:99], v212 offset:20480\n"
      "v_mfma_f32_16x16x32_bf16 v[68:71], a[148:151], v[176:179], v[68:71]\n"
      "v_exp_f32 v167, v167\n"
      "v_mfma_f32_16x16x32_bf16 v[72:75], a[152:155], v[176:179], v[72:75]\n"
      "buffer_load_dwordx4 v237, s[36:39], s101 offen offset:1024 lds\n"
      "v_mfma_f32_16x16x32_bf16 v[72:75], a[184:187], v[180:183], v[72:75]\n"
      "v_exp_f32 v168, v168\n"
      "v_mfma_f32_16x16x32_bf16 v[76:79], a[188:191], v[180:183], v[76:79]\n"
      "ds_read_b128 a[132:135], v217 offset:20480\n"
      "v_cvt_pk_bf16_f32 v187, v166, v167\n"
      "v_mfma_f32_16x16x32_bf16 v[76:79], a[156:159], v[176:179], v[76:79]\n"
      "s_waitcnt lgkmcnt(11)\n"
      "v_exp_f32 v169, v169\n"
      "s_branch L_ent0%=\n"
      ".p2align 6\n"
      "L_eloop%=:\n"
      "v_mfma_f32_16x16x32_bf16 v[168:171], a[80:83], a[64:67], v[208:211]\n"
      "s_waitcnt lgkmcnt(6)\n"
      "ds_read_b128 a[124:127], v219 offset:0\n"
      "v_cvt_pk_bf16_f32 v184, v160, v161\n"
      "v_mfma_f32_16x16x32_bf16 v[168:171], a[84:87], a[68:71], v[168:171]\n"
      "v_exp_f32 v162, v162\n"
      "v_mfma_f32_16x16x32_bf16 v[168:171], a[88:91], a[72:75], v[168:171]\n"
      "ds_read_b128 a[128:131], v216 offset:4096\n"
      "v_mfma_f32_16x16x32_bf16 v[168:171], a[92:95], a[76:79], v[168:171]\n"
      "v_exp_f32 v163, v163\n"
      "v_mfma_f32_16x16x32_bf16 v[172:175], a[108:111], a[76:79], v[208:211]\n"
      "ds_read_b128 a[120:123], v218 offset:0\n"
      "v_cvt_pk_bf16_f32 v185, v162, v163\n"
      "v_mfma_f32_16x16x32_bf16 v[172:175], a[104:107], a[72:75], v[172:175]\n"
      "s_waitcnt lgkmcnt(6)\n"
      "v_exp_f32 v164, v164\n"
      "v_mfma_f32_16x16x32_bf16 v[172:175], a[100:103], a[68:71], v[172:175]\n"
      "s_waitcnt lgkmcnt(4)\n"
      "ds_read_b128 a[112:115], v216 offset:0\n"
      "v_mfma_f32_16x16x32_bf16 v[172:175], a[96:99], a[64:67], v[172:175]\n"
      "v_exp_f32 v165, v165\n"
      "v_mfma_f32_16x16x32_bf16 v[80:83], a[160:163], v[188:191], v[80:83]\n"
      "v_cvt_pk_bf16_f32 v186, v164, v165\n"
      "v_mfma_f32_16x16x32_bf16 v[80:83], a[192:195], v[176:179], v[80:83]\n"
      "v_exp_f32 v166, v166\n"
      "v_mfma_f32_16x16x32_bf16 v[84:87], a[196:199], v[176:179], v[84:87]\n"
      "L_ev3%=:\n"
      "s_nop 0\n"
      "v_mfma_f32_16x16x32_bf16 v[84:87], a[164:167], v[188:191], v[84:87]\n"
      "v_exp_f32 v167, v167\n"
      "v_mfma_f32_16x16x32_bf16 v[88:91], a[168:171], v[188:191], v[88:91]\n"
      "v_cvt_pk_bf16_f32 v187, v166, v167\n"
      "v_mfma_f32_16x16x32_bf16 v[88:91], a[200:203], v[176:179], v[88:91]\n"
      "v_exp_f32 v168, v168\n"
      "v_mfma_f32_16x16x32_bf16 v[92:95], a[204:207], v[176:179], v[92:95]\n"
      "buffer_load_dwordx4 v238, s[36:39], s101 offen offset:2048 nt lds\n"
      "v_mfma_f32_16x16x32_bf16 v[92:95], a[172:175], v[188:191], v[92:95]\n"
      "v_exp_f32 v169, v169\n"
      "v_mfma_f32_16x16x32_bf16 v[160:163], a[80:83], a[0:3], v[192:195]\n"
      "v_add_f32 v246, v246, v169\n"
      "v_cvt_pk_bf16_f32 v188, v168, v169\n"
      "v_mfma_f32_16x16x32_bf16 v[160:163], a[84:87], a[4:7], v[160:163]\n"
      "v_exp_f32 v170, v170\n"
      "v_mfma_f32_16x16x32_bf16 v[160:163], a[88:91], a[8:11], v[160:163]\n"
      "v_add_f32 v247, v247, v168\n"
      "v_add_f32 v246, v246, v170\n"
      "v_mfma_f32_16x16x32_bf16 v[160:163], a[92:95], a[12:15], v[160:163]\n"
      "v_exp_f32 v171, v171\n"
      "v_mfma_f32_16x16x32_bf16 v[164:167], a[108:111], a[12:15], v[192:195]\n"
      "v_add_f32 v247, v247, v171\n"
      "v_cvt_pk_bf16_f32 v189, v170, v171\n"
      "v_mfma_f32_16x16x32_bf16 v[164:167], a[104:107], a[8:11], v[164:167]\n"
      "v_exp_f32 v172, v172\n"
      "v_mfma_f32_16x16x32_bf16 v[164:167], a[100:103], a[4:7], v[164:167]\n"
      "v_add_f32 v246, v246, v172\n"
      "v_mfma_f32_16x16x32_bf16 v[164:167], a[96:99], a[0:3], v[164:167]\n"
      "v_exp_f32 v173, v173\n"
      "v_mfma_f32_16x16x32_bf16 v[96:99], a[144:147], v[180:183], v[96:99]\n"
      "v_add_f32 v247, v247, v173\n"
      "v_cvt_pk_bf16_f32 v190, v172, v173\n"
      "v_mfma_f32_16x16x32_bf16 v[96:99], a[176:179], v[184:187], v[96:99]\n"
      "v_exp_f32 v174, v174\n"
      "v_mfma_f32_16x16x32_bf16 v[100:103], a[180:183], v[184:187], v[100:103]\n"
      "ds_read_b64_tr_b16 a[146:147], v220 offset:4096\n"
      "ds_read_b64_tr_b16 a[182:183], v229 offset:4096\n"
      "v_mfma_f32_16x16x32_bf16 v[100:103], a[148:151], v[180:183], v[100:103]\n"
      "v_exp_f32 v175, v175\n"
      "v_mfma_f32_16x16x32_bf16 v[104:107], a[152:155], v[180:183], v[104:107]\n"
      "ds_read_b64_tr_b16 a[150:151], v221 offset:4096\n"
      "v_add_f32 v246, v246, v175\n"
      "v_mfma_f32_16x16x32_bf16 v[104:107], a[184:187], v[184:187], v[104:107]\n"
      "v_exp_f32 v160, v160\n"
      "v_mfma_f32_16x16x32_bf16 v[108:111], a[188:191], v[184:187], v[108:111]\n"
      "ds_read_b64_tr_b16 a[144:145], v220 offset:0\n"
      "v_cvt_pk_bf16_f32 v191, v174, v175\n"
      "v_mfma_f32_16x16x32_bf16 v[108:111], a[156:159], v[180:183], v[108:111]\n"
      "s_waitcnt lgkmcnt(4)\n"
      "v_exp_f32 v161, v161\n"
      "v_mfma_f32_16x16x32_bf16 v[168:171], a[112:115], a[0:3], v[192:195]\n"
      "buffer_load_dwordx4 v239, s[36:39], s101 offen offset:3072 nt lds\n"
      "v_mfma_f32_16x16x32_bf16 v[168:171], a[116:119], a[4:7], v[168:171]\n"
      "v_exp_f32 v162, v162\n"
      "v_mfma_f32_16x16x32_bf16 v[168:171], a[120:123], a[8:11], v[168:171]\n"
      "v_add_f32 v247, v247, v174\n"
      "v_cvt_pk_bf16_f32 v176, v160, v161\n"
      "v_mfma_f32_16x16x32_bf16 v[168:171], a[124:127], a[12:15], v[168:171]\n"
      "v_exp_f32 v163, v163\n"
      "v_mfma_f32_16x16x32_bf16 v[172:175], a[140:143], a[12:15], v[192:195]\n"
      "ds_read_b64_tr_b16 a[148:149], v221 offset:0\n"
      "ds_read_b64_tr_b16 a[154:155], v222 offset:4096\n"
      "v_mfma_f32_16x16x32_bf16 v[172:175], a[136:139], a[8:11], v[172:175]\n"
      "v_exp_f32 v164, v164\n"
      "v_mfma_f32_16x16x32_bf16 v[172:175], a[132:135], a[4:7], v[172:175]\n"
      "ds_read_b64_tr_b16 a[156:157], v223 offset:0\n"
      "ds_read_b64_tr_b16 a[158:159], v223 offset:4096\n"
      "v_mfma_f32_16x16x32_bf16 v[172:175], a[128:131], a[0:3], v[172:175]\n"
      "v_exp_f32 v165, v165\n"
      "v_mfma_f32_16x16x32_bf16 v[112:115], a[160:163], v[180:183], v[112:115]\n"
      "v_cvt_pk_bf16_f32 v177, v162, v163\n"
      "v_cvt_pk_bf16_f32 v178, v164, v165\n"
      "v_mfma_f32_16x16x32_bf16 v[112:115], a[192:195], v[184:187], v[112:115]\n"
      "v_exp_f32 v166, v166\n"
      "v_mfma_f32_16x16x32_bf16 v[116:119], a[196:199], v[184:187], v[116:119]\n"
      "ds_read_b64_tr_b16 a[152:153], v222 offset:0\n"
      "ds_read_b64_tr_b16 a[160:161], v224 offset:0\n"
      "v_mfma_f32_16x16x32_bf16 v[116:119], a[164:167], v[180:183], v[116:119]\n"
      "v_exp_f32 v167, v167\n"
      "v_mfma_f32_16x16x32_bf16 v[120:123], a[168:171], v[180:183], v[120:123]\n"
      "ds_read_b64_tr_b16 a[162:163], v224 offset:4096\n"
      "ds_read_b64_tr_b16 a[170:171], v226 offset:4096\n"
      "v_mfma_f32_16x16x32_bf16 v[120:123], a[200:203], v[184:187], v[120:123]\n"
      "ds_read_b64_tr_b16 a[196:197], v233 offset:0\n"
      "ds_read_b64_tr_b16 a[198:199], v233 offset:4096\n"
      "v_mfma_f32_16x16x32_bf16 v[124:127], a[204:207], v[184:187], v[124:127]\n"
      "ds_read_b64_tr_b16 a[204:205], v235 offset:0\n"
      "ds_read_b64_tr_b16 a[206:207], v235 offset:4096\n"
      "v_mfma_f32_16x16x32_bf16 v[124:127], a[172:175], v[180:183], v[124:127]\n"
      "v_exp_f32 v168, v168\n"
      "v_mfma_f32_16x16x32_bf16 a[220:223], a[228:231], v[180:183], a[220:223]\n"
      "ds_read_b64_tr_b16 a[166:167], v225 offset:4096\n"
      "ds_read_b64_tr_b16 a[168:169], v226 offset:0\n"
      "v_mfma_f32_16x16x32_bf16 a[220:223], a[228:231], v[184:187], a[220:223]\n"
      "s_mov_b32 m0, s60\n"
      "v_exp_f32 v169, v169\n"
      "v_mfma_f32_16x16x32_bf16 v[160:163], a[80:83], a[16:19], v[196:199]\n"
      "buffer_load_dwordx4 v240, s[40:43], s101 offen offset:0 nt lds\n"
      "v_mfma_f32_16x16x32_bf16 v[160:163], a[84:87], a[20:23], v[160:163]\n"
      "v_exp_f32 v170, v170\n"
      "v_mfma_f32_16x16x32_bf16 v[160:163], a[88:91], a[24:27], v[160:163]\n"
      "ds_read_b64_tr_b16 a[164:165], v225 offset:0\n"
      "v_cvt_pk_bf16_f32 v179, v166, v167\n"
      "v_mfma_f32_16x16x32_bf16 v[160:163], a[92:95], a[28:31], v[160:163]\n"
      "v_exp_f32 v171, v171\n"
      "v_mfma_f32_16x16x32_bf16 v[164:167], a[108:111], a[28:31], v[196:199]\n"
      "ds_read_b64_tr_b16 a[172:173], v227 offset:0\n"
      "ds_read_b64_tr_b16 a[174:175], v227 offset:4096\n"
      "v_mfma_f32_16x16x32_bf16 v[164:167], a[104:107], a[24:27], v[164:167]\n"
      "v_exp_f32 v172, v172\n"
      "v_mfma_f32_16x16x32_bf16 v[164:167], a[100:103], a[20:23], v[164:167]\n"
      "ds_read_b64_tr_b16 a[200:201], v234 offset:0\n"
      "ds_read_b64_tr_b16 a[202:203], v234 offset:4096\n"
      "v_mfma_f32_16x16x32_bf16 v[164:167], a[96:99], a[16:19], v[164:167]\n"
      "s_waitcnt lgkmcnt(15)\n"
      "v_exp_f32 v173, v173\n"
      "v_mfma_f32_16x16x32_bf16 v[128:131], a[144:147], v[188:191], v[128:131]\n"
      "s_waitcnt lgkmcnt(15)\n"
      "ds_read_b64_tr_b16 a[186:187], v230 offset:4096\n"
      "ds_read_b64_tr_b16 a[194:195], v232 offset:4096\n"
      "v_mfma_f32_16x16x32_bf16 v[132:135], a[148:151], v[188:191], v[132:135]\n"
      "s_waitcnt lgkmcnt(15)\n"
      "v_exp_f32 v174, v174\n"
      "v_mfma_f32_16x16x32_bf16 v[136:139], a[152:155], v[188:191], v[136:139]\n"
      "ds_read_b64_tr_b16 a[176:177], v228 offset:0\n"
      "ds_read_b64_tr_b16 a[184:185], v230 offset:0\n"
      "v_mfma_f32_16x16x32_bf16 v[140:143], a[156:159], v[188:191], v[140:143]\n"
      "s_waitcnt lgkmcnt(15)\n"
      "v_exp_f32 v175, v175\n"
      "v_mfma_f32_16x16x32_bf16 v[144:147], a[160:163], v[188:191], v[144:147]\n"
      "s_waitcnt lgkmcnt(8)\n"
      "ds_read_b64_tr_b16 a[178:179], v228 offset:4096\n"
      "ds_read_b64_tr_b16 a[180:181], v229 offset:0\n"
      "v_mfma_f32_16x16x32_bf16 v[148:151], a[164:167], v[188:191], v[148:151]\n"
      "v_exp_f32 v160, v160\n"
      "v_mfma_f32_16x16x32_bf16 v[152:155], a[168:171], v[188:191], v[152:155]\n"
      "s_waitcnt lgkmcnt(8)\n"
      "v_cvt_pk_bf16_f32 v180, v168, v169\n"
      "v_cvt_pk_bf16_f32 v181, v170, v171\n"
      "v_mfma_f32_16x16x32_bf16 v[156:159], a[172:175], v[188:191], v[156:159]\n"
      "v_exp_f32 v161, v161\n"
      "v_mfma_f32_16x16x32_bf16 v[168:171], a[112:115], a[16:19], v[196:199]\n"
      "v_cvt_pk_bf16_f32 v182, v172, v173\n"
      "v_cvt_pk_bf16_f32 v183, v174, v175\n"
      "v_mfma_f32_16x16x32_bf16 v[168:171], a[116:119], a[20:23], v[168:171]\n"
      "v_exp_f32 v162, v162\n"
      "v_mfma_f32_16x16x32_bf16 v[168:171], a[120:123], a[24:27], v[168:171]\n"
      "buffer_load_dwordx4 v241, s[40:43], s101 offen offset:1024 nt lds\n"
      "v_mfma_f32_16x16x32_bf16 v[168:171], a[124:127], a[28:31], v[168:171]\n"
      "v_exp_f32 v163, v163\n"
      "v_mfma_f32_16x16x32_bf16 v[172:175], a[140:143], a[28:31], v[196:199]\n"
      "ds_read_b64_tr_b16 a[188:189], v231 offset:0\n"
      "ds_read_b64_tr_b16 a[190:191], v231 offset:4096\n"
      "v_mfma_f32_16x16x32_bf16 v[172:175], a[136:139], a[24:27], v[172:175]\n"
      "v_exp_f32 v164, v164\n"
      "v_mfma_f32_16x16x32_bf16 v[172:175], a[132:135], a[20:23], v[172:175]\n"
      "ds_read_b64_tr_b16 a[192:193], v232 offset:0\n"
      "v_add_f32 v242, v242, v160\n"
      "v_mfma_f32_16x16x32_bf16 v[172:175], a[128:131], a[16:19], v[172:175]\n"
      "v_exp_f32 v165, v165\n"
      "v_mfma_f32_16x16x32_bf16 v[0:3], a[144:147], v[176:179], v[0:3]\n"
      "s_waitcnt lgkmcnt(4)\n"
      "v_add_f32 v243, v243, v161\n"
      "v_cvt_pk_bf16_f32 v184, v160, v161\n"
      "v_mfma_f32_16x16x32_bf16 v[0:3], a[176:179], v[180:183], v[0:3]\n"
      "s_waitcnt lgkmcnt(3)\n"
      "v_exp_f32 v166, v166\n"
      "v_mfma_f32_16x16x32_bf16 v[4:7], a[180:183], v[180:183], v[4:7]\n"
      "v_add_f32 v242, v242, v163\n"
      "v_cvt_pk_bf16_f32 v185, v162, v163\n"
      "v_mfma_f32_16x16x32_bf16 v[4:7], a[148:151], v[176:179], v[4:7]\n"
      "v_exp_f32 v167, v167\n"
      "v_mfma_f32_16x16x32_bf16 v[8:11], a[152:155], v[176:179], v[8:11]\n"
      "v_add_f32 v243, v243, v162\n"
      "v_add_f32 v242, v242, v164\n"
      "v_mfma_f32_16x16x32_bf16 v[8:11], a[184:187], v[180:183], v[8:11]\n"
      "s_waitcnt lgkmcnt(1)\n"
      "v_exp_f32 v168, v168\n"
      "v_mfma_f32_16x16x32_bf16 v[12:15], a[188:191], v[180:183], v[12:15]\n"
      "v_add_f32 v243, v243, v165\n"
      "v_cvt_pk_bf16_f32 v186, v164, v165\n"
      "v_mfma_f32_16x16x32_bf16 v[12:15], a[156:159], v[176:179], v[12:15]\n"
      "v_exp_f32 v169, v169\n"
      "v_mfma_f32_16x16x32_bf16 v[160:163], a[80:83], a[32:35], v[200:203]\n"
      "v_add_f32 v242, v242, v166\n"
      "v_cvt_pk_bf16_f32 v187, v166, v167\n"
      "v_mfma_f32_16x16x32_bf16 v[160:163], a[84:87], a[36:39], v[160:163]\n"
      "v_exp_f32 v170, v170\n"
      "v_mfma_f32_16x16x32_bf16 v[160:163], a[88:91], a[40:43], v[160:163]\n"
      "v_add_f32 v243, v243, v167\n"
      "v_add_f32 v242, v242, v168\n"
      "v_mfma_f32_16x16x32_bf16 v[160:163], a[92:95], a[44:47], v[160:163]\n"
      "v_exp_f32 v171, v171\n"
      "v_mfma_f32_16x16x32_bf16 v[164:167], a[108:111], a[44:47], v[200:203]\n"
      "buffer_load_dwordx4 v240, s[40:43], s101 offen offset:2048 nt lds\n"
      "v_mfma_f32_16x16x32_bf16 v[164:167], a[104:107], a[40:43], v[164:167]\n"
      "v_exp_f32 v172, v172\n"
      "v_mfma_f32_16x16x32_bf16 v[164:167], a[100:103], a[36:39], v[164:167]\n"
      "v_add_f32 v243, v243, v169\n"
      "v_cvt_pk_bf16_f32 v188, v168, v169\n"
      "v_mfma_f32_16x16x32_bf16 v[164:167], a[96:99], a[32:35], v[164:167]\n"
      "v_exp_f32 v173, v173\n"
      "v_mfma_f32_16x16x32_bf16 v[16:19], a[160:163], v[176:179], v[16:19]\n"
      "s_waitcnt lgkmcnt(0)\n"
      "v_add_f32 v242, v242, v170\n"
      "v_add_f32 v243, v243, v171\n"
      "v_mfma_f32_16x16x32_bf16 v[16:19], a[192:195], v[180:183], v[16:19]\n"
      "v_exp_f32 v174, v174\n"
      "v_mfma_f32_16x16x32_bf16 v[20:23], a[196:199], v[180:183], v[20:23]\n"
      "v_cvt_pk_bf16_f32 v189, v170, v171\n"
      "v_cvt_pk_bf16_f32 v190, v172, v173\n"
      "v_mfma_f32_16x16x32_bf16 v[20:23], a[164:167], v[176:179], v[20:23]\n"
      "v_exp_f32 v175, v175\n"
      "v_mfma_f32_16x16x32_bf16 v[24:27], a[168:171], v[176:179], v[24:27]\n"
      "v_add_f32 v242, v242, v172\n"
      "v_add_f32 v243, v243, v173\n"
      "v_mfma_f32_16x16x32_bf16 v[24:27], a[200:203], v[180:183], v[24:27]\n"
      "v_add_f32 v242, v242, v174\n"
      "v_cvt_pk_bf16_f32 v191, v174, v175\n"
      "v_mfma_f32_16x16x32_bf16 v[28:31], a[204:207], v[180:183], v[28:31]\n"
      "v_add_f32 v243, v243, v175\n"
      "v_mfma_f32_16x16x32_bf16 v[28:31], a[172:175], v[176:179], v[28:31]\n"
      "v_exp_f32 v160, v160\n"
      "v_mfma_f32_16x16x32_bf16 a[208:211], a[228:231], v[176:179], a[208:211]\n"
      "v_add_f32 v244, v244, v160\n"
      "v_mfma_f32_16x16x32_bf16 a[208:211], a[228:231], v[180:183], a[208:211]\n"
      "v_exp_f32 v161, v161\n"
      "v_mfma_f32_16x16x32_bf16 v[168:171], a[112:115], a[32:35], v[200:203]\n"
      "v_add_f32 v245, v245, v161\n"
      "v_cvt_pk_bf16_f32 v176, v160, v161\n"
      "v_mfma_f32_16x16x32_bf16 v[168:171], a[116:119], a[36:39], v[168:171]\n"
      "v_exp_f32 v162, v162\n"
      "v_mfma_f32_16x16x32_bf16 v[168:171], a[120:123], a[40:43], v[168:171]\n"
      "v_add_f32 v244, v244, v162\n"
      "v_mfma_f32_16x16x32_bf16 v[168:171], a[124:127], a[44:47], v[168:171]\n"
      "v_exp_f32 v163, v163\n"
      "v_mfma_f32_16x16x32_bf16 v[172:175], a[140:143], a[44:47], v[200:203]\n"
      "buffer_load_dwordx4 v241, s[40:43], s101 offen offset:3072 nt lds\n"
      "v_mfma_f32_16x16x32_bf16 v[172:175], a[136:139], a[40:43], v[172:175]\n"
      "s_add_u32 s52, s52, 0x100\n"
      "v_exp_f32 v164, v164\n"
      "v_mfma_f32_16x16x32_bf16 v[172:175], a[132:135], a[36:39], v[172:175]\n"
      "s_or_b32 s52, s52, 0x1bc00\n"
      "v_add_f32 v245, v245, v163\n"
      "v_cvt_pk_bf16_f32 v177, v162, v163\n"
      "v_mfma_f32_16x16x32_bf16 v[172:175], a[128:131], a[32:35], v[172:175]\n"
      "v_exp_f32 v165, v165\n"
      "v_mfma_f32_16x16x32_bf16 v[32:35], a[144:147], v[184:187], v[32:35]\n"
      "s_xor_b32 s101, s52, s100\n"
      "v_add_f32 v244, v244, v164\n"
      "v_cvt_pk_bf16_f32 v178, v164, v165\n"
      "v_mfma_f32_16x16x32_bf16 v[32:35], a[176:179], v[188:191], v[32:35]\n"
      "v_exp_f32 v166, v166\n"
      "v_mfma_f32_16x16x32_bf16 v[36:39], a[180:183], v[188:191], v[36:39]\n"
      "v_add_f32 v245, v245, v165\n"
      "v_add_f32 v244, v244, v166\n"
      "v_mfma_f32_16x16x32_bf16 v[36:39], a[148:151], v[184:187], v[36:39]\n"
      "v_exp_f32 v167, v167\n"
      "v_mfma_f32_16x16x32_bf16 v[40:43], a[152:155], v[184:187], v[40:43]\n"
      "v_add_f32 v245, v245, v167\n"
      "v_cvt_pk_bf16_f32 v179, v166, v167\n"
      "v_mfma_f32_16x16x32_bf16 v[40:43], a[184:187], v[188:191], v[40:43]\n"
      "v_exp_f32 v168, v168\n"
      "v_mfma_f32_16x16x32_bf16 v[44:47], a[188:191], v[188:191], v[44:47]\n"
      "v_add_f32 v244, v244, v168\n"
      "v_mfma_f32_16x16x32_bf16 v[44:47], a[156:159], v[184:187], v[44:47]\n"
      "s_waitcnt vmcnt(8) lgkmcnt(0)\n"
      "s_barrier\n"
      "v_exp_f32 v169, v169\n"
      "v_mfma_f32_16x16x32_bf16 v[160:163], a[80:83], a[48:51], v[204:207]\n"
      "ds_read_b128 a[80:83], v212 offset:16384\n"
      "v_add_f32 v245, v245, v169\n"
      "v_mfma_f32_16x16x32_bf16 v[160:163], a[84:87], a[52:55], v[160:163]\n"
      "v_exp_f32 v170, v170\n"
      "v_mfma_f32_16x16x32_bf16 v[160:163], a[88:91], a[56:59], v[160:163]\n"
      "v_add_f32 v244, v244, v170\n"
      "v_cvt_pk_bf16_f32 v180, v168, v169\n"
      "v_mfma_f32_16x16x32_bf16 v[160:163], a[92:95], a[60:63], v[160:163]\n"
      "v_exp_f32 v171, v171\n"
      "v_mfma_f32_16x16x32_bf16 v[164:167], a[108:111], a[60:63], v[204:207]\n"
      "ds_read_b128 a[108:111], v215 offset:20480\n"
      "v_add_f32 v245, v245, v171\n"
      "v_mfma_f32_16x16x32_bf16 v[164:167], a[104:107], a[56:59], v[164:167]\n"
      "v_exp_f32 v172, v172\n"
      "v_mfma_f32_16x16x32_bf16 v[164:167], a[100:103], a[52:55], v[164:167]\n"
      "ds_read_b128 a[88:91], v214 offset:16384\n"
      "v_cvt_pk_bf16_f32 v181, v170, v171\n"
      "v_mfma_f32_16x16x32_bf16 v[164:167], a[96:99], a[48:51], v[164:167]\n"
      "v_exp_f32 v173, v173\n"
      "v_mfma_f32_16x16x32_bf16 v[48:51], a[160:163], v[184:187], v[48:51]\n"
      "ds_read_b128 a[104:107], v214 offset:20480\n"
      "v_add_f32 v244, v244, v173\n"
      "v_mfma_f32_16x16x32_bf16 v[48:51], a[192:195], v[188:191], v[48:51]\n"
      "s_mov_b32 m0, s57\n"
      "v_exp_f32 v174, v174\n"
      "v_mfma_f32_16x16x32_bf16 v[52:55], a[196:199], v[188:191], v[52:55]\n"
      "buffer_load_dwordx4 v236, s[36:39], s101 offen offset:0 nt lds\n"
      "v_mfma_f32_16x16x32_bf16 v[52:55], a[164:167], v[184:187], v[52:55]\n"
      "v_exp_f32 v175, v175\n"
      "v_mfma_f32_16x16x32_bf16 v[56:59], a[168:171], v[184:187], v[56:59]\n"
      "ds_read_b128 a[92:95], v215 offset:16384\n"
      "v_add_f32 v245, v245, v172\n"
      "v_mfma_f32_16x16x32_bf16 v[56:59], a[200:203], v[188:191], v[56:59]\n"
      "v_exp_f32 v160, v160\n"
      "v_mfma_f32_16x16x32_bf16 v[60:63], a[204:207], v[188:191], v[60:63]\n"
      "ds_read_b128 a[84:87], v213 offset:16384\n"
      "v_cvt_pk_bf16_f32 v182, v172, v173\n"
      "v_mfma_f32_16x16x32_bf16 v[60:63], a[172:175], v[184:187], v[60:63]\n"
      "v_exp_f32 v161, v161\n"
      "v_mfma_f32_16x16x32_bf16 v[168:171], a[112:115], a[48:51], v[204:207]\n"
      "v_add_f32 v244, v244, v175\n"
      "v_cvt_pk_bf16_f32 v183, v174, v175\n"
      "v_mfma_f32_16x16x32_bf16 v[168:171], a[116:119], a[52:55], v[168:171]\n"
      "v_exp_f32 v162, v162\n"
      "v_mfma_f32_16x16x32_bf16 v[168:171], a[120:123], a[56:59], v[168:171]\n"
      "ds_read_b128 a[116:119], v217 offset:16384\n"
      "v_add_f32 v245, v245, v174\n"
      "v_mfma_f32_16x16x32_bf16 v[168:171], a[124:127], a[60:63], v[168:171]\n"
      "v_exp_f32 v163, v163\n"
      "v_mfma_f32_16x16x32_bf16 v[172:175], a[140:143], a[60:63], v[204:207]\n"
      "ds_read_b128 a[140:143], v219 offset:20480\n"
      "v_cvt_pk_bf16_f32 v185, v162, v163\n"
      "v_mfma_f32_16x16x32_bf16 v[172:175], a[136:139], a[56:59], v[172:175]\n"
      "v_exp_f32 v164, v164\n"
      "v_mfma_f32_16x16x32_bf16 v[172:175], a[132:135], a[52:55], v[172:175]\n"
      "ds_read_b128 a[100:103], v213 offset:20480\n"
      "v_cvt_pk_bf16_f32 v184, v160, v161\n"
      "v_mfma_f32_16x16x32_bf16 v[172:175], a[128:131], a[48:51], v[172:175]\n"
      "v_exp_f32 v165, v165\n"
      "v_mfma_f32_16x16x32_bf16 v[64:67], a[144:147], v[176:179], v[64:67]\n"
      "ds_read_b128 a[136:139], v218 offset:20480\n"
      "v_cvt_pk_bf16_f32 v186, v164, v165\n"
      "v_mfma_f32_16x16x32_bf16 v[64:67], a[176:179], v[180:183], v[64:67]\n"
      "v_exp_f32 v166, v166\n"
      "v_mfma_f32_16x16x32_bf16 v[68:71], a[180:183], v[180:183], v[68:71]\n"
      "ds_read_b128 a[96:99], v212 offset:20480\n"
      "v_mfma_f32_16x16x32_bf16 v[68:71], a[148:151], v[176:179], v[68:71]\n"
      "v_exp_f32 v167, v167\n"
      "v_mfma_f32_16x16x32_bf16 v[72:75], a[152:155], v[176:179], v[72:75]\n"
      "buffer_load_dwordx4 v237, s[36:39], s101 offen offset:1024 nt lds\n"
      "v_mfma_f32_16x16x32_bf16 v[72:75], a[184:187], v[180:183], v[72:75]\n"
      "v_exp_f32 v168, v168\n"
      "v_mfma_f32_16x16x32_bf16 v[76:79], a[188:191], v[180:183], v[76:79]\n"
      "ds_read_b128 a[132:135], v217 offset:20480\n"
      "v_cvt_pk_bf16_f32 v187, v166, v167\n"
      "v_mfma_f32_16x16x32_bf16 v[76:79], a[156:159], v[176:179], v[76:79]\n"
      "s_waitcnt lgkmcnt(11)\n"
      "v_exp_f32 v169, v169\n"
      "v_mfma_f32_16x16x32_bf16 v[160:163], a[80:83], a[64:67], v[208:211]\n"
      "s_waitcnt lgkmcnt(6)\n"
      "ds_read_b128 a[124:127], v219 offset:16384\n"
      "v_cvt_pk_bf16_f32 v188, v168, v169\n"
      "v_mfma_f32_16x16x32_bf16 v[160:163], a[84:87], a[68:71], v[160:163]\n"
      "v_exp_f32 v170, v170\n"
      "v_mfma_f32_16x16x32_bf16 v[160:163], a[88:91], a[72:75], v[160:163]\n"
      "ds_read_b128 a[128:131], v216 offset:20480\n"
      "v_mfma_f32_16x16x32_bf16 v[160:163], a[92:95], a[76:79], v[160:163]\n"
      "v_exp_f32 v171, v171\n"
      "v_mfma_f32_16x16x32_bf16 v[164:167], a[108:111], a[76:79], v[208:211]\n"
      "ds_read_b128 a[120:123], v218 offset:16384\n"
      "v_cvt_pk_bf16_f32 v189, v170, v171\n"
      "v_mfma_f32_16x16x32_bf16 v[164:167], a[104:107], a[72:75], v[164:167]\n"
      "s_waitcnt lgkmcnt(6)\n"
      "v_exp_f32 v172, v172\n"
      "v_mfma_f32_16x16x32_bf16 v[164:167], a[100:103], a[68:71], v[164:167]\n"
      "s_waitcnt lgkmcnt(4)\n"
      "ds_read_b128 a[112:115], v216 offset:16384\n"
      "v_mfma_f32_16x16x32_bf16 v[164:167], a[96:99], a[64:67], v[164:167]\n"
      "v_exp_f32 v173, v173\n"
      "v_mfma_f32_16x16x32_bf16 v[80:83], a[160:163], v[176:179], v[80:83]\n"
      "v_cvt_pk_bf16_f32 v190, v172, v173\n"
      "v_mfma_f32_16x16x32_bf16 v[80:83], a[192:195], v[180:183], v[80:83]\n"
      "v_exp_f32 v174, v174\n"
      "v_mfma_f32_16x16x32_bf16 v[84:87], a[196:199], v[180:183], v[84:87]\n"
      "L_ev0%=:\n"
      "s_nop 0\n"
      "v_mfma_f32_16x16x32_bf16 v[84:87], a[164:167], v[176:179], v[84:87]\n"
      "v_exp_f32 v175, v175\n"
      "v_mfma_f32_16x16x32_bf16 v[88:91], a[168:171], v[176:179], v[88:91]\n"
      "v_cvt_pk_bf16_f32 v191, v174, v175\n"
      "v_mfma_f32_16x16x32_bf16 v[88:91], a[200:203], v[180:183], v[88:91]\n"
      "v_exp_f32 v160, v160\n"
      "v_mfma_f32_16x16x32_bf16 v[92:95], a[204:207], v[180:183], v[92:95]\n"
      "buffer_load_dwordx4 v238, s[36:39], s101 offen offset:2048 nt lds\n"
      "v_mfma_f32_16x16x32_bf16 v[92:95], a[172:175], v[176:179], v[92:95]\n"
      "v_exp_f32 v161, v161\n"
      "v_mfma_f32_16x16x32_bf16 v[168:171], a[80:83], a[0:3], v[192:195]\n"
      "v_add_f32 v246, v246, v161\n"
      "v_cvt_pk_bf16_f32 v176, v160, v161\n"
      "v_mfma_f32_16x16x32_bf16 v[168:171], a[84:87], a[4:7], v[168:171]\n"
      "v_exp_f32 v162, v162\n"
      "v_mfma_f32_16x16x32_bf16 v[168:171], a[88:91], a[8:11], v[168:171]\n"
      "v_add_f32 v247, v247, v160\n"
      "v_add_f32 v246, v246, v162\n"
      "v_mfma_f32_16x16x32_bf16 v[168:171], a[92:95], a[12:15], v[168:171]\n"
      "v_exp_f32 v163, v163\n"
      "v_mfma_f32_16x16x32_bf16 v[172:175], a[108:111], a[12:15], v[192:195]\n"
      "v_add_f32 v247, v247, v163\n"
      "v_cvt_pk_bf16_f32 v177, v162, v163\n"
      "v_mfma_f32_16x16x32_bf16 v[172:175], a[104:107], a[8:11], v[172:175]\n"
      "v_exp_f32 v164, v164\n"
      "v_mfma_f32_16x16x32_bf16 v[172:175], a[100:103], a[4:7], v[172:175]\n"
      "v_add_f32 v246, v246, v164\n"
      "v_mfma_f32_16x16x32_bf16 v[172:175], a[96:99], a[0:3], v[172:175]\n"
      "v_exp_f32 v165, v165\n"
      "v_mfma_f32_16x16x32_bf16 v[96:99], a[144:147], v[184:187], v[96:99]\n"
      "v_add_f32 v247, v247, v165\n"
      "v_cvt_pk_bf16_f32 v178, v164, v165\n"
      "v_mfma_f32_16x16x32_bf16 v[96:99], a[176:179], v[188:191], v[96:99]\n"
      "v_exp_f32 v166, v166\n"
      "v_mfma_f32_16x16x32_bf16 v[100:103], a[180:183], v[188:191], v[100:103]\n"
      "ds_read_b64_tr_b16 a[146:147], v220 offset:20480\n"
      "ds_read_b64_tr_b16 a[182:183], v229 offset:20480\n"
      "v_mfma_f32_16x16x32_bf16 v[100:103], a[148:151], v[184:187], v[100:103]\n"
      "v_exp_f32 v167, v167\n"
      "v_mfma_f32_16x16x32_bf16 v[104:107], a[152:155], v[184:187], v[104:107]\n"
      "ds_read_b64_tr_b16 a[150:151], v221 offset:20480\n"
      "v_add_f32 v246, v246, v167\n"
      "v_mfma_f32_16x16x32_bf16 v[104:107], a[184:187], v[188:191], v[104:107]\n"
      "v_exp_f32 v168, v168\n"
      "v_mfma_f32_16x16x32_bf16 v[108:111], a[188:191], v[188:191], v[108:111]\n"
      "ds_read_b64_tr_b16 a[144:145], v220 offset:16384\n"
      "v_cvt_pk_bf16_f32 v179, v166, v167\n"
      "v_mfma_f32_16x16x32_bf16 v[108:111], a[156:159], v[184:187], v[108:111]\n"
      "s_waitcnt lgkmcnt(4)\n"
      "v_exp_f32 v169, v169\n"
      "v_mfma_f32_16x16x32_bf16 v[160:163], a[112:115], a[0:3], v[192:195]\n"
      "buffer_load_dwordx4 v239, s[36:39], s101 offen offset:3072 nt lds\n"
      "v_mfma_f32_16x16x32_bf16 v[160:163], a[116:119], a[4:7], v[160:163]\n"
      "v_exp_f32 v170, v170\n"
      "v_mfma_f32_16x16x32_bf16 v[160:163], a[120:123], a[8:11], v[160:163]\n"
      "v_add_f32 v247, v247, v166\n"
      "v_cvt_pk_bf16_f32 v180, v168, v169\n"
      "v_mfma_f32_16x16x32_bf16 v[160:163], a[124:127], a[12:15], v[160:163]\n"
      "v_exp_f32 v171, v171\n"
      "v_mfma_f32_16x16x32_bf16 v[164:167], a[140:143], a[12:15], v[192:195]\n"
      "ds_read_b64_tr_b16 a[148:149], v221 offset:16384\n"
      "ds_read_b64_tr_b16 a[154:155], v222 offset:20480\n"
      "v_mfma_f32_16x16x32_bf16 v[164:167], a[136:139], a[8:11], v[164:167]\n"
      "v_exp_f32 v172, v172\n"
      "v_mfma_f32_16x16x32_bf16 v[164:167], a[132:135], a[4:7], v[164:167]\n"
      "ds_read_b64_tr_b16 a[156:157], v223 offset:16384\n"
      "ds_read_b64_tr_b16 a[158:159], v223 offset:20480\n"
      "v_mfma_f32_16x16x32_bf16 v[164:167], a[128:131], a[0:3], v[164:167]\n"
      "v_exp_f32 v173, v173\n"
      "v_mfma_f32_16x16x32_bf16 v[112:115], a[160:163], v[184:187], v[112:115]\n"
      "v_cvt_pk_bf16_f32 v181, v170, v171\n"
      "v_cvt_pk_bf16_f32 v182, v172, v173\n"
      "v_mfma_f32_16x16x32_bf16 v[112:115], a[192:195], v[188:191], v[112:115]\n"
      "v_exp_f32 v174, v174\n"
      "v_mfma_f32_16x16x32_bf16 v[116:119], a[196:199], v[188:191], v[116:119]\n"
      "ds_read_b64_tr_b16 a[152:153], v222 offset:16384\n"
      "ds_read_b64_tr_b16 a[160:161], v224 offset:16384\n"
      "v_mfma_f32_16x16x32_bf16 v[116:119], a[164:167], v[184:187], v[116:119]\n"
      "v_exp_f32 v175, v175\n"
      "v_mfma_f32_16x16x32_bf16 v[120:123], a[168:171], v[184:187], v[120:123]\n"
      "ds_read_b64_tr_b16 a[162:163], v224 offset:20480\n"
      "ds_read_b64_tr_b16 a[170:171], v226 offset:20480\n"
      "v_mfma_f32_16x16x32_bf16 v[120:123], a[200:203], v[188:191], v[120:123]\n"
      "ds_read_b64_tr_b16 a[196:197], v233 offset:16384\n"
      "ds_read_b64_tr_b16 a[198:199], v233 offset:20480\n"
      "v_mfma_f32_16x16x32_bf16 v[124:127], a[204:207], v[188:191], v[124:127]\n"
      "ds_read_b64_tr_b16 a[204:205], v235 offset:16384\n"
      "ds_read_b64_tr_b16 a[206:207], v235 offset:20480\n"
      "v_mfma_f32_16x16x32_bf16 v[124:127], a[172:175], v[184:187], v[124:127]\n"
      "v_exp_f32 v160, v160\n"
      "v_mfma_f32_16x16x32_bf16 a[220:223], a[228:231], v[184:187], a[220:223]\n"
      "ds_read_b64_tr_b16 a[166:167], v225 offset:20480\n"
      "ds_read_b64_tr_b16 a[168:169], v226 offset:16384\n"
      "v_mfma_f32_16x16x32_bf16 a[220:223], a[228:231], v[188:191], a[220:223]\n"
      "s_mov_b32 m0, s61\n"
      "v_exp_f32 v161, v161\n"
      "v_mfma_f32_16x16x32_bf16 v[168:171], a[80:83], a[16:19], v[196:199]\n"
      "buffer_load_dwordx4 v240, s[40:43], s101 offen offset:0 nt lds\n"
      "v_mfma_f32_16x16x32_bf16 v[168:171], a[84:87], a[20:23], v[168:171]\n"
      "v_exp_f32 v162, v162\n"
      "v_mfma_f32_16x16x32_bf16 v[168:171], a[88:91], a[24:27], v[168:171]\n"
      "ds_read_b64_tr_b16 a[164:165], v225 offset:16384\n"
      "v_cvt_pk_bf16_f32 v183, v174, v175\n"
      "v_mfma_f32_16x16x32_bf16 v[168:171], a[92:95], a[28:31], v[168:171]\n"
      "v_exp_f32 v163, v163\n"
      "v_mfma_f32_16x16x32_bf16 v[172:175], a[108:111], a[28:31], v[196:199]\n"
      "ds_read_b64_tr_b16 a[172:173], v227 offset:16384\n"
      "ds_read_b64_tr_b16 a[174:175], v227 offset:20480\n"
      "v_mfma_f32_16x16x32_bf16 v[172:175], a[104:107], a[24:27], v[172:175]\n"
      "v_exp_f32 v164, v164\n"
      "v_mfma_f32_16x16x32_bf16 v[172:175], a[100:103], a[20:23], v[172:175]\n"
      "ds_read_b64_tr_b16 a[200:201], v234 offset:16384\n"
      "ds_read_b64_tr_b16 a[202:203], v234 offset:20480\n"
      "v_mfma_f32_16x16x32_bf16 v[172:175], a[96:99], a[16:19], v[172:175]\n"
      "s_waitcnt lgkmcnt(15)\n"
      "v_exp_f32 v165, v165\n"
      "v_mfma_f32_16x16x32_bf16 v[128:131], a[144:147], v[176:179], v[128:131]\n"
      "s_waitcnt lgkmcnt(15)\n"
      "ds_read_b64_tr_b16 a[186:187], v230 offset:20480\n"
      "ds_read_b64_tr_b16 a[194:195], v232 offset:20480\n"
      "v_mfma_f32_16x16x32_bf16 v[132:135], a[148:151], v[176:179], v[132:135]\n"
      "s_waitcnt lgkmcnt(15)\n"
      "v_exp_f32 v166, v166\n"
      "v_mfma_f32_16x16x32_bf16 v[136:139], a[152:155], v[176:179], v[136:139]\n"
      "ds_read_b64_tr_b16 a[176:177], v228 offset:16384\n"
      "ds_read_b64_tr_b16 a[184:185], v230 offset:16384\n"
      "v_mfma_f32_16x16x32_bf16 v[140:143], a[156:159], v[176:179], v[140:143]\n"
      "s_waitcnt lgkmcnt(15)\n"
      "v_exp_f32 v167, v167\n"
      "v_mfma_f32_16x16x32_bf16 v[144:147], a[160:163], v[176:179], v[144:147]\n"
      "s_waitcnt lgkmcnt(8)\n"
      "ds_read_b64_tr_b16 a[178:179], v228 offset:20480\n"
      "ds_read_b64_tr_b16 a[180:181], v229 offset:16384\n"
      "v_mfma_f32_16x16x32_bf16 v[148:151], a[164:167], v[176:179], v[148:151]\n"
      "v_exp_f32 v168, v168\n"
      "v_mfma_f32_16x16x32_bf16 v[152:155], a[168:171], v[176:179], v[152:155]\n"
      "s_waitcnt lgkmcnt(8)\n"
      "v_cvt_pk_bf16_f32 v184, v160, v161\n"
      "v_cvt_pk_bf16_f32 v185, v162, v163\n"
      "v_mfma_f32_16x16x32_bf16 v[156:159], a[172:175], v[176:179], v[156:159]\n"
      "v_exp_f32 v169, v169\n"
      "v_mfma_f32_16x16x32_bf16 v[160:163], a[112:115], a[16:19], v[196:199]\n"
      "v_cvt_pk_bf16_f32 v186, v164, v165\n"
      "v_cvt_pk_bf16_f32 v187, v166, v167\n"
      "v_mfma_f32_16x16x32_bf16 v[160:163], a[116:119], a[20:23], v[160:163]\n"
      "v_exp_f32 v170, v170\n"
      "v_mfma_f32_16x16x32_bf16 v[160:163], a[120:123], a[24:27], v[160:163]\n"
      "buffer_load_dwordx4 v241, s[40:43], s101 offen offset:1024 nt lds\n"
      "v_mfma_f32_16x16x32_bf16 v[160:163], a[124:127], a[28:31], v[160:163]\n"
      "v_exp_f32 v171, v171\n"
      "v_mfma_f32_16x16x32_bf16 v[164:167], a[140:143], a[28:31], v[196:199]\n"
      "ds_read_b64_tr_b16 a[188:189], v231 offset:16384\n"
      "ds_read_b64_tr_b16 a[190:191], v231 offset:20480\n"
      "v_mfma_f32_16x16x32_bf16 v[164:167], a[136:139], a[24:27], v[164:167]\n"
      "v_exp_f32 v172, v172\n"
      "v_mfma_f32_16x16x32_bf16 v[164:167], a[132:135], a[20:23], v[164:167]\n"
      "ds_read_b64_tr_b16 a[192:193], v232 offset:16384\n"
      "v_add_f32 v242, v242, v168\n"
      "v_mfma_f32_16x16x32_bf16 v[164:167], a[128:131], a[16:19], v[164:167]\n"
      "v_exp_f32 v173, v173\n"
      "v_mfma_f32_16x16x32_bf16 v[0:3], a[144:147], v[180:183], v[0:3]\n"
      "s_waitcnt lgkmcnt(4)\n"
      "v_add_f32 v243, v243, v169\n"
      "v_cvt_pk_bf16_f32 v188, v168, v169\n"
      "v_mfma_f32_16x16x32_bf16 v[0:3], a[176:179], v[184:187], v[0:3]\n"
      "s_waitcnt lgkmcnt(3)\n"
      "v_exp_f32 v174, v174\n"
      "v_mfma_f32_16x16x32_bf16 v[4:7], a[180:183], v[184:187], v[4:7]\n"
      "v_add_f32 v242, v242, v171\n"
      "v_cvt_pk_bf16_f32 v189, v170, v171\n"
      "v_mfma_f32_16x16x32_bf16 v[4:7], a[148:151], v[180:183], v[4:7]\n"
      "v_exp_f32 v175, v175\n"
      "v_mfma_f32_16x16x32_bf16 v[8:11], a[152:155], v[180:183], v[8:11]\n"
      "v_add_f32 v243, v243, v170\n"
      "v_add_f32 v242, v242, v172\n"
      "v_mfma_f32_16x16x32_bf16 v[8:11], a[184:187], v[184:187], v[8:11]\n"
      "s_waitcnt lgkmcnt(1)\n"
      "v_exp_f32 v160, v160\n"
      "v_mfma_f32_16x16x32_bf16 v[12:15], a[188:191], v[184:187], v[12:15]\n"
      "v_add_f32 v243, v243, v173\n"
      "v_cvt_pk_bf16_f32 v190, v172, v173\n"
      "v_mfma_f32_16x16x32_bf16 v[12:15], a[156:159], v[180:183], v[12:15]\n"
      "v_exp_f32 v161, v161\n"
      "v_mfma_f32_16x16x32_bf16 v[168:171], a[80:83], a[32:35], v[200:203]\n"
      "v_add_f32 v242, v242, v174\n"
      "v_cvt_pk_bf16_f32 v191, v174, v175\n"
      "v_mfma_f32_16x16x32_bf16 v[168:171], a[84:87], a[36:39], v[168:171]\n"
      "v_exp_f32 v162, v162\n"
      "v_mfma_f32_16x16x32_bf16 v[168:171], a[88:91], a[40:43], v[168:171]\n"
      "v_add_f32 v243, v243, v175\n"
      "v_add_f32 v242, v242, v160\n"
      "v_mfma_f32_16x16x32_bf16 v[168:171], a[92:95], a[44:47], v[168:171]\n"
      "v_exp_f32 v163, v163\n"
      "v_mfma_f32_16x16x32_bf16 v[172:175], a[108:111], a[44:47], v[200:203]\n"
      "buffer_load_dwordx4 v240, s[40:43], s101 offen offset:2048 nt lds\n"
      "v_mfma_f32_16x16x32_bf16 v[172:175], a[104:107], a[40:43], v[172:175]\n"
      "v_exp_f32 v164, v164\n"
      "v_mfma_f32_16x16x32_bf16 v[172:175], a[100:103], a[36:39], v[172:175]\n"
      "v_add_f32 v243, v243, v161\n"
      "v_cvt_pk_bf16_f32 v176, v160, v161\n"
      "v_mfma_f32_16x16x32_bf16 v[172:175], a[96:99], a[32:35], v[172:175]\n"
      "v_exp_f32 v165, v165\n"
      "v_mfma_f32_16x16x32_bf16 v[16:19], a[160:163], v[180:183], v[16:19]\n"
      "s_waitcnt lgkmcnt(0)\n"
      "v_add_f32 v242, v242, v162\n"
      "v_add_f32 v243, v243, v163\n"
      "v_mfma_f32_16x16x32_bf16 v[16:19], a[192:195], v[184:187], v[16:19]\n"
      "v_exp_f32 v166, v166\n"
      "v_mfma_f32_16x16x32_bf16 v[20:23], a[196:199], v[184:187], v[20:23]\n"
      "v_cvt_pk_bf16_f32 v177, v162, v163\n"
      "v_cvt_pk_bf16_f32 v178, v164, v165\n"
      "v_mfma_f32_16x16x32_bf16 v[20:23], a[164:167], v[180:183], v[20:23]\n"
      "v_exp_f32 v167, v167\n"
      "v_mfma_f32_16x16x32_bf16 v[24:27], a[168:171], v[180:183], v[24:27]\n"
      "v_add_f32 v242, v242, v164\n"
      "v_add_f32 v243, v243, v165\n"
      "v_mfma_f32_16x16x32_bf16 v[24:27], a[200:203], v[184:187], v[24:27]\n"
      "v_add_f32 v242, v242, v166\n"
      "v_cvt_pk_bf16_f32 v179, v166, v167\n"
      "v_mfma_f32_16x16x32_bf16 v[28:31], a[204:207], v[184:187], v[28:31]\n"
      "v_add_f32 v243, v243, v167\n"
      "v_mfma_f32_16x16x32_bf16 v[28:31], a[172:175], v[180:183], v[28:31]\n"
      "v_exp_f32 v168, v168\n"
      "v_mfma_f32_16x16x32_bf16 a[208:211], a[228:231], v[180:183], a[208:211]\n"
      "v_add_f32 v244, v244, v168\n"
      "v_mfma_f32_16x16x32_bf16 a[208:211], a[228:231], v[184:187], a[208:211]\n"
      "v_exp_f32 v169, v169\n"
      "v_mfma_f32_16x16x32_bf16 v[160:163], a[112:115], a[32:35], v[200:203]\n"
      "v_add_f32 v245, v245, v169\n"
      "v_cvt_pk_bf16_f32 v180, v168, v169\n"
      "v_mfma_f32_16x16x32_bf16 v[160:163], a[116:119], a[36:39], v[160:163]\n"
      "v_exp_f32 v170, v170\n"
      "v_mfma_f32_16x16x32_bf16 v[160:163], a[120:123], a[40:43], v[160:163]\n"
      "v_add_f32 v244, v244, v170\n"
      "v_mfma_f32_16x16x32_bf16 v[160:163], a[124:127], a[44:47], v[160:163]\n"
      "v_exp_f32 v171, v171\n"
      "v_mfma_f32_16x16x32_bf16 v[164:167], a[140:143], a[44:47], v[200:203]\n"
      "buffer_load_dwordx4 v241, s[40:43], s101 offen offset:3072 nt lds\n"
      "v_mfma_f32_16x16x32_bf16 v[164:167], a[136:139], a[40:43], v[164:167]\n"
      "s_add_u32 s52, s52, 0x100\n"
      "v_exp_f32 v172, v172\n"
      "v_mfma_f32_16x16x32_bf16 v[164:167], a[132:135], a[36:39], v[164:167]\n"
      "s_or_b32 s52, s52, 0x1bc00\n"
      "v_add_f32 v245, v245, v171\n"
      "v_cvt_pk_bf16_f32 v181, v170, v171\n"
      "v_mfma_f32_16x16x32_bf16 v[164:167], a[128:131], a[32:35], v[164:167]\n"
      "v_exp_f32 v173, v173\n"
      "v_mfma_f32_16x16x32_bf16 v[32:35], a[144:147], v[188:191], v[32:35]\n"
      "s_xor_b32 s101, s52, s100\n"
      "v_add_f32 v244, v244, v172\n"
      "v_cvt_pk_bf16_f32 v182, v172, v173\n"
      "v_mfma_f32_16x16x32_bf16 v[32:35], a[176:179], v[176:179], v[32:35]\n"
      "v_exp_f32 v174, v174\n"
      "v_mfma_f32_16x16x32_bf16 v[36:39], a[180:183], v[176:179], v[36:39]\n"
      "v_add_f32 v245, v245, v173\n"
      "v_add_f32 v244, v244, v174\n"
      "v_mfma_f32_16x16x32_bf16 v[36:39], a[148:151], v[188:191], v[36:39]\n"
      "v_exp_f32 v175, v175\n"
      "v_mfma_f32_16x16x32_bf16 v[40:43], a[152:155], v[188:191], v[40:43]\n"
      "v_add_f32 v245, v245, v175\n"
      "v_cvt_pk_bf16_f32 v183, v174, v175\n"
      "v_mfma_f32_16x16x32_bf16 v[40:43], a[184:187], v[176:179], v[40:43]\n"
      "v_exp_f32 v160, v160\n"
      "v_mfma_f32_16x16x32_bf16 v[44:47], a[188:191], v[176:179], v[44:47]\n"
      "v_add_f32 v244, v244, v160\n"
      "v_mfma_f32_16x16x32_bf16 v[44:47], a[156:159], v[188:191], v[44:47]\n"
      "s_waitcnt vmcnt(8) lgkmcnt(0)\n"
      "s_barrier\n"
      "v_exp_f32 v161, v161\n"
      "v_mfma_f32_16x16x32_bf16 v[168:171], a[80:83], a[48:51], v[204:207]\n"
      "ds_read_b128 a[80:83], v212 offset:32768\n"
      "v_add_f32 v245, v245, v161\n"
      "v_mfma_f32_16x16x32_bf16 v[168:171], a[84:87], a[52:55], v[168:171]\n"
      "v_exp_f32 v162, v162\n"
      "v_mfma_f32_16x16x32_bf16 v[168:171], a[88:91], a[56:59], v[168:171]\n"
      "v_add_f32 v244, v244, v162\n"
      "v_cvt_pk_bf16_f32 v184, v160, v161\n"
      "v_mfma_f32_16x16x32_bf16 v[168:171], a[92:95], a[60:63], v[168:171]\n"
      "v_exp_f32 v163, v163\n"
      "v_mfma_f32_16x16x32_bf16 v[172:175], a[108:111], a[60:63], v[204:207]\n"
      "ds_read_b128 a[108:111], v215 offset:36864\n"
      "v_add_f32 v245, v245, v163\n"
      "v_mfma_f32_16x16x32_bf16 v[172:175], a[104:107], a[56:59], v[172:175]\n"
      "v_exp_f32 v164, v164\n"
      "v_mfma_f32_16x16x32_bf16 v[172:175], a[100:103], a[52:55], v[172:175]\n"
      "ds_read_b128 a[88:91], v214 offset:32768\n"
      "v_cvt_pk_bf16_f32 v185, v162, v163\n"
      "v_mfma_f32_16x16x32_bf16 v[172:175], a[96:99], a[48:51], v[172:175]\n"
      "v_exp_f32 v165, v165\n"
      "v_mfma_f32_16x16x32_bf16 v[48:51], a[160:163], v[188:191], v[48:51]\n"
      "ds_read_b128 a[104:107], v214 offset:36864\n"
      "v_add_f32 v244, v244, v165\n"
      "v_mfma_f32_16x16x32_bf16 v[48:51], a[192:195], v[176:179], v[48:51]\n"
      "s_mov_b32 m0, s54\n"
      "v_exp_f32 v166, v166\n"
      "v_mfma_f32_16x16x32_bf16 v[52:55], a[196:199], v[176:179], v[52:55]\n"
      "buffer_load_dwordx4 v236, s[36:39], s101 offen offset:0 nt lds\n"
      "v_mfma_f32_16x16x32_bf16 v[52:55], a[164:167], v[188:191], v[52:55]\n"
      "v_exp_f32 v167, v167\n"
      "v_mfma_f32_16x16x32_bf16 v[56:59], a[168:171], v[188:191], v[56:59]\n"
      "ds_read_b128 a[92:95], v215 offset:32768\n"
      "v_add_f32 v245, v245, v164\n"
      "v_mfma_f32_16x16x32_bf16 v[56:59], a[200:203], v[176:179], v[56:59]\n"
      "v_exp_f32 v168, v168\n"
      "v_mfma_f32_16x16x32_bf16 v[60:63], a[204:207], v[176:179], v[60:63]\n"
      "ds_read_b128 a[84:87], v213 offset:32768\n"
      "v_cvt_pk_bf16_f32 v186, v164, v165\n"
      "v_mfma_f32_16x16x32_bf16 v[60:63], a[172:175], v[188:191], v[60:63]\n"
      "v_exp_f32 v169, v169\n"
      "v_mfma_f32_16x16x32_bf16 v[160:163], a[112:115], a[48:51], v[204:207]\n"
      "v_add_f32 v244, v244, v167\n"
      "v_cvt_pk_bf16_f32 v187, v166, v167\n"
      "v_mfma_f32_16x16x32_bf16 v[160:163], a[116:119], a[52:55], v[160:163]\n"
      "v_exp_f32 v170, v170\n"
      "v_mfma_f32_16x16x32_bf16 v[160:163], a[120:123], a[56:59], v[160:163]\n"
      "ds_read_b128 a[116:119], v217 offset:32768\n"
      "v_add_f32 v245, v245, v166\n"
      "v_mfma_f32_16x16x32_bf16 v[160:163], a[124:127], a[60:63], v[160:163]\n"
      "v_exp_f32 v171, v171\n"
      "v_mfma_f32_16x16x32_bf16 v[164:167], a[140:143], a[60:63], v[204:207]\n"
      "ds_read_b128 a[140:143], v219 offset:36864\n"
      "v_cvt_pk_bf16_f32 v189, v170, v171\n"
      "v_mfma_f32_16x16x32_bf16 v[164:167], a[136:139], a[56:59], v[164:167]\n"
      "v_exp_f32 v172, v172\n"
      "v_mfma_f32_16x16x32_bf16 v[164:167], a[132:135], a[52:55], v[164:167]\n"
      "ds_read_b128 a[100:103], v213 offset:36864\n"
      "v_cvt_pk_bf16_f32 v188, v168, v169\n"
      "v_mfma_f32_16x16x32_bf16 v[164:167], a[128:131], a[48:51], v[164:167]\n"
      "v_exp_f32 v173, v173\n"
      "v_mfma_f32_16x16x32_bf16 v[64:67], a[144:147], v[180:183], v[64:67]\n"
      "ds_read_b128 a[136:139], v218 offset:36864\n"
      "v_cvt_pk_bf16_f32 v190, v172, v173\n"
      "v_mfma_f32_16x16x32_bf16 v[64:67], a[176:179], v[184:187], v[64:67]\n"
      "v_exp_f32 v174, v174\n"
      "v_mfma_f32_16x16x32_bf16 v[68:71], a[180:183], v[184:187], v[68:71]\n"
      "ds_read_b128 a[96:99], v212 offset:36864\n"
      "v_mfma_f32_16x16x32_bf16 v[68:71], a[148:151], v[180:183], v[68:71]\n"
      "v_exp_f32 v175, v175\n"
      "v_mfma_f32_16x16x32_bf16 v[72:75], a[152:155], v[180:183], v[72:75]\n"
      "buffer_load_dwordx4 v237, s[36:39], s101 offen offset:1024 nt lds\n"
      "v_mfma_f32_16x16x32_bf16 v[72:75], a[184:187], v[184:187], v[72:75]\n"
      "v_exp_f32 v160, v160\n"
      "v_mfma_f32_16x16x32_bf16 v[76:79], a[188:191], v[184:187], v[76:79]\n"
      "ds_read_b128 a[132:135], v217 offset:36864\n"
      "v_cvt_pk_bf16_f32 v191, v174, v175\n"
      "v_mfma_f32_16x16x32_bf16 v[76:79], a[156:159], v[180:183], v[76:79]\n"
      "s_waitcnt lgkmcnt(11)\n"
      "v_exp_f32 v161, v161\n"
      "v_mfma_f32_16x16x32_bf16 v[168:171], a[80:83], a[64:67], v[208:211]\n"
      "s_waitcnt lgkmcnt(6)\n"
      "ds_read_b128 a[124:127], v219 offset:32768\n"
      "v_cvt_pk_bf16_f32 v176, v160, v161\n"
      "v_mfma_f32_16x16x32_bf16 v[168:171], a[84:87], a[68:71], v[168:171]\n"
      "v_exp_f32 v162, v162\n"
      "v_mfma_f32_16x16x32_bf16 v[168:171], a[88:91], a[72:75], v[168:171]\n"
      "ds_read_b128 a[128:131], v216 offset:36864\n"
      "v_mfma_f32_16x16x32_bf16 v[168:171], a[92:95], a[76:79], v[168:171]\n"
      "v_exp_f32 v163, v163\n"
      "v_mfma_f32_16x16x32_bf16 v[172:175], a[108:111], a[76:79], v[208:211]\n"
      "ds_read_b128 a[120:123], v218 offset:32768\n"
      "v_cvt_pk_bf16_f32 v177, v162, v163\n"
      "v_mfma_f32_16x16x32_bf16 v[172:175], a[104:107], a[72:75], v[172:175]\n"
      "s_waitcnt lgkmcnt(6)\n"
      "v_exp_f32 v164, v164\n"
      "v_mfma_f32_16x16x32_bf16 v[172:175], a[100:103], a[68:71], v[172:175]\n"
      "s_waitcnt lgkmcnt(4)\n"
      "ds_read_b128 a[112:115], v216 offset:32768\n"
      "v_mfma_f32_16x16x32_bf16 v[172:175], a[96:99], a[64:67], v[172:175]\n"
      "v_exp_f32 v165, v165\n"
      "v_mfma_f32_16x16x32_bf16 v[80:83], a[160:163], v[180:183], v[80:83]\n"
      "v_cvt_pk_bf16_f32 v178, v164, v165\n"
      "v_mfma_f32_16x16x32_bf16 v[80:83], a[192:195], v[184:187], v[80:83]\n"
      "v_exp_f32 v166, v166\n"
      "v_mfma_f32_16x16x32_bf16 v[84:87], a[196:199], v[184:187], v[84:87]\n"
      "L_ev1%=:\n"
      "s_nop 0\n"
      "v_mfma_f32_16x16x32_bf16 v[84:87], a[164:167], v[180:183], v[84:87]\n"
      "v_exp_f32 v167, v167\n"
      "v_mfma_f32_16x16x32_bf16 v[88:91], a[168:171], v[180:183], v[88:91]\n"
      "v_cvt_pk_bf16_f32 v179, v166, v167\n"
      "v_mfma_f32_16x16x32_bf16 v[88:91], a[200:203], v[184:187], v[88:91]\n"
      "v_exp_f32 v168, v168\n"
      "v_mfma_f32_16x16x32_bf16 v[92:95], a[204:207], v[184:187], v[92:95]\n"
      "buffer_load_dwordx4 v238, s[36:39], s101 offen offset:2048 nt lds\n"
      "v_mfma_f32_16x16x32_bf16 v[92:95], a[172:175], v[180:183], v[92:95]\n"
      "v_exp_f32 v169, v169\n"
      "v_mfma_f32_16x16x32_bf16 v[160:163], a[80:83], a[0:3], v[192:195]\n"
      "v_add_f32 v246, v246, v169\n"
      "v_cvt_pk_bf16_f32 v180, v168, v169\n"
      "v_mfma_f32_16x16x32_bf16 v[160:163], a[84:87], a[4:7], v[160:163]\n"
      "v_exp_f32 v170, v170\n"
      "v_mfma_f32_16x16x32_bf16 v[160:163], a[88:91], a[8:11], v[160:163]\n"
      "v_add_f32 v247, v247, v168\n"
      "v_add_f32 v246, v246, v170\n"
      "v_mfma_f32_16x16x32_bf16 v[160:163], a[92:95], a[12:15], v[160:163]\n"
      "v_exp_f32 v171, v171\n"
      "v_mfma_f32_16x16x32_bf16 v[164:167], a[108:111], a[12:15], v[192:195]\n"
      "v_add_f32 v247, v247, v171\n"
      "v_cvt_pk_bf16_f32 v181, v170, v171\n"
      "v_mfma_f32_16x16x32_bf16 v[164:167], a[104:107], a[8:11], v[164:167]\n"
      "v_exp_f32 v172, v172\n"
      "v_mfma_f32_16x16x32_bf16 v[164:167], a[100:103], a[4:7], v[164:167]\n"
      "v_add_f32 v246, v246, v172\n"
      "v_mfma_f32_16x16x32_bf16 v[164:167], a[96:99], a[0:3], v[164:167]\n"
      "v_exp_f32 v173, v173\n"
      "v_mfma_f32_16x16x32_bf16 v[96:99], a[144:147], v[188:191], v[96:99]\n"
      "v_add_f32 v247, v247, v173\n"
      "v_cvt_pk_bf16_f32 v182, v172, v173\n"
      "v_mfma_f32_16x16x32_bf16 v[96:99], a[176:179], v[176:179], v[96:99]\n"
      "v_exp_f32 v174, v174\n"
      "v_mfma_f32_16x16x32_bf16 v[100:103], a[180:183], v[176:179], v[100:103]\n"
      "ds_read_b64_tr_b16 a[146:147], v220 offset:36864\n"
      "ds_read_b64_tr_b16 a[182:183], v229 offset:36864\n"
      "v_mfma_f32_16x16x32_bf16 v[100:103], a[148:151], v[188:191], v[100:103]\n"
      "v_exp_f32 v175, v175\n"
      "v_mfma_f32_16x16x32_bf16 v[104:107], a[152:155], v[188:191], v[104:107]\n"
      "ds_read_b64_tr_b16 a[150:151], v221 offset:36864\n"
      "v_add_f32 v246, v246, v175\n"
      "v_mfma_f32_16x16x32_bf16 v[104:107], a[184:187], v[176:179], v[104:107]\n"
      "v_exp_f32 v160, v160\n"
      "v_mfma_f32_16x16x32_bf16 v[108:111], a[188:191], v[176:179], v[108:111]\n"
      "ds_read_b64_tr_b16 a[144:145], v220 offset:32768\n"
      "v_cvt_pk_bf16_f32 v183, v174, v175\n"
      "v_mfma_f32_16x16x32_bf16 v[108:111], a[156:159], v[188:191], v[108:111]\n"
      "s_waitcnt lgkmcnt(4)\n"
      "v_exp_f32 v161, v161\n"
      "v_mfma_f32_16x16x32_bf16 v[168:171], a[112:115], a[0:3], v[192:195]\n"
      "buffer_load_dwordx4 v239, s[36:39], s101 offen offset:3072 nt lds\n"
      "v_mfma_f32_16x16x32_bf16 v[168:171], a[116:119], a[4:7], v[168:171]\n"
      "v_exp_f32 v162, v162\n"
      "v_mfma_f32_16x16x32_bf16 v[168:171], a[120:123], a[8:11], v[168:171]\n"
      "v_add_f32 v247, v247, v174\n"
      "v_cvt_pk_bf16_f32 v184, v160, v161\n"
      "v_mfma_f32_16x16x32_bf16 v[168:171], a[124:127], a[12:15], v[168:171]\n"
      "v_exp_f32 v163, v163\n"
      "v_mfma_f32_16x16x32_bf16 v[172:175], a[140:143], a[12:15], v[192:195]\n"
      "ds_read_b64_tr_b16 a[148:149], v221 offset:32768\n"
      "ds_read_b64_tr_b16 a[154:155], v222 offset:36864\n"
      "v_mfma_f32_16x16x32_bf16 v[172:175], a[136:139], a[8:11], v[172:175]\n"
      "v_exp_f32 v164, v164\n"
      "v_mfma_f32_16x16x32_bf16 v[172:175], a[132:135], a[4:7], v[172:175]\n"
      "ds_read_b64_tr_b16 a[156:157], v223 offset:32768\n"
      "ds_read_b64_tr_b16 a[158:159], v223 offset:36864\n"
      "v_mfma_f32_16x16x32_bf16 v[172:175], a[128:131], a[0:3], v[172:175]\n"
      "v_exp_f32 v165, v165\n"
      "v_mfma_f32_16x16x32_bf16 v[112:115], a[160:163], v[188:191], v[112:115]\n"
      "v_cvt_pk_bf16_f32 v185, v162, v163\n"
      "v_cvt_pk_bf16_f32 v186, v164, v165\n"
      "v_mfma_f32_16x16x32_bf16 v[112:115], a[192:195], v[176:179], v[112:115]\n"
      "v_exp_f32 v166, v166\n"
      "v_mfma_f32_16x16x32_bf16 v[116:119], a[196:199], v[176:179], v[116:119]\n"
      "ds_read_b64_tr_b16 a[152:153], v222 offset:32768\n"
      "ds_read_b64_tr_b16 a[160:161], v224 offset:32768\n"
      "v_mfma_f32_16x16x32_bf16 v[116:119], a[164:167], v[188:191], v[116:119]\n"
      "v_exp_f32 v167, v167\n"
      "v_mfma_f32_16x16x32_bf16 v[120:123], a[168:171], v[188:191], v[120:123]\n"
      "ds_read_b64_tr_b16 a[162:163], v224 offset:36864\n"
      "ds_read_b64_tr_b16 a[170:171], v226 offset:36864\n"
      "v_mfma_f32_16x16x32_bf16 v[120:123], a[200:203], v[176:179], v[120:123]\n"
      "ds_read_b64_tr_b16 a[196:197], v233 offset:32768\n"
      "ds_read_b64_tr_b16 a[198:199], v233 offset:36864\n"
      "v_mfma_f32_16x16x32_bf16 v[124:127], a[204:207], v[176:179], v[124:127]\n"
      "ds_read_b64_tr_b16 a[204:205], v235 offset:32768\n"
      "ds_read_b64_tr_b16 a[206:207], v235 offset:36864\n"
      "v_mfma_f32_16x16x32_bf16 v[124:127], a[172:175], v[188:191], v[124:127]\n"
      "v_exp_f32 v168, v168\n"
      "v_mfma_f32_16x16x32_bf16 a[220:223], a[228:231], v[188:191], a[220:223]\n"
      "ds_read_b64_tr_b16 a[166:167], v225 offset:36864\n"
      "ds_read_b64_tr_b16 a[168:169], v226 offset:32768\n"
      "v_mfma_f32_16x16x32_bf16 a[220:223], a[228:231], v[176:179], a[220:223]\n"
      "s_mov_b32 m0, s58\n"
      "v_exp_f32 v169, v169\n"
      "v_mfma_f32_16x16x32_bf16 v[160:163], a[80:83], a[16:19], v[196:199]\n"
      "buffer_load_dwordx4 v240, s[40:43], s101 offen offset:0 nt lds\n"
      "v_mfma_f32_16x16x32_bf16 v[160:163], a[84:87], a[20:23], v[160:163]\n"
      "v_exp_f32 v170, v170\n"
      "v_mfma_f32_16x16x32_bf16 v[160:163], a[88:91], a[24:27], v[160:163]\n"
      "ds_read_b64_tr_b16 a[164:165], v225 offset:32768\n"
      "v_cvt_pk_bf16_f32 v187, v166, v167\n"
      "v_mfma_f32_16x16x32_bf16 v[160:163], a[92:95], a[28:31], v[160:163]\n"
      "v_exp_f32 v171, v171\n"
      "v_mfma_f32_16x16x32_bf16 v[164:167], a[108:111], a[28:31], v[196:199]\n"
      "ds_read_b64_tr_b16 a[172:173], v227 offset:32768\n"
      "ds_read_b64_tr_b16 a[174:175], v227 offset:36864\n"
      "v_mfma_f32_16x16x32_bf16 v[164:167], a[104:107], a[24:27], v[164:167]\n"
      "v_exp_f32 v172, v172\n"
      "v_mfma_f32_16x16x32_bf16 v[164:167], a[100:103], a[20:23], v[164:167]\n"
      "ds_read_b64_tr_b16 a[200:201], v234 offset:32768\n"
      "ds_read_b64_tr_b16 a[202:203], v234 offset:36864\n"
      "v_mfma_f32_16x16x32_bf16 v[164:167], a[96:99], a[16:19], v[164:167]\n"
      "s_waitcnt lgkmcnt(15)\n"
      "v_exp_f32 v173, v173\n"
      "v_mfma_f32_16x16x32_bf16 v[128:131], a[144:147], v[180:183], v[128:131]\n"
      "s_waitcnt lgkmcnt(15)\n"
      "ds_read_b64_tr_b16 a[186:187], v230 offset:36864\n"
      "ds_read_b64_tr_b16 a[194:195], v232 offset:36864\n"
      "v_mfma_f32_16x16x32_bf16 v[132:135], a[148:151], v[180:183], v[132:135]\n"
      "s_waitcnt lgkmcnt(15)\n"
      "v_exp_f32 v174, v174\n"
      "v_mfma_f32_16x16x32_bf16 v[136:139], a[152:155], v[180:183], v[136:139]\n"
      "ds_read_b64_tr_b16 a[176:177], v228 offset:32768\n"
      "ds_read_b64_tr_b16 a[184:185], v230 offset:32768\n"
      "v_mfma_f32_16x16x32_bf16 v[140:143], a[156:159], v[180:183], v[140:143]\n"
      "s_waitcnt lgkmcnt(15)\n"
      "v_exp_f32 v175, v175\n"
      "v_mfma_f32_16x16x32_bf16 v[144:147], a[160:163], v[180:183], v[144:147]\n"
      "s_waitcnt lgkmcnt(8)\n"
      "ds_read_b64_tr_b16 a[178:179], v228 offset:36864\n"
      "ds_read_b64_tr_b16 a[180:181], v229 offset:32768\n"
      "v_mfma_f32_16x16x32_bf16 v[148:151], a[164:167], v[180:183], v[148:151]\n"
      "v_exp_f32 v160, v160\n"
      "v_mfma_f32_16x16x32_bf16 v[152:155], a[168:171], v[180:183], v[152:155]\n"
      "s_waitcnt lgkmcnt(8)\n"
      "v_cvt_pk_bf16_f32 v188, v168, v169\n"
      "v_cvt_pk_bf16_f32 v189, v170, v171\n"
      "v_mfma_f32_16x16x32_bf16 v[156:159], a[172:175], v[180:183], v[156:159]\n"
      "v_exp_f32 v161, v161\n"
      "v_mfma_f32_16x16x32_bf16 v[168:171], a[112:115], a[16:19], v[196:199]\n"
      "v_cvt_pk_bf16_f32 v190, v172, v173\n"
      "v_cvt_pk_bf16_f32 v191, v174, v175\n"
      "v_mfma_f32_16x16x32_bf16 v[168:171], a[116:119], a[20:23], v[168:171]\n"
      "v_exp_f32 v162, v162\n"
      "v_mfma_f32_16x16x32_bf16 v[168:171], a[120:123], a[24:27], v[168:171]\n"
      "buffer_load_dwordx4 v241, s[40:43], s101 offen offset:1024 nt lds\n"
      "v_mfma_f32_16x16x32_bf16 v[168:171], a[124:127], a[28:31], v[168:171]\n"
      "v_exp_f32 v163, v163\n"
      "v_mfma_f32_16x16x32_bf16 v[172:175], a[140:143], a[28:31], v[196:199]\n"
      "ds_read_b64_tr_b16 a[188:189], v231 offset:32768\n"
      "ds_read_b64_tr_b16 a[190:191], v231 offset:36864\n"
      "v_mfma_f32_16x16x32_bf16 v[172:175], a[136:139], a[24:27], v[172:175]\n"
      "v_exp_f32 v164, v164\n"
      "v_mfma_f32_16x16x32_bf16 v[172:175], a[132:135], a[20:23], v[172:175]\n"
      "ds_read_b64_tr_b16 a[192:193], v232 offset:32768\n"
      "v_add_f32 v242, v242, v160\n"
      "v_mfma_f32_16x16x32_bf16 v[172:175], a[128:131], a[16:19], v[172:175]\n"
      "v_exp_f32 v165, v165\n"
      "v_mfma_f32_16x16x32_bf16 v[0:3], a[144:147], v[184:187], v[0:3]\n"
      "s_waitcnt lgkmcnt(4)\n"
      "v_add_f32 v243, v243, v161\n"
      "v_cvt_pk_bf16_f32 v176, v160, v161\n"
      "v_mfma_f32_16x16x32_bf16 v[0:3], a[176:179], v[188:191], v[0:3]\n"
      "s_waitcnt lgkmcnt(3)\n"
      "v_exp_f32 v166, v166\n"
      "v_mfma_f32_16x16x32_bf16 v[4:7], a[180:183], v[188:191], v[4:7]\n"
      "v_add_f32 v242, v242, v163\n"
      "v_cvt_pk_bf16_f32 v177, v162, v163\n"
      "v_mfma_f32_16x16x32_bf16 v[4:7], a[148:151], v[184:187], v[4:7]\n"
      "v_exp_f32 v167, v167\n"
      "v_mfma_f32_16x16x32_bf16 v[8:11], a[152:155], v[184:187], v[8:11]\n"
      "v_add_f32 v243, v243, v162\n"
      "v_add_f32 v242, v242, v164\n"
      "v_mfma_f32_16x16x32_bf16 v[8:11], a[184:187], v[188:191], v[8:11]\n"
      "s_waitcnt lgkmcnt(1)\n"
      "v_exp_f32 v168, v168\n"
      "v_mfma_f32_16x16x32_bf16 v[12:15], a[188:191], v[188:191], v[12:15]\n"
      "v_add_f32 v243, v243, v165\n"
      "v_cvt_pk_bf16_f32 v178, v164, v165\n"
      "v_mfma_f32_16x16x32_bf16 v[12:15], a[156:159], v[184:187], v[12:15]\n"
      "v_exp_f32 v169, v169\n"
      "v_mfma_f32_16x16x32_bf16 v[160:163], a[80:83], a[32:35], v[200:203]\n"
      "v_add_f32 v242, v242, v166\n"
      "v_cvt_pk_bf16_f32 v179, v166, v167\n"
      "v_mfma_f32_16x16x32_bf16 v[160:163], a[84:87], a[36:39], v[160:163]\n"
      "v_exp_f32 v170, v170\n"
      "v_mfma_f32_16x16x32_bf16 v[160:163], a[88:91], a[40:43], v[160:163]\n"
      "v_add_f32 v243, v243, v167\n"
      "v_add_f32 v242, v242, v168\n"
      "v_mfma_f32_16x16x32_bf16 v[160:163], a[92:95], a[44:47], v[160:163]\n"
      "v_exp_f32 v171, v171\n"
      "v_mfma_f32_16x16x32_bf16 v[164:167], a[108:111], a[44:47], v[200:203]\n"
      "buffer_load_dwordx4 v240, s[40:43], s101 offen offset:2048 nt lds\n"
      "v_mfma_f32_16x16x32_bf16 v[164:167], a[104:107], a[40:43], v[164:167]\n"
      "v_exp_f32 v172, v172\n"
      "v_mfma_f32_16x16x32_bf16 v[164:167], a[100:103], a[36:39], v[164:167]\n"
      "v_add_f32 v243, v243, v169\n"
      "v_cvt_pk_bf16_f32 v180, v168, v169\n"
      "v_mfma_f32_16x16x32_bf16 v[164:167], a[96:99], a[32:35], v[164:167]\n"
      "v_exp_f32 v173, v173\n"
      "v_mfma_f32_16x16x32_bf16 v[16:19], a[160:163], v[184:187], v[16:19]\n"
      "s_waitcnt lgkmcnt(0)\n"
      "v_add_f32 v242, v242, v170\n"
      "v_add_f32 v243, v243, v171\n"
      "v_mfma_f32_16x16x32_bf16 v[16:19], a[192:195], v[188:191], v[16:19]\n"
      "v_exp_f32 v174, v174\n"
      "v_mfma_f32_16x16x32_bf16 v[20:23], a[196:199], v[188:191], v[20:23]\n"
      "v_cvt_pk_bf16_f32 v181, v170, v171\n"
      "v_cvt_pk_bf16_f32 v182, v172, v173\n"
      "v_mfma_f32_16x16x32_bf16 v[20:23], a[164:167], v[184:187], v[20:23]\n"
      "v_exp_f32 v175, v175\n"
      "v_mfma_f32_16x16x32_bf16 v[24:27], a[168:171], v[184:187], v[24:27]\n"
      "v_add_f32 v242, v242, v172\n"
      "v_add_f32 v243, v243, v173\n"
      "v_mfma_f32_16x16x32_bf16 v[24:27], a[200:203], v[188:191], v[24:27]\n"
      "v_add_f32 v242, v242, v174\n"
      "v_cvt_pk_bf16_f32 v183, v174, v175\n"
      "v_mfma_f32_16x16x32_bf16 v[28:31], a[204:207], v[188:191], v[28:31]\n"
      "v_add_f32 v243, v243, v175\n"
      "v_mfma_f32_16x16x32_bf16 v[28:31], a[172:175], v[184:187], v[28:31]\n"
      "v_exp_f32 v160, v160\n"
      "v_mfma_f32_16x16x32_bf16 a[208:211], a[228:231], v[184:187], a[208:211]\n"
      "v_add_f32 v244, v244, v160\n"
      "v_mfma_f32_16x16x32_bf16 a[208:211], a[228:231], v[188:191], a[208:211]\n"
      "v_exp_f32 v161, v161\n"
      "v_mfma_f32_16x16x32_bf16 v[168:171], a[112:115], a[32:35], v[200:203]\n"
      "v_add_f32 v245, v245, v161\n"
      "v_cvt_pk_bf16_f32 v184, v160, v161\n"
      "v_mfma_f32_16x16x32_bf16 v[168:171], a[116:119], a[36:39], v[168:171]\n"
      "v_exp_f32 v162, v162\n"
      "v_mfma_f32_16x16x32_bf16 v[168:171], a[120:123], a[40:43], v[168:171]\n"
      "v_add_f32 v244, v244, v162\n"
      "v_mfma_f32_16x16x32_bf16 v[168:171], a[124:127], a[44:47], v[168:171]\n"
      "v_exp_f32 v163, v163\n"
      "v_mfma_f32_16x16x32_bf16 v[172:175], a[140:143], a[44:47], v[200:203]\n"
      "buffer_load_dwordx4 v241, s[40:43], s101 offen offset:3072 nt lds\n"
      "v_mfma_f32_16x16x32_bf16 v[172:175], a[136:139], a[40:43], v[172:175]\n"
      "s_add_u32 s52, s52, 0x100\n"
      "v_exp_f32 v164, v164\n"
      "v_mfma_f32_16x16x32_bf16 v[172:175], a[132:135], a[36:39], v[172:175]\n"
      "s_or_b32 s52, s52, 0x1bc00\n"
      "v_add_f32 v245, v245, v163\n"
      "v_cvt_pk_bf16_f32 v185, v162, v163\n"
      "v_mfma_f32_16x16x32_bf16 v[172:175], a[128:131], a[32:35], v[172:175]\n"
      "v_exp_f32 v165, v165\n"
      "v_mfma_f32_16x16x32_bf16 v[32:35], a[144:147], v[176:179], v[32:35]\n"
      "s_xor_b32 s101, s52, s100\n"
      "v_add_f32 v244, v244, v164\n"
      "v_cvt_pk_bf16_f32 v186, v164, v165\n"
      "v_mfma_f32_16x16x32_bf16 v[32:35], a[176:179], v[180:183], v[32:35]\n"
      "v_exp_f32 v166, v166\n"
      "v_mfma_f32_16x16x32_bf16 v[36:39], a[180:183], v[180:183], v[36:39]\n"
      "v_add_f32 v245, v245, v165\n"
      "v_add_f32 v244, v244, v166\n"
      "v_mfma_f32_16x16x32_bf16 v[36:39], a[148:151], v[176:179], v[36:39]\n"
      "v_exp_f32 v167, v167\n"
      "v_mfma_f32_16x16x32_bf16 v[40:43], a[152:155], v[176:179], v[40:43]\n"
      "v_add_f32 v245, v245, v167\n"
      "v_cvt_pk_bf16_f32 v187, v166, v167\n"
      "v_mfma_f32_16x16x32_bf16 v[40:43], a[184:187], v[180:183], v[40:43]\n"
      "v_exp_f32 v168, v168\n"
      "v_mfma_f32_16x16x32_bf16 v[44:47], a[188:191], v[180:183], v[44:47]\n"
      "v_add_f32 v244, v244, v168\n"
      "v_mfma_f32_16x16x32_bf16 v[44:47], a[156:159], v[176:179], v[44:47]\n"
      "s_waitcnt vmcnt(8) lgkmcnt(0)\n"
      "s_barrier\n"
      "v_exp_f32 v169, v169\n"
      "v_mfma_f32_16x16x32_bf16 v[160:163], a[80:83], a[48:51], v[204:207]\n"
      "ds_read_b128 a[80:83], v212 offset:49152\n"
      "v_add_f32 v245, v245, v169\n"
      "v_mfma_f32_16x16x32_bf16 v[160:163], a[84:87], a[52:55], v[160:163]\n"
      "v_exp_f32 v170, v170\n"
      "v_mfma_f32_16x16x32_bf16 v[160:163], a[88:91], a[56:59], v[160:163]\n"
      "v_add_f32 v244, v244, v170\n"
      "v_cvt_pk_bf16_f32 v188, v168, v169\n"
      "v_mfma_f32_16x16x32_bf16 v[160:163], a[92:95], a[60:63], v[160:163]\n"
      "v_exp_f32 v171, v171\n"
      "v_mfma_f32_16x16x32_bf16 v[164:167], a[108:111], a[60:63], v[204:207]\n"
      "ds_read_b128 a[108:111], v215 offset:53248\n"
      "v_add_f32 v245, v245, v171\n"
      "v_mfma_f32_16x16x32_bf16 v[164:167], a[104:107], a[56:59], v[164:167]\n"
      "v_exp_f32 v172, v172\n"
      "v_mfma_f32_16x16x32_bf16 v[164:167], a[100:103], a[52:55], v[164:167]\n"
      "ds_read_b128 a[88:91], v214 offset:49152\n"
      "v_cvt_pk_bf16_f32 v189, v170, v171\n"
      "v_mfma_f32_16x16x32_bf16 v[164:167], a[96:99], a[48:51], v[164:167]\n"
      "v_exp_f32 v173, v173\n"
      "v_mfma_f32_16x16x32_bf16 v[48:51], a[160:163], v[176:179], v[48:51]\n"
      "ds_read_b128 a[104:107], v214 offset:53248\n"
      "v_add_f32 v244, v244, v173\n"
      "v_mfma_f32_16x16x32_bf16 v[48:51], a[192:195], v[180:183], v[48:51]\n"
      "s_mov_b32 m0, s55\n"
      "v_exp_f32 v174, v174\n"
      "v_mfma_f32_16x16x32_bf16 v[52:55], a[196:199], v[180:183], v[52:55]\n"
      "buffer_load_dwordx4 v236, s[36:39], s101 offen offset:0 nt lds\n"
      "v_mfma_f32_16x16x32_bf16 v[52:55], a[164:167], v[176:179], v[52:55]\n"
      "v_exp_f32 v175, v175\n"
      "v_mfma_f32_16x16x32_bf16 v[56:59], a[168:171], v[176:179], v[56:59]\n"
      "ds_read_b128 a[92:95], v215 offset:49152\n"
      "v_add_f32 v245, v245, v172\n"
      "v_mfma_f32_16x16x32_bf16 v[56:59], a[200:203], v[180:183], v[56:59]\n"
      "v_exp_f32 v160, v160\n"
      "v_mfma_f32_16x16x32_bf16 v[60:63], a[204:207], v[180:183], v[60:63]\n"
      "ds_read_b128 a[84:87], v213 offset:49152\n"
      "v_cvt_pk_bf16_f32 v190, v172, v173\n"
      "v_mfma_f32_16x16x32_bf16 v[60:63], a[172:175], v[176:179], v[60:63]\n"
      "v_exp_f32 v161, v161\n"
      "v_mfma_f32_16x16x32_bf16 v[168:171], a[112:115], a[48:51], v[204:207]\n"
      "v_add_f32 v244, v244, v175\n"
      "v_cvt_pk_bf16_f32 v191, v174, v175\n"
      "v_mfma_f32_16x16x32_bf16 v[168:171], a[116:119], a[52:55], v[168:171]\n"
      "v_exp_f32 v162, v162\n"
      "v_mfma_f32_16x16x32_bf16 v[168:171], a[120:123], a[56:59], v[168:171]\n"
      "ds_read_b128 a[116:119], v217 offset:49152\n"
      "v_add_f32 v245, v245, v174\n"
      "v_mfma_f32_16x16x32_bf16 v[168:171], a[124:127], a[60:63], v[168:171]\n"
      "v_exp_f32 v163, v163\n"
      "v_mfma_f32_16x16x32_bf16 v[172:175], a[140:143], a[60:63], v[204:207]\n"
      "ds_read_b128 a[140:143], v219 offset:53248\n"
      "v_cvt_pk_bf16_f32 v177, v162, v163\n"
      "v_mfma_f32_16x16x32_bf16 v[172:175], a[136:139], a[56:59], v[172:175]\n"
      "v_exp_f32 v164, v164\n"
      "v_mfma_f32_16x16x32_bf16 v[172:175], a[132:135], a[52:55], v[172:175]\n"
      "ds_read_b128 a[100:103], v213 offset:53248\n"
      "v_cvt_pk_bf16_f32 v176, v160, v161\n"
      "v_mfma_f32_16x16x32_bf16 v[172:175], a[128:131], a[48:51], v[172:175]\n"
      "v_exp_f32 v165, v165\n"
      "v_mfma_f32_16x16x32_bf16 v[64:67], a[144:147], v[184:187], v[64:67]\n"
      "ds_read_b128 a[136:139], v218 offset:53248\n"
      "v_cvt_pk_bf16_f32 v178, v164, v165\n"
      "v_mfma_f32_16x16x32_bf16 v[64:67], a[176:179], v[188:191], v[64:67]\n"
      "v_exp_f32 v166, v166\n"
      "v_mfma_f32_16x16x32_bf16 v[68:71], a[180:183], v[188:191], v[68:71]\n"
      "ds_read_b128 a[96:99], v212 offset:53248\n"
      "v_mfma_f32_16x16x32_bf16 v[68:71], a[148:151], v[184:187], v[68:71]\n"
      "v_exp_f32 v167, v167\n"
      "v_mfma_f32_16x16x32_bf16 v[72:75], a[152:155], v[184:187], v[72:75]\n"
      "buffer_load_dwordx4 v237, s[36:39], s101 offen offset:1024 nt lds\n"
      "v_mfma_f32_16x16x32_bf16 v[72:75], a[184:187], v[188:191], v[72:75]\n"
      "v_exp_f32 v168, v168\n"
      "v_mfma_f32_16x16x32_bf16 v[76:79], a[188:191], v[188:191], v[76:79]\n"
      "ds_read_b128 a[132:135], v217 offset:53248\n"
      "v_cvt_pk_bf16_f32 v179, v166, v167\n"
      "v_mfma_f32_16x16x32_bf16 v[76:79], a[156:159], v[184:187], v[76:79]\n"
      "s_waitcnt lgkmcnt(11)\n"
      "v_exp_f32 v169, v169\n"
      "v_mfma_f32_16x16x32_bf16 v[160:163], a[80:83], a[64:67], v[208:211]\n"
      "s_waitcnt lgkmcnt(6)\n"
      "ds_read_b128 a[124:127], v219 offset:49152\n"
      "v_cvt_pk_bf16_f32 v180, v168, v169\n"
      "v_mfma_f32_16x16x32_bf16 v[160:163], a[84:87], a[68:71], v[160:163]\n"
      "v_exp_f32 v170, v170\n"
      "v_mfma_f32_16x16x32_bf16 v[160:163], a[88:91], a[72:75], v[160:163]\n"
      "ds_read_b128 a[128:131], v216 offset:53248\n"
      "v_mfma_f32_16x16x32_bf16 v[160:163], a[92:95], a[76:79], v[160:163]\n"
      "v_exp_f32 v171, v171\n"
      "v_mfma_f32_16x16x32_bf16 v[164:167], a[108:111], a[76:79], v[208:211]\n"
      "ds_read_b128 a[120:123], v218 offset:49152\n"
      "v_cvt_pk_bf16_f32 v181, v170, v171\n"
      "v_mfma_f32_16x16x32_bf16 v[164:167], a[104:107], a[72:75], v[164:167]\n"
      "s_waitcnt lgkmcnt(6)\n"
      "v_exp_f32 v172, v172\n"
      "v_mfma_f32_16x16x32_bf16 v[164:167], a[100:103], a[68:71], v[164:167]\n"
      "s_waitcnt lgkmcnt(4)\n"
      "ds_read_b128 a[112:115], v216 offset:49152\n"
      "v_mfma_f32_16x16x32_bf16 v[164:167], a[96:99], a[64:67], v[164:167]\n"
      "v_exp_f32 v173, v173\n"
      "v_mfma_f32_16x16x32_bf16 v[80:83], a[160:163], v[184:187], v[80:83]\n"
      "v_cvt_pk_bf16_f32 v182, v172, v173\n"
      "v_mfma_f32_16x16x32_bf16 v[80:83], a[192:195], v[188:191], v[80:83]\n"
      "v_exp_f32 v174, v174\n"
      "v_mfma_f32_16x16x32_bf16 v[84:87], a[196:199], v[188:191], v[84:87]\n"
      "L_ev2%=:\n"
      "s_nop 0\n"
      "v_mfma_f32_16x16x32_bf16 v[84:87], a[164:167], v[184:187], v[84:87]\n"
      "v_exp_f32 v175, v175\n"
      "v_mfma_f32_16x16x32_bf16 v[88:91], a[168:171], v[184:187], v[88:91]\n"
      "v_cvt_pk_bf16_f32 v183, v174, v175\n"
      "v_mfma_f32_16x16x32_bf16 v[88:91], a[200:203], v[188:191], v[88:91]\n"
      "v_exp_f32 v160, v160\n"
      "v_mfma_f32_16x16x32_bf16 v[92:95], a[204:207], v[188:191], v[92:95]\n"
      "buffer_load_dwordx4 v238, s[36:39], s101 offen offset:2048 nt lds\n"
      "v_mfma_f32_16x16x32_bf16 v[92:95], a[172:175], v[184:187], v[92:95]\n"
      "v_exp_f32 v161, v161\n"
      "v_mfma_f32_16x16x32_bf16 v[168:171], a[80:83], a[0:3], v[192:195]\n"
      "v_add_f32 v246, v246, v161\n"
      "v_cvt_pk_bf16_f32 v184, v160, v161\n"
      "v_mfma_f32_16x16x32_bf16 v[168:171], a[84:87], a[4:7], v[168:171]\n"
      "v_exp_f32 v162, v162\n"
      "v_mfma_f32_16x16x32_bf16 v[168:171], a[88:91], a[8:11], v[168:171]\n"
      "v_add_f32 v247, v247, v160\n"
      "v_add_f32 v246, v246, v162\n"
      "v_mfma_f32_16x16x32_bf16 v[168:171], a[92:95], a[12:15], v[168:171]\n"
      "v_exp_f32 v163, v163\n"
      "v_mfma_f32_16x16x32_bf16 v[172:175], a[108:111], a[12:15], v[192:195]\n"
      "v_add_f32 v247, v247, v163\n"
      "v_cvt_pk_bf16_f32 v185, v162, v163\n"
      "v_mfma_f32_16x16x32_bf16 v[172:175], a[104:107], a[8:11], v[172:175]\n"
      "v_exp_f32 v164, v164\n"
      "v_mfma_f32_16x16x32_bf16 v[172:175], a[100:103], a[4:7], v[172:175]\n"
      "v_add_f32 v246, v246, v164\n"
      "v_mfma_f32_16x16x32_bf16 v[172:175], a[96:99], a[0:3], v[172:175]\n"
      "v_exp_f32 v165, v165\n"
      "v_mfma_f32_16x16x32_bf16 v[96:99], a[144:147], v[176:179], v[96:99]\n"
      "v_add_f32 v247, v247, v165\n"
      "v_cvt_pk_bf16_f32 v186, v164, v165\n"
      "v_mfma_f32_16x16x32_bf16 v[96:99], a[176:179], v[180:183], v[96:99]\n"
      "v_exp_f32 v166, v166\n"
      "v_mfma_f32_16x16x32_bf16 v[100:103], a[180:183], v[180:183], v[100:103]\n"
      "ds_read_b64_tr_b16 a[146:147], v220 offset:53248\n"
      "ds_read_b64_tr_b16 a[182:183], v229 offset:53248\n"
      "v_mfma_f32_16x16x32_bf16 v[100:103], a[148:151], v[176:179], v[100:103]\n"
      "v_exp_f32 v167, v167\n"
      "v_mfma_f32_16x16x32_bf16 v[104:107], a[152:155], v[176:179], v[104:107]\n"
      "ds_read_b64_tr_b16 a[150:151], v221 offset:53248\n"
      "v_add_f32 v246, v246, v167\n"
      "v_mfma_f32_16x16x32_bf16 v[104:107], a[184:187], v[180:183], v[104:107]\n"
      "v_exp_f32 v168, v168\n"
      "v_mfma_f32_16x16x32_bf16 v[108:111], a[188:191], v[180:183], v[108:111]\n"
      "ds_read_b64_tr_b16 a[144:145], v220 offset:49152\n"
      "v_cvt_pk_bf16_f32 v187, v166, v167\n"
      "v_mfma_f32_16x16x32_bf16 v[108:111], a[156:159], v[176:179], v[108:111]\n"
      "s_waitcnt lgkmcnt(4)\n"
      "v_exp_f32 v169, v169\n"
      "v_mfma_f32_16x16x32_bf16 v[160:163], a[112:115], a[0:3], v[192:195]\n"
      "buffer_load_dwordx4 v239, s[36:39], s101 offen offset:3072 nt lds\n"
      "v_mfma_f32_16x16x32_bf16 v[160:163], a[116:119], a[4:7], v[160:163]\n"
      "v_exp_f32 v170, v170\n"
      "v_mfma_f32_16x16x32_bf16 v[160:163], a[120:123], a[8:11], v[160:163]\n"
      "v_add_f32 v247, v247, v166\n"
      "v_cvt_pk_bf16_f32 v188, v168, v169\n"
      "v_mfma_f32_16x16x32_bf16 v[160:163], a[124:127], a[12:15], v[160:163]\n"
      "v_exp_f32 v171, v171\n"
      "v_mfma_f32_16x16x32_bf16 v[164:167], a[140:143], a[12:15], v[192:195]\n"
      "ds_read_b64_tr_b16 a[148:149], v221 offset:49152\n"
      "ds_read_b64_tr_b16 a[154:155], v222 offset:53248\n"
      "v_mfma_f32_16x16x32_bf16 v[164:167], a[136:139], a[8:11], v[164:167]\n"
      "v_exp_f32 v172, v172\n"
      "v_mfma_f32_16x16x32_bf16 v[164:167], a[132:135], a[4:7], v[164:167]\n"
      "ds_read_b64_tr_b16 a[156:157], v223 offset:49152\n"
      "ds_read_b64_tr_b16 a[158:159], v223 offset:53248\n"
      "v_mfma_f32_16x16x32_bf16 v[164:167], a[128:131], a[0:3], v[164:167]\n"
      "v_exp_f32 v173, v173\n"
      "v_mfma_f32_16x16x32_bf16 v[112:115], a[160:163], v[176:179], v[112:115]\n"
      "v_cvt_pk_bf16_f32 v189, v170, v171\n"
      "v_cvt_pk_bf16_f32 v190, v172, v173\n"
      "v_mfma_f32_16x16x32_bf16 v[112:115], a[192:195], v[180:183], v[112:115]\n"
      "v_exp_f32 v174, v174\n"
      "v_mfma_f32_16x16x32_bf16 v[116:119], a[196:199], v[180:183], v[116:119]\n"
      "ds_read_b64_tr_b16 a[152:153], v222 offset:49152\n"
      "ds_read_b64_tr_b16 a[160:161], v224 offset:49152\n"
      "v_mfma_f32_16x16x32_bf16 v[116:119], a[164:167], v[176:179], v[116:119]\n"
      "v_exp_f32 v175, v175\n"
      "v_mfma_f32_16x16x32_bf16 v[120:123], a[168:171], v[176:179], v[120:123]\n"
      "ds_read_b64_tr_b16 a[162:163], v224 offset:53248\n"
      "ds_read_b64_tr_b16 a[170:171], v226 offset:53248\n"
      "v_mfma_f32_16x16x32_bf16 v[120:123], a[200:203], v[180:183], v[120:123]\n"
      "ds_read_b64_tr_b16 a[196:197], v233 offset:49152\n"
      "ds_read_b64_tr_b16 a[198:199], v233 offset:53248\n"
      "v_mfma_f32_16x16x32_bf16 v[124:127], a[204:207], v[180:183], v[124:127]\n"
      "ds_read_b64_tr_b16 a[204:205], v235 offset:49152\n"
      "ds_read_b64_tr_b16 a[206:207], v235 offset:53248\n"
      "v_mfma_f32_16x16x32_bf16 v[124:127], a[172:175], v[176:179], v[124:127]\n"
      "v_exp_f32 v160, v160\n"
      "v_mfma_f32_16x16x32_bf16 a[220:223], a[228:231], v[176:179], a[220:223]\n"
      "ds_read_b64_tr_b16 a[166:167], v225 offset:53248\n"
      "ds_read_b64_tr_b16 a[168:169], v226 offset:49152\n"
      "v_mfma_f32_16x16x32_bf16 a[220:223], a[228:231], v[180:183], a[220:223]\n"
      "s_mov_b32 m0, s59\n"
      "v_exp_f32 v161, v161\n"
      "v_mfma_f32_16x16x32_bf16 v[168:171], a[80:83], a[16:19], v[196:199]\n"
      "buffer_load_dwordx4 v240, s[40:43], s101 offen offset:0 nt lds\n"
      "v_mfma_f32_16x16x32_bf16 v[168:171], a[84:87], a[20:23], v[168:171]\n"
      "v_exp_f32 v162, v162\n"
      "v_mfma_f32_16x16x32_bf16 v[168:171], a[88:91], a[24:27], v[168:171]\n"
      "ds_read_b64_tr_b16 a[164:165], v225 offset:49152\n"
      "v_cvt_pk_bf16_f32 v191, v174, v175\n"
      "v_mfma_f32_16x16x32_bf16 v[168:171], a[92:95], a[28:31], v[168:171]\n"
      "v_exp_f32 v163, v163\n"
      "v_mfma_f32_16x16x32_bf16 v[172:175], a[108:111], a[28:31], v[196:199]\n"
      "ds_read_b64_tr_b16 a[172:173], v227 offset:49152\n"
      "ds_read_b64_tr_b16 a[174:175], v227 offset:53248\n"
      "v_mfma_f32_16x16x32_bf16 v[172:175], a[104:107], a[24:27], v[172:175]\n"
      "v_exp_f32 v164, v164\n"
      "v_mfma_f32_16x16x32_bf16 v[172:175], a[100:103], a[20:23], v[172:175]\n"
      "ds_read_b64_tr_b16 a[200:201], v234 offset:49152\n"
      "ds_read_b64_tr_b16 a[202:203], v234 offset:53248\n"
      "v_mfma_f32_16x16x32_bf16 v[172:175], a[96:99], a[16:19], v[172:175]\n"
      "s_waitcnt lgkmcnt(15)\n"
      "v_exp_f32 v165, v165\n"
      "v_mfma_f32_16x16x32_bf16 v[128:131], a[144:147], v[184:187], v[128:131]\n"
      "s_waitcnt lgkmcnt(15)\n"
      "ds_read_b64_tr_b16 a[186:187], v230 offset:53248\n"
      "ds_read_b64_tr_b16 a[194:195], v232 offset:53248\n"
      "v_mfma_f32_16x16x32_bf16 v[132:135], a[148:151], v[184:187], v[132:135]\n"
      "s_waitcnt lgkmcnt(15)\n"
      "v_exp_f32 v166, v166\n"
      "v_mfma_f32_16x16x32_bf16 v[136:139], a[152:155], v[184:187], v[136:139]\n"
      "ds_read_b64_tr_b16 a[176:177], v228 offset:49152\n"
      "ds_read_b64_tr_b16 a[184:185], v230 offset:49152\n"
      "v_mfma_f32_16x16x32_bf16 v[140:143], a[156:159], v[184:187], v[140:143]\n"
      "s_waitcnt lgkmcnt(15)\n"
      "v_exp_f32 v167, v167\n"
      "v_mfma_f32_16x16x32_bf16 v[144:147], a[160:163], v[184:187], v[144:147]\n"
      "s_waitcnt lgkmcnt(8)\n"
      "ds_read_b64_tr_b16 a[178:179], v228 offset:53248\n"
      "ds_read_b64_tr_b16 a[180:181], v229 offset:49152\n"
      "v_mfma_f32_16x16x32_bf16 v[148:151], a[164:167], v[184:187], v[148:151]\n"
      "v_exp_f32 v168, v168\n"
      "v_mfma_f32_16x16x32_bf16 v[152:155], a[168:171], v[184:187], v[152:155]\n"
      "s_waitcnt lgkmcnt(8)\n"
      "v_cvt_pk_bf16_f32 v176, v160, v161\n"
      "v_cvt_pk_bf16_f32 v177, v162, v163\n"
      "v_mfma_f32_16x16x32_bf16 v[156:159], a[172:175], v[184:187], v[156:159]\n"
      "v_exp_f32 v169, v169\n"
      "v_mfma_f32_16x16x32_bf16 v[160:163], a[112:115], a[16:19], v[196:199]\n"
      "v_cvt_pk_bf16_f32 v178, v164, v165\n"
      "v_cvt_pk_bf16_f32 v179, v166, v167\n"
      "v_mfma_f32_16x16x32_bf16 v[160:163], a[116:119], a[20:23], v[160:163]\n"
      "v_exp_f32 v170, v170\n"
      "v_mfma_f32_16x16x32_bf16 v[160:163], a[120:123], a[24:27], v[160:163]\n"
      "buffer_load_dwordx4 v241, s[40:43], s101 offen offset:1024 nt lds\n"
      "v_mfma_f32_16x16x32_bf16 v[160:163], a[124:127], a[28:31], v[160:163]\n"
      "v_exp_f32 v171, v171\n"
      "v_mfma_f32_16x16x32_bf16 v[164:167], a[140:143], a[28:31], v[196:199]\n"
      "ds_read_b64_tr_b16 a[188:189], v231 offset:49152\n"
      "ds_read_b64_tr_b16 a[190:191], v231 offset:53248\n"
      "v_mfma_f32_16x16x32_bf16 v[164:167], a[136:139], a[24:27], v[164:167]\n"
      "v_exp_f32 v172, v172\n"
      "v_mfma_f32_16x16x32_bf16 v[164:167], a[132:135], a[20:23], v[164:167]\n"
      "ds_read_b64_tr_b16 a[192:193], v232 offset:49152\n"
      "v_add_f32 v242, v242, v168\n"
      "v_mfma_f32_16x16x32_bf16 v[164:167], a[128:131], a[16:19], v[164:167]\n"
      "v_exp_f32 v173, v173\n"
      "v_mfma_f32_16x16x32_bf16 v[0:3], a[144:147], v[188:191], v[0:3]\n"
      "s_waitcnt lgkmcnt(4)\n"
      "v_add_f32 v243, v243, v169\n"
      "v_cvt_pk_bf16_f32 v180, v168, v169\n"
      "v_mfma_f32_16x16x32_bf16 v[0:3], a[176:179], v[176:179], v[0:3]\n"
      "s_waitcnt lgkmcnt(3)\n"
      "v_exp_f32 v174, v174\n"
      "v_mfma_f32_16x16x32_bf16 v[4:7], a[180:183], v[176:179], v[4:7]\n"
      "v_add_f32 v242, v242, v171\n"
      "v_cvt_pk_bf16_f32 v181, v170, v171\n"
      "v_mfma_f32_16x16x32_bf16 v[4:7], a[148:151], v[188:191], v[4:7]\n"
      "v_exp_f32 v175, v175\n"
      "v_mfma_f32_16x16x32_bf16 v[8:11], a[152:155], v[188:191], v[8:11]\n"
      "v_add_f32 v243, v243, v170\n"
      "v_add_f32 v242, v242, v172\n"
      "v_mfma_f32_16x16x32_bf16 v[8:11], a[184:187], v[176:179], v[8:11]\n"
      "s_waitcnt lgkmcnt(1)\n"
      "v_exp_f32 v160, v160\n"
      "v_mfma_f32_16x16x32_bf16 v[12:15], a[188:191], v[176:179], v[12:15]\n"
      "v_add_f32 v243, v243, v173\n"
      "v_cvt_pk_bf16_f32 v182, v172, v173\n"
      "v_mfma_f32_16x16x32_bf16 v[12:15], a[156:159], v[188:191], v[12:15]\n"
      "v_exp_f32 v161, v161\n"
      "v_mfma_f32_16x16x32_bf16 v[168:171], a[80:83], a[32:35], v[200:203]\n"
      "v_add_f32 v242, v242, v174\n"
      "v_cvt_pk_bf16_f32 v183, v174, v175\n"
      "v_mfma_f32_16x16x32_bf16 v[168:171], a[84:87], a[36:39], v[168:171]\n"
      "v_exp_f32 v162, v162\n"
      "v_mfma_f32_16x16x32_bf16 v[168:171], a[88:91], a[40:43], v[168:171]\n"
      "v_add_f32 v243, v243, v175\n"
      "v_add_f32 v242, v242, v160\n"
      "v_mfma_f32_16x16x32_bf16 v[168:171], a[92:95], a[44:47], v[168:171]\n"
      "v_exp_f32 v163, v163\n"
      "v_mfma_f32_16x16x32_bf16 v[172:175], a[108:111], a[44:47], v[200:203]\n"
      "buffer_load_dwordx4 v240, s[40:43], s101 offen offset:2048 nt lds\n"
      "v_mfma_f32_16x16x32_bf16 v[172:175], a[104:107], a[40:43], v[172:175]\n"
      "v_exp_f32 v164, v164\n"
      "v_mfma_f32_16x16x32_bf16 v[172:175], a[100:103], a[36:39], v[172:175]\n"
      "v_add_f32 v243, v243, v161\n"
      "v_cvt_pk_bf16_f32 v184, v160, v161\n"
      "v_mfma_f32_16x16x32_bf16 v[172:175], a[96:99], a[32:35], v[172:175]\n"
      "v_exp_f32 v165, v165\n"
      "v_mfma_f32_16x16x32_bf16 v[16:19], a[160:163], v[188:191], v[16:19]\n"
      "s_waitcnt lgkmcnt(0)\n"
      "v_add_f32 v242, v242, v162\n"
      "v_add_f32 v243, v243, v163\n"
      "v_mfma_f32_16x16x32_bf16 v[16:19], a[192:195], v[176:179], v[16:19]\n"
      "v_exp_f32 v166, v166\n"
      "v_mfma_f32_16x16x32_bf16 v[20:23], a[196:199], v[176:179], v[20:23]\n"
      "v_cvt_pk_bf16_f32 v185, v162, v163\n"
      "v_cvt_pk_bf16_f32 v186, v164, v165\n"
      "v_mfma_f32_16x16x32_bf16 v[20:23], a[164:167], v[188:191], v[20:23]\n"
      "v_exp_f32 v167, v167\n"
      "v_mfma_f32_16x16x32_bf16 v[24:27], a[168:171], v[188:191], v[24:27]\n"
      "v_add_f32 v242, v242, v164\n"
      "v_add_f32 v243, v243, v165\n"
      "v_mfma_f32_16x16x32_bf16 v[24:27], a[200:203], v[176:179], v[24:27]\n"
      "v_add_f32 v242, v242, v166\n"
      "v_cvt_pk_bf16_f32 v187, v166, v167\n"
      "v_mfma_f32_16x16x32_bf16 v[28:31], a[204:207], v[176:179], v[28:31]\n"
      "v_add_f32 v243, v243, v167\n"
      "v_mfma_f32_16x16x32_bf16 v[28:31], a[172:175], v[188:191], v[28:31]\n"
      "v_exp_f32 v168, v168\n"
      "v_mfma_f32_16x16x32_bf16 a[208:211], a[228:231], v[188:191], a[208:211]\n"
      "v_add_f32 v244, v244, v168\n"
      "v_mfma_f32_16x16x32_bf16 a[208:211], a[228:231], v[176:179], a[208:211]\n"
      "v_exp_f32 v169, v169\n"
      "v_mfma_f32_16x16x32_bf16 v[160:163], a[112:115], a[32:35], v[200:203]\n"
      "v_add_f32 v245, v245, v169\n"
      "v_cvt_pk_bf16_f32 v188, v168, v169\n"
      "v_mfma_f32_16x16x32_bf16 v[160:163], a[116:119], a[36:39], v[160:163]\n"
      "v_exp_f32 v170, v170\n"
      "v_mfma_f32_16x16x32_bf16 v[160:163], a[120:123], a[40:43], v[160:163]\n"
      "v_add_f32 v244, v244, v170\n"
      "v_mfma_f32_16x16x32_bf16 v[160:163], a[124:127], a[44:47], v[160:163]\n"
      "v_exp_f32 v171, v171\n"
      "v_mfma_f32_16x16x32_bf16 v[164:167], a[140:143], a[44:47], v[200:203]\n"
      "buffer_load_dwordx4 v241, s[40:43], s101 offen offset:3072 nt lds\n"
      "v_mfma_f32_16x16x32_bf16 v[164:167], a[136:139], a[40:43], v[164:167]\n"
      "s_add_u32 s52, s52, 0x100\n"
      "v_exp_f32 v172, v172\n"
      "v_mfma_f32_16x16x32_bf16 v[164:167], a[132:135], a[36:39], v[164:167]\n"
      "s_or_b32 s52, s52, 0x1bc00\n"
      "v_add_f32 v245, v245, v171\n"
      "v_cvt_pk_bf16_f32 v189, v170, v171\n"
      "v_mfma_f32_16x16x32_bf16 v[164:167], a[128:131], a[32:35], v[164:167]\n"
      "v_exp_f32 v173, v173\n"
      "v_mfma_f32_16x16x32_bf16 v[32:35], a[144:147], v[180:183], v[32:35]\n"
      "s_xor_b32 s101, s52, s100\n"
      "v_add_f32 v244, v244, v172\n"
      "v_cvt_pk_bf16_f32 v190, v172, v173\n"
      "v_mfma_f32_16x16x32_bf16 v[32:35], a[176:179], v[184:187], v[32:35]\n"
      "v_exp_f32 v174, v174\n"
      "v_mfma_f32_16x16x32_bf16 v[36:39], a[180:183], v[184:187], v[36:39]\n"
      "v_add_f32 v245, v245, v173\n"
      "v_add_f32 v244, v244, v174\n"
      "v_mfma_f32_16x16x32_bf16 v[36:39], a[148:151], v[180:183], v[36:39]\n"
      "v_exp_f32 v175, v175\n"
      "v_mfma_f32_16x16x32_bf16 v[40:43], a[152:155], v[180:183], v[40:43]\n"
      "v_add_f32 v245, v245, v175\n"
      "v_cvt_pk_bf16_f32 v191, v174, v175\n"
      "v_mfma_f32_16x16x32_bf16 v[40:43], a[184:187], v[184:187], v[40:43]\n"
      "v_exp_f32 v160, v160\n"
      "v_mfma_f32_16x16x32_bf16 v[44:47], a[188:191], v[184:187], v[44:47]\n"
      "v_add_f32 v244, v244, v160\n"
      "v_mfma_f32_16x16x32_bf16 v[44:47], a[156:159], v[180:183], v[44:47]\n"
      "s_waitcnt vmcnt(8) lgkmcnt(0)\n"
      "s_barrier\n"
      "v_exp_f32 v161, v161\n"
      "v_mfma_f32_16x16x32_bf16 v[168:171], a[80:83], a[48:51], v[204:207]\n"
      "ds_read_b128 a[80:83], v212 offset:0\n"
      "v_add_f32 v245, v245, v161\n"
      "v_mfma_f32_16x16x32_bf16 v[168:171], a[84:87], a[52:55], v[168:171]\n"
      "v_exp_f32 v162, v162\n"
      "v_mfma_f32_16x16x32_bf16 v[168:171], a[88:91], a[56:59], v[168:171]\n"
      "v_add_f32 v244, v244, v162\n"
      "v_cvt_pk_bf16_f32 v176, v160, v161\n"
      "v_mfma_f32_16x16x32_bf16 v[168:171], a[92:95], a[60:63], v[168:171]\n"
      "v_exp_f32 v163, v163\n"
      "v_mfma_f32_16x16x32_bf16 v[172:175], a[108:111], a[60:63], v[204:207]\n"
      "ds_read_b128 a[108:111], v215 offset:4096\n"
      "v_add_f32 v245, v245, v163\n"
      "v_mfma_f32_16x16x32_bf16 v[172:175], a[104:107], a[56:59], v[172:175]\n"
      "v_exp_f32 v164, v164\n"
      "v_mfma_f32_16x16x32_bf16 v[172:175], a[100:103], a[52:55], v[172:175]\n"
      "ds_read_b128 a[88:91], v214 offset:0\n"
      "v_cvt_pk_bf16_f32 v177, v162, v163\n"
      "v_mfma_f32_16x16x32_bf16 v[172:175], a[96:99], a[48:51], v[172:175]\n"
      "v_exp_f32 v165, v165\n"
      "v_mfma_f32_16x16x32_bf16 v[48:51], a[160:163], v[180:183], v[48:51]\n"
      "ds_read_b128 a[104:107], v214 offset:4096\n"
      "v_add_f32 v244, v244, v165\n"
      "v_mfma_f32_16x16x32_bf16 v[48:51], a[192:195], v[184:187], v[48:51]\n"
      "s_mov_b32 m0, s56\n"
      "v_exp_f32 v166, v166\n"
      "v_mfma_f32_16x16x32_bf16 v[52:55], a[196:199], v[184:187], v[52:55]\n"
      "buffer_load_dwordx4 v236, s[36:39], s101 offen offset:0 nt lds\n"
      "v_mfma_f32_16x16x32_bf16 v[52:55], a[164:167], v[180:183], v[52:55]\n"
      "v_exp_f32 v167, v167\n"
      "v_mfma_f32_16x16x32_bf16 v[56:59], a[168:171], v[180:183], v[56:59]\n"
      "ds_read_b128 a[92:95], v215 offset:0\n"
      "v_add_f32 v245, v245, v164\n"
      "v_mfma_f32_16x16x32_bf16 v[56:59], a[200:203], v[184:187], v[56:59]\n"
      "v_exp_f32 v168, v168\n"
      "v_mfma_f32_16x16x32_bf16 v[60:63], a[204:207], v[184:187], v[60:63]\n"
      "ds_read_b128 a[84:87], v213 offset:0\n"
      "v_cvt_pk_bf16_f32 v178, v164, v165\n"
      "v_mfma_f32_16x16x32_bf16 v[60:63], a[172:175], v[180:183], v[60:63]\n"
      "v_exp_f32 v169, v169\n"
      "v_mfma_f32_16x16x32_bf16 v[160:163], a[112:115], a[48:51], v[204:207]\n"
      "v_add_f32 v244, v244, v167\n"
      "v_cvt_pk_bf16_f32 v179, v166, v167\n"
      "v_mfma_f32_16x16x32_bf16 v[160:163], a[116:119], a[52:55], v[160:163]\n"
      "v_exp_f32 v170, v170\n"
      "v_mfma_f32_16x16x32_bf16 v[160:163], a[120:123], a[56:59], v[160:163]\n"
      "ds_read_b128 a[116:119], v217 offset:0\n"
      "v_add_f32 v245, v245, v166\n"
      "v_mfma_f32_16x16x32_bf16 v[160:163], a[124:127], a[60:63], v[160:163]\n"
      "v_exp_f32 v171, v171\n"
      "v_mfma_f32_16x16x32_bf16 v[164:167], a[140:143], a[60:63], v[204:207]\n"
      "ds_read_b128 a[140:143], v219 offset:4096\n"
      "v_cvt_pk_bf16_f32 v181, v170, v171\n"
      "v_mfma_f32_16x16x32_bf16 v[164:167], a[136:139], a[56:59], v[164:167]\n"
      "v_exp_f32 v172, v172\n"
      "v_mfma_f32_16x16x32_bf16 v[164:167], a[132:135], a[52:55], v[164:167]\n"
      "ds_read_b128 a[100:103], v213 offset:4096\n"
      "v_cvt_pk_bf16_f32 v180, v168, v169\n"
      "v_mfma_f32_16x16x32_bf16 v[164:167], a[128:131], a[48:51], v[164:167]\n"
      "v_exp_f32 v173, v173\n"
      "v_mfma_f32_16x16x32_bf16 v[64:67], a[144:147], v[188:191], v[64:67]\n"
      "ds_read_b128 a[136:139], v218 offset:4096\n"
      "v_cvt_pk_bf16_f32 v182, v172, v173\n"
      "v_mfma_f32_16x16x32_bf16 v[64:67], a[176:179], v[176:179], v[64:67]\n"
      "v_exp_f32 v174, v174\n"
      "v_mfma_f32_16x16x32_bf16 v[68:71], a[180:183], v[176:179], v[68:71]\n"
      "ds_read_b128 a[96:99], v212 offset:4096\n"
      "v_mfma_f32_16x16x32_bf16 v[68:71], a[148:151], v[188:191], v[68:71]\n"
      "v_exp_f32 v175, v175\n"
      "v_mfma_f32_16x16x32_bf16 v[72:75], a[152:155], v[188:191], v[72:75]\n"
      "buffer_load_dwordx4 v237, s[36:39], s101 offen offset:1024 nt lds\n"
      "v_mfma_f32_16x16x32_bf16 v[72:75], a[184:187], v[176:179], v[72:75]\n"
      "v_exp_f32 v160, v160\n"
      "v_mfma_f32_16x16x32_bf16 v[76:79], a[188:191], v[176:179], v[76:79]\n"
      "s_cmp_lt_u32 s52, 0x9bd00\n"
      "ds_read_b128 a[132:135], v217 offset:4096\n"
      "v_cvt_pk_bf16_f32 v183, v174, v175\n"
      "v_mfma_f32_16x16x32_bf16 v[76:79], a[156:159], v[188:191], v[76:79]\n"
      "s_waitcnt lgkmcnt(11)\n"
      "v_exp_f32 v161, v161\n"
      "s_cbranch_scc1 L_eloop%=\n"
      "s_branch L_epi%=\n"
      ".p2align 6\n"
      "L_loop%=:\n"
      "L_ent3%=:\n"
      "v_mfma_f32_16x16x32_bf16 v[168:171], a[80:83], a[64:67], v[208:211]\n"
      "s_waitcnt lgkmcnt(6)\n"
      "ds_read_b128 a[124:127], v219 offset:0\n"
      "v_cvt_pk_bf16_f32 v184, v160, v161\n"
      "v_mfma_f32_16x16x32_bf16 v[168:171], a[84:87], a[68:71], v[168:171]\n"
      "v_exp_f32 v162, v162\n"
      "v_mfma_f32_16x16x32_bf16 v[168:171], a[88:91], a[72:75], v[168:171]\n"
      "ds_read_b128 a[128:131], v216 offset:4096\n"
      "v_mfma_f32_16x16x32_bf16 v[168:171], a[92:95], a[76:79], v[168:171]\n"
      "v_exp_f32 v163, v163\n"
      "v_mfma_f32_16x16x32_bf16 v[172:175], a[108:111], a[76:79], v[208:211]\n"
      "ds_read_b128 a[120:123], v218 offset:0\n"
      "v_cvt_pk_bf16_f32 v185, v162, v163\n"
      "v_mfma_f32_16x16x32_bf16 v[172:175], a[104:107], a[72:75], v[172:175]\n"
      "s_waitcnt lgkmcnt(6)\n"
      "v_exp_f32 v164, v164\n"
      "v_mfma_f32_16x16x32_bf16 v[172:175], a[100:103], a[68:71], v[172:175]\n"
      "s_waitcnt lgkmcnt(4)\n"
      "ds_read_b128 a[112:115], v216 offset:0\n"
      "v_mfma_f32_16x16x32_bf16 v[172:175], a[96:99], a[64:67], v[172:175]\n"
      "v_exp_f32 v165, v165\n"
      "v_mfma_f32_16x16x32_bf16 v[80:83], a[160:163], v[188:191], v[80:83]\n"
      "v_cvt_pk_bf16_f32 v186, v164, v165\n"
      "v_mfma_f32_16x16x32_bf16 v[80:83], a[192:195], v[176:179], v[80:83]\n"
      "v_exp_f32 v166, v166\n"
      "v_mfma_f32_16x16x32_bf16 v[84:87], a[196:199], v[176:179], v[84:87]\n"
      "s_cmp_gt_u32 s69, 5\n"
      "s_cbranch_scc1 L_ev3%=\n"
      "v_mfma_f32_16x16x32_bf16 v[84:87], a[164:167], v[188:191], v[84:87]\n"
      "v_exp_f32 v167, v167\n"
      "v_mfma_f32_16x16x32_bf16 v[88:91], a[168:171], v[188:191], v[88:91]\n"
      "v_cvt_pk_bf16_f32 v187, v166, v167\n"
      "v_mfma_f32_16x16x32_bf16 v[88:91], a[200:203], v[176:179], v[88:91]\n"
      "v_exp_f32 v168, v168\n"
      "v_mfma_f32_16x16x32_bf16 v[92:95], a[204:207], v[176:179], v[92:95]\n"
      "buffer_load_dwordx4 v238, s[36:39], s101 offen offset:2048 lds\n"
      "v_mfma_f32_16x16x32_bf16 v[92:95], a[172:175], v[188:191], v[92:95]\n"
      "v_exp_f32 v169, v169\n"
      "v_mfma_f32_16x16x32_bf16 v[160:163], a[80:83], a[0:3], v[192:195]\n"
      "v_add_f32 v246, v246, v169\n"
      "v_cvt_pk_bf16_f32 v188, v168, v169\n"
      "v_mfma_f32_16x16x32_bf16 v[160:163], a[84:87], a[4:7], v[160:163]\n"
      "v_exp_f32 v170, v170\n"
      "v_mfma_f32_16x16x32_bf16 v[160:163], a[88:91], a[8:11], v[160:163]\n"
      "v_add_f32 v247, v247, v168\n"
      "v_add_f32 v246, v246, v170\n"
      "v_mfma_f32_16x16x32_bf16 v[160:163], a[92:95], a[12:15], v[160:163]\n"
      "v_exp_f32 v171, v171\n"
      "v_mfma_f32_16x16x32_bf16 v[164:167], a[108:111], a[12:15], v[192:195]\n"
      "v_add_f32 v247, v247, v171\n"
      "v_cvt_pk_bf16_f32 v189, v170, v171\n"
      "v_mfma_f32_16x16x32_bf16 v[164:167], a[104:107], a[8:11], v[164:167]\n"
      "v_exp_f32 v172, v172\n"
      "v_mfma_f32_16x16x32_bf16 v[164:167], a[100:103], a[4:7], v[164:167]\n"
      "v_add_f32 v246, v246, v172\n"
      "v_mfma_f32_16x16x32_bf16 v[164:167], a[96:99], a[0:3], v[164:167]\n"
      "v_exp_f32 v173, v173\n"
      "v_mfma_f32_16x16x32_bf16 v[96:99], a[144:147], v[180:183], v[96:99]\n"
      "v_add_f32 v247, v247, v173\n"
      "v_cvt_pk_bf16_f32 v190, v172, v173\n"
      "v_mfma_f32_16x16x32_bf16 v[96:99], a[176:179], v[184:187], v[96:99]\n"
      "v_exp_f32 v174, v174\n"
      "v_mfma_f32_16x16x32_bf16 v[100:103], a[180:183], v[184:187], v[100:103]\n"
      "ds_read_b64_tr_b16 a[146:147], v220 offset:4096\n"
      "ds_read_b64_tr_b16 a[182:183], v229 offset:4096\n"
      "v_mfma_f32_16x16x32_bf16 v[100:103], a[148:151], v[180:183], v[100:103]\n"
      "v_exp_f32 v175, v175\n"
      "v_mfma_f32_16x16x32_bf16 v[104:107], a[152:155], v[180:183], v[104:107]\n"
      "ds_read_b64_tr_b16 a[150:151], v221 offset:4096\n"
      "v_add_f32 v246, v246, v175\n"
      "v_mfma_f32_16x16x32_bf16 v[104:107], a[184:187], v[184:187], v[104:107]\n"
      "v_exp_f32 v160, v160\n"
      "v_mfma_f32_16x16x32_bf16 v[108:111], a[188:191], v[184:187], v[108:111]\n"
      "ds_read_b64_tr_b16 a[144:145], v220 offset:0\n"
      "v_cvt_pk_bf16_f32 v191, v174, v175\n"
      "v_mfma_f32_16x16x32_bf16 v[108:111], a[156:159], v[180:183], v[108:111]\n"
      "s_waitcnt lgkmcnt(4)\n"
      "v_exp_f32 v161, v161\n"
      "v_mfma_f32_16x16x32_bf16 v[168:171], a[112:115], a[0:3], v[192:195]\n"
      "buffer_load_dwordx4 v239, s[36:39], s101 offen offset:3072 lds\n"
      "v_mfma_f32_16x16x32_bf16 v[168:171], a[116:119], a[4:7], v[168:171]\n"
      "v_exp_f32 v162, v162\n"
      "v_mfma_f32_16x16x32_bf16 v[168:171], a[120:123], a[8:11], v[168:171]\n"
      "v_add_f32 v247, v247, v174\n"
      "v_cvt_pk_bf16_f32 v176, v160, v161\n"
      "v_mfma_f32_16x16x32_bf16 v[168:171], a[124:127], a[12:15], v[168:171]\n"
      "v_exp_f32 v163, v163\n"
      "v_mfma_f32_16x16x32_bf16 v[172:175], a[140:143], a[12:15], v[192:195]\n"
      "ds_read_b64_tr_b16 a[148:149], v221 offset:0\n"
      "ds_read_b64_tr_b16 a[154:155], v222 offset:4096\n"
      "v_mfma_f32_16x16x32_bf16 v[172:175], a[136:139], a[8:11], v[172:175]\n"
      "v_exp_f32 v164, v164\n"
      "v_mfma_f32_16x16x32_bf16 v[172:175], a[132:135], a[4:7], v[172:175]\n"
      "ds_read_b64_tr_b16 a[156:157], v223 offset:0\n"
      "ds_read_b64_tr_b16 a[158:159], v223 offset:4096\n"
      "v_mfma_f32_16x16x32_bf16 v[172:175], a[128:131], a[0:3], v[172:175]\n"
      "v_exp_f32 v165, v165\n"
      "v_mfma_f32_16x16x32_bf16 v[112:115], a[160:163], v[180:183], v[112:115]\n"
      "v_cvt_pk_bf16_f32 v177, v162, v163\n"
      "v_cvt_pk_bf16_f32 v178, v164, v165\n"
      "v_mfma_f32_16x16x32_bf16 v[112:115], a[192:195], v[184:187], v[112:115]\n"
      "v_exp_f32 v166, v166\n"
      "v_mfma_f32_16x16x32_bf16 v[116:119], a[196:199], v[184:187], v[116:119]\n"
      "ds_read_b64_tr_b16 a[152:153], v222 offset:0\n"
      "ds_read_b64_tr_b16 a[160:161], v224 offset:0\n"
      "v_mfma_f32_16x16x32_bf16 v[116:119], a[164:167], v[180:183], v[116:119]\n"
      "v_exp_f32 v167, v167\n"
      "v_mfma_f32_16x16x32_bf16 v[120:123], a[168:171], v[180:183], v[120:123]\n"
      "ds_read_b64_tr_b16 a[162:163], v224 offset:4096\n"
      "ds_read_b64_tr_b16 a[170:171], v226 offset:4096\n"
      "v_mfma_f32_16x16x32_bf16 v[120:123], a[200:203], v[184:187], v[120:123]\n"
      "ds_read_b64_tr_b16 a[196:197], v233 offset:0\n"
      "ds_read_b64_tr_b16 a[198:199], v233 offset:4096\n"
      "v_mfma_f32_16x16x32_bf16 v[124:127], a[204:207], v[184:187], v[124:127]\n"
      "ds_read_b64_tr_b16 a[204:205], v235 offset:0\n"
      "ds_read_b64_tr_b16 a[206:207], v235 offset:4096\n"
      "v_mfma_f32_16x16x32_bf16 v[124:127], a[172:175], v[180:183], v[124:127]\n"
      "v_exp_f32 v168, v168\n"
      "v_mfma_f32_16x16x32_bf16 a[220:223], a[228:231], v[180:183], a[220:223]\n"
      "ds_read_b64_tr_b16 a[166:167], v225 offset:4096\n"
      "ds_read_b64_tr_b16 a[168:169], v226 offset:0\n"
      "v_mfma_f32_16x16x32_bf16 a[220:223], a[228:231], v[184:187], a[220:223]\n"
      "s_mov_b32 m0, s60\n"
      "v_exp_f32 v169, v169\n"
      "v_mfma_f32_16x16x32_bf16 v[160:163], a[80:83], a[16:19], v[196:199]\n"
      "buffer_load_dwordx4 v240, s[40:43], s101 offen offset:0 lds\n"
      "v_mfma_f32_16x16x32_bf16 v[160:163], a[84:87], a[20:23], v[160:163]\n"
      "v_exp_f32 v170, v170\n"
      "v_mfma_f32_16x16x32_bf16 v[160:163], a[88:91], a[24:27], v[160:163]\n"
      "ds_read_b64_tr_b16 a[164:165], v225 offset:0\n"
      "v_cvt_pk_bf16_f32 v179, v166, v167\n"
      "v_mfma_f32_16x16x32_bf16 v[160:163], a[92:95], a[28:31], v[160:163]\n"
      "v_exp_f32 v171, v171\n"
      "v_mfma_f32_16x16x32_bf16 v[164:167], a[108:111], a[28:31], v[196:199]\n"
      "ds_read_b64_tr_b16 a[172:173], v227 offset:0\n"
      "ds_read_b64_tr_b16 a[174:175], v227 offset:4096\n"
      "v_mfma_f32_16x16x32_bf16 v[164:167], a[104:107], a[24:27], v[164:167]\n"
      "v_exp_f32 v172, v172\n"
      "v_mfma_f32_16x16x32_bf16 v[164:167], a[100:103], a[20:23], v[164:167]\n"
      "ds_read_b64_tr_b16 a[200:201], v234 offset:0\n"
      "ds_read_b64_tr_b16 a[202:203], v234 offset:4096\n"
      "v_mfma_f32_16x16x32_bf16 v[164:167], a[96:99], a[16:19], v[164:167]\n"
      "s_waitcnt lgkmcnt(15)\n"
      "v_exp_f32 v173, v173\n"
      "v_mfma_f32_16x16x32_bf16 v[128:131], a[144:147], v[188:191], v[128:131]\n"
      "s_waitcnt lgkmcnt(15)\n"
      "ds_read_b64_tr_b16 a[186:187], v230 offset:4096\n"
      "ds_read_b64_tr_b16 a[194:195], v232 offset:4096\n"
      "v_mfma_f32_16x16x32_bf16 v[132:135], a[148:151], v[188:191], v[132:135]\n"
      "s_waitcnt lgkmcnt(15)\n"
      "v_exp_f32 v174, v174\n"
      "v_mfma_f32_16x16x32_bf16 v[136:139], a[152:155], v[188:191], v[136:139]\n"
      "ds_read_b64_tr_b16 a[176:177], v228 offset:0\n"
      "ds_read_b64_tr_b16 a[184:185], v230 offset:0\n"
      "v_mfma_f32_16x16x32_bf16 v[140:143], a[156:159], v[188:191], v[140:143]\n"
      "s_waitcnt lgkmcnt(15)\n"
      "v_exp_f32 v175, v175\n"
      "v_mfma_f32_16x16x32_bf16 v[144:147], a[160:163], v[188:191], v[144:147]\n"
      "s_waitcnt lgkmcnt(8)\n"
      "ds_read_b64_tr_b16 a[178:179], v228 offset:4096\n"
      "ds_read_b64_tr_b16 a[180:181], v229 offset:0\n"
      "v_mfma_f32_16x16x32_bf16 v[148:151], a[164:167], v[188:191], v[148:151]\n"
      "v_exp_f32 v160, v160\n"
      "v_mfma_f32_16x16x32_bf16 v[152:155], a[168:171], v[188:191], v[152:155]\n"
      "s_waitcnt lgkmcnt(8)\n"
      "v_cvt_pk_bf16_f32 v180, v168, v169\n"
      "v_cvt_pk_bf16_f32 v181, v170, v171\n"
      "v_mfma_f32_16x16x32_bf16 v[156:159], a[172:175], v[188:191], v[156:159]\n"
      "v_exp_f32 v161, v161\n"
      "v_mfma_f32_16x16x32_bf16 v[168:171], a[112:115], a[16:19], v[196:199]\n"
      "v_cvt_pk_bf16_f32 v182, v172, v173\n"
      "v_cvt_pk_bf16_f32 v183, v174, v175\n"
      "v_mfma_f32_16x16x32_bf16 v[168:171], a[116:119], a[20:23], v[168:171]\n"
      "v_exp_f32 v162, v162\n"
      "v_mfma_f32_16x16x32_bf16 v[168:171], a[120:123], a[24:27], v[168:171]\n"
      "buffer_load_dwordx4 v241, s[40:43], s101 offen offset:1024 nt lds\n"
      "v_mfma_f32_16x16x32_bf16 v[168:171], a[124:127], a[28:31], v[168:171]\n"
      "v_exp_f32 v163, v163\n"
      "v_mfma_f32_16x16x32_bf16 v[172:175], a[140:143], a[28:31], v[196:199]\n"
      "ds_read_b64_tr_b16 a[188:189], v231 offset:0\n"
      "ds_read_b64_tr_b16 a[190:191], v231 offset:4096\n"
      "v_mfma_f32_16x16x32_bf16 v[172:175], a[136:139], a[24:27], v[172:175]\n"
      "v_exp_f32 v164, v164\n"
      "v_mfma_f32_16x16x32_bf16 v[172:175], a[132:135], a[20:23], v[172:175]\n"
      "ds_read_b64_tr_b16 a[192:193], v232 offset:0\n"
      "v_add_f32 v242, v242, v160\n"
      "v_mfma_f32_16x16x32_bf16 v[172:175], a[128:131], a[16:19], v[172:175]\n"
      "v_exp_f32 v165, v165\n"
      "v_mfma_f32_16x16x32_bf16 v[0:3], a[144:147], v[176:179], v[0:3]\n"
      "s_waitcnt lgkmcnt(4)\n"
      "v_add_f32 v243, v243, v161\n"
      "v_cvt_pk_bf16_f32 v184, v160, v161\n"
      "v_mfma_f32_16x16x32_bf16 v[0:3], a[176:179], v[180:183], v[0:3]\n"
      "s_waitcnt lgkmcnt(3)\n"
      "v_exp_f32 v166, v166\n"
      "v_mfma_f32_16x16x32_bf16 v[4:7], a[180:183], v[180:183], v[4:7]\n"
      "v_add_f32 v242, v242, v163\n"
      "v_cvt_pk_bf16_f32 v185, v162, v163\n"
      "v_mfma_f32_16x16x32_bf16 v[4:7], a[148:151], v[176:179], v[4:7]\n"
      "v_exp_f32 v167, v167\n"
      "v_mfma_f32_16x16x32_bf16 v[8:11], a[152:155], v[176:179], v[8:11]\n"
      "v_add_f32 v243, v243, v162\n"
      "v_add_f32 v242, v242, v164\n"
      "v_mfma_f32_16x16x32_bf16 v[8:11], a[184:187], v[180:183], v[8:11]\n"
      "s_waitcnt lgkmcnt(1)\n"
      "v_exp_f32 v168, v168\n"
      "v_mfma_f32_16x16x32_bf16 v[12:15], a[188:191], v[180:183], v[12:15]\n"
      "v_add_f32 v243, v243, v165\n"
      "v_cvt_pk_bf16_f32 v186, v164, v165\n"
      "v_mfma_f32_16x16x32_bf16 v[12:15], a[156:159], v[176:179], v[12:15]\n"
      "v_exp_f32 v169, v169\n"
      "v_mfma_f32_16x16x32_bf16 v[160:163], a[80:83], a[32:35], v[200:203]\n"
      "v_add_f32 v242, v242, v166\n"
      "v_cvt_pk_bf16_f32 v187, v166, v167\n"
      "v_mfma_f32_16x16x32_bf16 v[160:163], a[84:87], a[36:39], v[160:163]\n"
      "v_exp_f32 v170, v170\n"
      "v_mfma_f32_16x16x32_bf16 v[160:163], a[88:91], a[40:43], v[160:163]\n"
      "v_add_f32 v243, v243, v167\n"
      "v_add_f32 v242, v242, v168\n"
      "v_mfma_f32_16x16x32_bf16 v[160:163], a[92:95], a[44:47], v[160:163]\n"
      "v_exp_f32 v171, v171\n"
      "v_mfma_f32_16x16x32_bf16 v[164:167], a[108:111], a[44:47], v[200:203]\n"
      "buffer_load_dwordx4 v240, s[40:43], s101 offen offset:2048 lds\n"
      "v_mfma_f32_16x16x32_bf16 v[164:167], a[104:107], a[40:43], v[164:167]\n"
      "v_exp_f32 v172, v172\n"
      "v_mfma_f32_16x16x32_bf16 v[164:167], a[100:103], a[36:39], v[164:167]\n"
      "v_add_f32 v243, v243, v169\n"
      "v_cvt_pk_bf16_f32 v188, v168, v169\n"
      "v_mfma_f32_16x16x32_bf16 v[164:167], a[96:99], a[32:35], v[164:167]\n"
      "v_exp_f32 v173, v173\n"
      "v_mfma_f32_16x16x32_bf16 v[16:19], a[160:163], v[176:179], v[16:19]\n"
      "s_waitcnt lgkmcnt(0)\n"
      "v_add_f32 v242, v242, v170\n"
      "v_add_f32 v243, v243, v171\n"
      "v_mfma_f32_16x16x32_bf16 v[16:19], a[192:195], v[180:183], v[16:19]\n"
      "v_exp_f32 v174, v174\n"
      "v_mfma_f32_16x16x32_bf16 v[20:23], a[196:199], v[180:183], v[20:23]\n"
      "v_cvt_pk_bf16_f32 v189, v170, v171\n"
      "v_cvt_pk_bf16_f32 v190, v172, v173\n"
      "v_mfma_f32_16x16x32_bf16 v[20:23], a[164:167], v[176:179], v[20:23]\n"
      "v_exp_f32 v175, v175\n"
      "v_mfma_f32_16x16x32_bf16 v[24:27], a[168:171], v[176:179], v[24:27]\n"
      "v_add_f32 v242, v242, v172\n"
      "v_add_f32 v243, v243, v173\n"
      "v_mfma_f32_16x16x32_bf16 v[24:27], a[200:203], v[180:183], v[24:27]\n"
      "v_add_f32 v242, v242, v174\n"
      "v_cvt_pk_bf16_f32 v191, v174, v175\n"
      "v_mfma_f32_16x16x32_bf16 v[28:31], a[204:207], v[180:183], v[28:31]\n"
      "v_add_f32 v243, v243, v175\n"
      "v_mfma_f32_16x16x32_bf16 v[28:31], a[172:175], v[176:179], v[28:31]\n"
      "v_exp_f32 v160, v160\n"
      "v_mfma_f32_16x16x32_bf16 a[208:211], a[228:231], v[176:179], a[208:211]\n"
      "v_add_f32 v244, v244, v160\n"
      "v_mfma_f32_16x16x32_bf16 a[208:211], a[228:231], v[180:183], a[208:211]\n"
      "v_exp_f32 v161, v161\n"
      "v_mfma_f32_16x16x32_bf16 v[168:171], a[112:115], a[32:35], v[200:203]\n"
      "v_add_f32 v245, v245, v161\n"
      "v_cvt_pk_bf16_f32 v176, v160, v161\n"
      "v_mfma_f32_16x16x32_bf16 v[168:171], a[116:119], a[36:39], v[168:171]\n"
      "v_exp_f32 v162, v162\n"
      "v_mfma_f32_16x16x32_bf16 v[168:171], a[120:123], a[40:43], v[168:171]\n"
      "v_add_f32 v244, v244, v162\n"
      "v_mfma_f32_16x16x32_bf16 v[168:171], a[124:127], a[44:47], v[168:171]\n"
      "v_exp_f32 v163, v163\n"
      "v_mfma_f32_16x16x32_bf16 v[172:175], a[140:143], a[44:47], v[200:203]\n"
      "buffer_load_dwordx4 v241, s[40:43], s101 offen offset:3072 nt lds\n"
      "v_mfma_f32_16x16x32_bf16 v[172:175], a[136:139], a[40:43], v[172:175]\n"
      "s_add_u32 s52, s52, 0x100\n"
      "v_exp_f32 v164, v164\n"
      "v_mfma_f32_16x16x32_bf16 v[172:175], a[132:135], a[36:39], v[172:175]\n"
      "s_or_b32 s52, s52, 0x1bc00\n"
      "v_add_f32 v245, v245, v163\n"
      "v_cvt_pk_bf16_f32 v177, v162, v163\n"
      "v_mfma_f32_16x16x32_bf16 v[172:175], a[128:131], a[32:35], v[172:175]\n"
      "v_exp_f32 v165, v165\n"
      "v_mfma_f32_16x16x32_bf16 v[32:35], a[144:147], v[184:187], v[32:35]\n"
      "s_xor_b32 s101, s52, s100\n"
      "v_add_f32 v244, v244, v164\n"
      "v_cvt_pk_bf16_f32 v178, v164, v165\n"
      "v_mfma_f32_16x16x32_bf16 v[32:35], a[176:179], v[188:191], v[32:35]\n"
      "v_exp_f32 v166, v166\n"
      "v_mfma_f32_16x16x32_bf16 v[36:39], a[180:183], v[188:191], v[36:39]\n"
      "v_add_f32 v245, v245, v165\n"
      "v_add_f32 v244, v244, v166\n"
      "v_mfma_f32_16x16x32_bf16 v[36:39], a[148:151], v[184:187], v[36:39]\n"
      "v_exp_f32 v167, v167\n"
      "v_mfma_f32_16x16x32_bf16 v[40:43], a[152:155], v[184:187], v[40:43]\n"
      "v_add_f32 v245, v245, v167\n"
      "v_cvt_pk_bf16_f32 v179, v166, v167\n"
      "v_mfma_f32_16x16x32_bf16 v[40:43], a[184:187], v[188:191], v[40:43]\n"
      "v_exp_f32 v168, v168\n"
      "v_mfma_f32_16x16x32_bf16 v[44:47], a[188:191], v[188:191], v[44:47]\n"
      "s_getreg_b32 s69, hwreg(7, 0, 4)\n"
      "v_add_f32 v244, v244, v168\n"
      "v_mfma_f32_16x16x32_bf16 v[44:47], a[156:159], v[184:187], v[44:47]\n"
      "s_waitcnt vmcnt(8) lgkmcnt(0)\n"
      "s_barrier\n"
      "v_exp_f32 v169, v169\n"
      "v_mfma_f32_16x16x32_bf16 v[160:163], a[80:83], a[48:51], v[204:207]\n"
      "ds_read_b128 a[80:83], v212 offset:16384\n"
      "v_add_f32 v245, v245, v169\n"
      "v_mfma_f32_16x16x32_bf16 v[160:163], a[84:87], a[52:55], v[160:163]\n"
      "v_exp_f32 v170, v170\n"
      "v_mfma_f32_16x16x32_bf16 v[160:163], a[88:91], a[56:59], v[160:163]\n"
      "v_add_f32 v244, v244, v170\n"
      "v_cvt_pk_bf16_f32 v180, v168, v169\n"
      "v_mfma_f32_16x16x32_bf16 v[160:163], a[92:95], a[60:63], v[160:163]\n"
      "v_exp_f32 v171, v171\n"
      "v_mfma_f32_16x16x32_bf16 v[164:167], a[108:111], a[60:63], v[204:207]\n"
      "ds_read_b128 a[108:111], v215 offset:20480\n"
      "v_add_f32 v245, v245, v171\n"
      "v_mfma_f32_16x16x32_bf16 v[164:167], a[104:107], a[56:59], v[164:167]\n"
      "v_exp_f32 v172, v172\n"
      "v_mfma_f32_16x16x32_bf16 v[164:167], a[100:103], a[52:55], v[164:167]\n"
      "ds_read_b128 a[88:91], v214 offset:16384\n"
      "v_cvt_pk_bf16_f32 v181, v170, v171\n"
      "v_mfma_f32_16x16x32_bf16 v[164:167], a[96:99], a[48:51], v[164:167]\n"
      "v_exp_f32 v173, v173\n"
      "v_mfma_f32_16x16x32_bf16 v[48:51], a[160:163], v[184:187], v[48:51]\n"
      "ds_read_b128 a[104:107], v214 offset:20480\n"
      "v_add_f32 v244, v244, v173\n"
      "v_mfma_f32_16x16x32_bf16 v[48:51], a[192:195], v[188:191], v[48:51]\n"
      "s_mov_b32 m0, s57\n"
      "v_exp_f32 v174, v174\n"
      "v_mfma_f32_16x16x32_bf16 v[52:55], a[196:199], v[188:191], v[52:55]\n"
      "buffer_load_dwordx4 v236, s[36:39], s101 offen offset:0 lds\n"
      "v_mfma_f32_16x16x32_bf16 v[52:55], a[164:167], v[184:187], v[52:55]\n"
      "v_exp_f32 v175, v175\n"
      "v_mfma_f32_16x16x32_bf16 v[56:59], a[168:171], v[184:187], v[56:59]\n"
      "ds_read_b128 a[92:95], v215 offset:16384\n"
      "v_add_f32 v245, v245, v172\n"
      "v_mfma_f32_16x16x32_bf16 v[56:59], a[200:203], v[188:191], v[56:59]\n"
      "v_exp_f32 v160, v160\n"
      "v_mfma_f32_16x16x32_bf16 v[60:63], a[204:207], v[188:191], v[60:63]\n"
      "ds_read_b128 a[84:87], v213 offset:16384\n"
      "v_cvt_pk_bf16_f32 v182, v172, v173\n"
      "v_mfma_f32_16x16x32_bf16 v[60:63], a[172:175], v[184:187], v[60:63]\n"
      "v_exp_f32 v161, v161\n"
      "v_mfma_f32_16x16x32_bf16 v[168:171], a[112:115], a[48:51], v[204:207]\n"
      "v_add_f32 v244, v244, v175\n"
      "v_cvt_pk_bf16_f32 v183, v174, v175\n"
      "v_mfma_f32_16x16x32_bf16 v[168:171], a[116:119], a[52:55], v[168:171]\n"
      "v_exp_f32 v162, v162\n"
      "v_mfma_f32_16x16x32_bf16 v[168:171], a[120:123], a[56:59], v[168:171]\n"
      "ds_read_b128 a[116:119], v217 offset:16384\n"
      "v_add_f32 v245, v245, v174\n"
      "v_mfma_f32_16x16x32_bf16 v[168:171], a[124:127], a[60:63], v[168:171]\n"
      "v_exp_f32 v163, v163\n"
      "v_mfma_f32_16x16x32_bf16 v[172:175], a[140:143], a[60:63], v[204:207]\n"
      "ds_read_b128 a[140:143], v219 offset:20480\n"
      "v_cvt_pk_bf16_f32 v185, v162, v163\n"
      "v_mfma_f32_16x16x32_bf16 v[172:175], a[136:139], a[56:59], v[172:175]\n"
      "v_exp_f32 v164, v164\n"
      "v_mfma_f32_16x16x32_bf16 v[172:175], a[132:135], a[52:55], v[172:175]\n"
      "ds_read_b128 a[100:103], v213 offset:20480\n"
      "v_cvt_pk_bf16_f32 v184, v160, v161\n"
      "v_mfma_f32_16x16x32_bf16 v[172:175], a[128:131], a[48:51], v[172:175]\n"
      "v_exp_f32 v165, v165\n"
      "v_mfma_f32_16x16x32_bf16 v[64:67], a[144:147], v[176:179], v[64:67]\n"
      "ds_read_b128 a[136:139], v218 offset:20480\n"
      "v_cvt_pk_bf16_f32 v186, v164, v165\n"
      "v_mfma_f32_16x16x32_bf16 v[64:67], a[176:179], v[180:183], v[64:67]\n"
      "v_exp_f32 v166, v166\n"
      "v_mfma_f32_16x16x32_bf16 v[68:71], a[180:183], v[180:183], v[68:71]\n"
      "ds_read_b128 a[96:99], v212 offset:20480\n"
      "v_mfma_f32_16x16x32_bf16 v[68:71], a[148:151], v[176:179], v[68:71]\n"
      "v_exp_f32 v167, v167\n"
      "v_mfma_f32_16x16x32_bf16 v[72:75], a[152:155], v[176:179], v[72:75]\n"
      "buffer_load_dwordx4 v237, s[36:39], s101 offen offset:1024 lds\n"
      "v_mfma_f32_16x16x32_bf16 v[72:75], a[184:187], v[180:183], v[72:75]\n"
      "v_exp_f32 v168, v168\n"
      "v_mfma_f32_16x16x32_bf16 v[76:79], a[188:191], v[180:183], v[76:79]\n"
      "ds_read_b128 a[132:135], v217 offset:20480\n"
      "v_cvt_pk_bf16_f32 v187, v166, v167\n"
      "v_mfma_f32_16x16x32_bf16 v[76:79], a[156:159], v[176:179], v[76:79]\n"
      "s_waitcnt lgkmcnt(11)\n"
      "v_exp_f32 v169, v169\n"
      "L_ent0%=:\n"
      "v_mfma_f32_16x16x32_bf16 v[160:163], a[80:83], a[64:67], v[208:211]\n"
      "s_waitcnt lgkmcnt(6)\n"
      "ds_read_b128 a[124:127], v219 offset:16384\n"
      "v_cvt_pk_bf16_f32 v188, v168, v169\n"
      "v_mfma_f32_16x16x32_bf16 v[160:163], a[84:87], a[68:71], v[160:163]\n"
      "v_exp_f32 v170, v170\n"
      "v_mfma_f32_16x16x32_bf16 v[160:163], a[88:91], a[72:75], v[160:163]\n"
      "ds_read_b128 a[128:131], v216 offset:20480\n"
      "v_mfma_f32_16x16x32_bf16 v[160:163], a[92:95], a[76:79], v[160:163]\n"
      "v_exp_f32 v171, v171\n"
      "v_mfma_f32_16x16x32_bf16 v[164:167], a[108:111], a[76:79], v[208:211]\n"
      "ds_read_b128 a[120:123], v218 offset:16384\n"
      "v_cvt_pk_bf16_f32 v189, v170, v171\n"
      "v_mfma_f32_16x16x32_bf16 v[164:167], a[104:107], a[72:75], v[164:167]\n"
      "s_waitcnt lgkmcnt(6)\n"
      "v_exp_f32 v172, v172\n"
      "v_mfma_f32_16x16x32_bf16 v[164:167], a[100:103], a[68:71], v[164:167]\n"
      "s_waitcnt lgkmcnt(4)\n"
      "ds_read_b128 a[112:115], v216 offset:16384\n"
      "v_mfma_f32_16x16x32_bf16 v[164:167], a[96:99], a[64:67], v[164:167]\n"
      "v_exp_f32 v173, v173\n"
      "v_mfma_f32_16x16x32_bf16 v[80:83], a[160:163], v[176:179], v[80:83]\n"
      "v_cvt_pk_bf16_f32 v190, v172, v173\n"
      "v_mfma_f32_16x16x32_bf16 v[80:83], a[192:195], v[180:183], v[80:83]\n"
      "v_exp_f32 v174, v174\n"
      "v_mfma_f32_16x16x32_bf16 v[84:87], a[196:199], v[180:183], v[84:87]\n"
      "s_cmp_gt_u32 s69, 5\n"
      "s_cbranch_scc1 L_ev0%=\n"
      "v_mfma_f32_16x16x32_bf16 v[84:87], a[164:167], v[176:179], v[84:87]\n"
      "v_exp_f32 v175, v175\n"
      "v_mfma_f32_16x16x32_bf16 v[88:91], a[168:171], v[176:179], v[88:91]\n"
      "v_cvt_pk_bf16_f32 v191, v174, v175\n"
      "v_mfma_f32_16x16x32_bf16 v[88:91], a[200:203], v[180:183], v[88:91]\n"
      "v_exp_f32 v160, v160\n"
      "v_mfma_f32_16x16x32_bf16 v[92:95], a[204:207], v[180:183], v[92:95]\n"
      "buffer_load_dwordx4 v238, s[36:39], s101 offen offset:2048 lds\n"
      "v_mfma_f32_16x16x32_bf16 v[92:95], a[172:175], v[176:179], v[92:95]\n"
      "v_exp_f32 v161, v161\n"
      "v_mfma_f32_16x16x32_bf16 v[168:171], a[80:83], a[0:3], v[192:195]\n"
      "v_add_f32 v246, v246, v161\n"
      "v_cvt_pk_bf16_f32 v176, v160, v161\n"
      "v_mfma_f32_16x16x32_bf16 v[168:171], a[84:87], a[4:7], v[168:171]\n"
      "v_exp_f32 v162, v162\n"
      "v_mfma_f32_16x16x32_bf16 v[168:171], a[88:91], a[8:11], v[168:171]\n"
      "v_add_f32 v247, v247, v160\n"
      "v_add_f32 v246, v246, v162\n"
      "v_mfma_f32_16x16x32_bf16 v[168:171], a[92:95], a[12:15], v[168:171]\n"
      "v_exp_f32 v163, v163\n"
      "v_mfma_f32_16x16x32_bf16 v[172:175], a[108:111], a[12:15], v[192:195]\n"
      "v_add_f32 v247, v247, v163\n"
      "v_cvt_pk_bf16_f32 v177, v162, v163\n"
      "v_mfma_f32_16x16x32_bf16 v[172:175], a[104:107], a[8:11], v[172:175]\n"
      "v_exp_f32 v164, v164\n"
      "v_mfma_f32_16x16x32_bf16 v[172:175], a[100:103], a[4:7], v[172:175]\n"
      "v_add_f32 v246, v246, v164\n"
      "v_mfma_f32_16x16x32_bf16 v[172:175], a[96:99], a[0:3], v[172:175]\n"
      "v_exp_f32 v165, v165\n"
      "v_mfma_f32_16x16x32_bf16 v[96:99], a[144:147], v[184:187], v[96:99]\n"
      "v_add_f32 v247, v247, v165\n"
      "v_cvt_pk_bf16_f32 v178, v164, v165\n"
      "v_mfma_f32_16x16x32_bf16 v[96:99], a[176:179], v[188:191], v[96:99]\n"
      "v_exp_f32 v166, v166\n"
      "v_mfma_f32_16x16x32_bf16 v[100:103], a[180:183], v[188:191], v[100:103]\n"
      "ds_read_b64_tr_b16 a[146:147], v220 offset:20480\n"
      "ds_read_b64_tr_b16 a[182:183], v229 offset:20480\n"
      "v_mfma_f32_16x16x32_bf16 v[100:103], a[148:151], v[184:187], v[100:103]\n"
      "v_exp_f32 v167, v167\n"
      "v_mfma_f32_16x16x32_bf16 v[104:107], a[152:155], v[184:187], v[104:107]\n"
      "ds_read_b64_tr_b16 a[150:151], v221 offset:20480\n"
      "v_add_f32 v246, v246, v167\n"
      "v_mfma_f32_16x16x32_bf16 v[104:107], a[184:187], v[188:191], v[104:107]\n"
      "v_exp_f32 v168, v168\n"
      "v_mfma_f32_16x16x32_bf16 v[108:111], a[188:191], v[188:191], v[108:111]\n"
      "ds_read_b64_tr_b16 a[144:145], v220 offset:16384\n"
      "v_cvt_pk_bf16_f32 v179, v166, v167\n"
      "v_mfma_f32_16x16x32_bf16 v[108:111], a[156:159], v[184:187], v[108:111]\n"
      "s_waitcnt lgkmcnt(4)\n"
      "v_exp_f32 v169, v169\n"
      "v_mfma_f32_16x16x32_bf16 v[160:163], a[112:115], a[0:3], v[192:195]\n"
      "buffer_load_dwordx4 v239, s[36:39], s101 offen offset:3072 lds\n"
      "v_mfma_f32_16x16x32_bf16 v[160:163], a[116:119], a[4:7], v[160:163]\n"
      "v_exp_f32 v170, v170\n"
      "v_mfma_f32_16x16x32_bf16 v[160:163], a[120:123], a[8:11], v[160:163]\n"
      "v_add_f32 v247, v247, v166\n"
      "v_cvt_pk_bf16_f32 v180, v168, v169\n"
      "v_mfma_f32_16x16x32_bf16 v[160:163], a[124:127], a[12:15], v[160:163]\n"
      "v_exp_f32 v171, v171\n"
      "v_mfma_f32_16x16x32_bf16 v[164:167], a[140:143], a[12:15], v[192:195]\n"
      "ds_read_b64_tr_b16 a[148:149], v221 offset:16384\n"
      "ds_read_b64_tr_b16 a[154:155], v222 offset:20480\n"
      "v_mfma_f32_16x16x32_bf16 v[164:167], a[136:139], a[8:11], v[164:167]\n"
      "v_exp_f32 v172, v172\n"
      "v_mfma_f32_16x16x32_bf16 v[164:167], a[132:135], a[4:7], v[164:167]\n"
      "ds_read_b64_tr_b16 a[156:157], v223 offset:16384\n"
      "ds_read_b64_tr_b16 a[158:159], v223 offset:20480\n"
      "v_mfma_f32_16x16x32_bf16 v[164:167], a[128:131], a[0:3], v[164:167]\n"
      "v_exp_f32 v173, v173\n"
      "v_mfma_f32_16x16x32_bf16 v[112:115], a[160:163], v[184:187], v[112:115]\n"
      "v_cvt_pk_bf16_f32 v181, v170, v171\n"
      "v_cvt_pk_bf16_f32 v182, v172, v173\n"
      "v_mfma_f32_16x16x32_bf16 v[112:115], a[192:195], v[188:191], v[112:115]\n"
      "v_exp_f32 v174, v174\n"
      "v_mfma_f32_16x16x32_bf16 v[116:119], a[196:199], v[188:191], v[116:119]\n"
      "ds_read_b64_tr_b16 a[152:153], v222 offset:16384\n"
      "ds_read_b64_tr_b16 a[160:161], v224 offset:16384\n"
      "v_mfma_f32_16x16x32_bf16 v[116:119], a[164:167], v[184:187], v[116:119]\n"
      "v_exp_f32 v175, v175\n"
      "v_mfma_f32_16x16x32_bf16 v[120:123], a[168:171], v[184:187], v[120:123]\n"
      "ds_read_b64_tr_b16 a[162:163], v224 offset:20480\n"
      "ds_read_b64_tr_b16 a[170:171], v226 offset:20480\n"
      "v_mfma_f32_16x16x32_bf16 v[120:123], a[200:203], v[188:191], v[120:123]\n"
      "ds_read_b64_tr_b16 a[196:197], v233 offset:16384\n"
      "ds_read_b64_tr_b16 a[198:199], v233 offset:20480\n"
      "v_mfma_f32_16x16x32_bf16 v[124:127], a[204:207], v[188:191], v[124:127]\n"
      "ds_read_b64_tr_b16 a[204:205], v235 offset:16384\n"
      "ds_read_b64_tr_b16 a[206:207], v235 offset:20480\n"
      "v_mfma_f32_16x16x32_bf16 v[124:127], a[172:175], v[184:187], v[124:127]\n"
      "v_exp_f32 v160, v160\n"
      "v_mfma_f32_16x16x32_bf16 a[220:223], a[228:231], v[184:187], a[220:223]\n"
      "ds_read_b64_tr_b16 a[166:167], v225 offset:20480\n"
      "ds_read_b64_tr_b16 a[168:169], v226 offset:16384\n"
      "v_mfma_f32_16x16x32_bf16 a[220:223], a[228:231], v[188:191], a[220:223]\n"
      "s_mov_b32 m0, s61\n"
      "v_exp_f32 v161, v161\n"
      "v_mfma_f32_16x16x32_bf16 v[168:171], a[80:83], a[16:19], v[196:199]\n"
      "buffer_load_dwordx4 v240, s[40:43], s101 offen offset:0 lds\n"
      "v_mfma_f32_16x16x32_bf16 v[168:171], a[84:87], a[20:23], v[168:171]\n"
      "v_exp_f32 v162, v162\n"
      "v_mfma_f32_16x16x32_bf16 v[168:171], a[88:91], a[24:27], v[168:171]\n"
      "ds_read_b64_tr_b16 a[164:165], v225 offset:16384\n"
      "v_cvt_pk_bf16_f32 v183, v174, v175\n"
      "v_mfma_f32_16x16x32_bf16 v[168:171], a[92:95], a[28:31], v[168:171]\n"
      "v_exp_f32 v163, v163\n"
      "v_mfma_f32_16x16x32_bf16 v[172:175], a[108:111], a[28:31], v[196:199]\n"
      "ds_read_b64_tr_b16 a[172:173], v227 offset:16384\n"
      "ds_read_b64_tr_b16 a[174:175], v227 offset:20480\n"
      "v_mfma_f32_16x16x32_bf16 v[172:175], a[104:107], a[24:27], v[172:175]\n"
      "v_exp_f32 v164, v164\n"
      "v_mfma_f32_16x16x32_bf16 v[172:175], a[100:103], a[20:23], v[172:175]\n"
      "ds_read_b64_tr_b16 a[200:201], v234 offset:16384\n"
      "ds_read_b64_tr_b16 a[202:203], v234 offset:20480\n"
      "v_mfma_f32_16x16x32_bf16 v[172:175], a[96:99], a[16:19], v[172:175]\n"
      "s_waitcnt lgkmcnt(15)\n"
      "v_exp_f32 v165, v165\n"
      "v_mfma_f32_16x16x32_bf16 v[128:131], a[144:147], v[176:179], v[128:131]\n"
      "s_waitcnt lgkmcnt(15)\n"
      "ds_read_b64_tr_b16 a[186:187], v230 offset:20480\n"
      "ds_read_b64_tr_b16 a[194:195], v232 offset:20480\n"
      "v_mfma_f32_16x16x32_bf16 v[132:135], a[148:151], v[176:179], v[132:135]\n"
      "s_waitcnt lgkmcnt(15)\n"
      "v_exp_f32 v166, v166\n"
      "v_mfma_f32_16x16x32_bf16 v[136:139], a[152:155], v[176:179], v[136:139]\n"
      "ds_read_b64_tr_b16 a[176:177], v228 offset:16384\n"
      "ds_read_b64_tr_b16 a[184:185], v230 offset:16384\n"
      "v_mfma_f32_16x16x32_bf16 v[140:143], a[156:159], v[176:179], v[140:143]\n"
      "s_waitcnt lgkmcnt(15)\n"
      "v_exp_f32 v167, v167\n"
      "v_mfma_f32_16x16x32_bf16 v[144:147], a[160:163], v[176:179], v[144:147]\n"
      "s_waitcnt lgkmcnt(8)\n"
      "ds_read_b64_tr_b16 a[178:179], v228 offset:20480\n"
      "ds_read_b64_tr_b16 a[180:181], v229 offset:16384\n"
      "v_mfma_f32_16x16x32_bf16 v[148:151], a[164:167], v[176:179], v[148:151]\n"
      "v_exp_f32 v168, v168\n"
      "v_mfma_f32_16x16x32_bf16 v[152:155], a[168:171], v[176:179], v[152:155]\n"
      "s_waitcnt lgkmcnt(8)\n"
      "v_cvt_pk_bf16_f32 v184, v160, v161\n"
      "v_cvt_pk_bf16_f32 v185, v162, v163\n"
      "v_mfma_f32_16x16x32_bf16 v[156:159], a[172:175], v[176:179], v[156:159]\n"
      "v_exp_f32 v169, v169\n"
      "v_mfma_f32_16x16x32_bf16 v[160:163], a[112:115], a[16:19], v[196:199]\n"
      "v_cvt_pk_bf16_f32 v186, v164, v165\n"
      "v_cvt_pk_bf16_f32 v187, v166, v167\n"
      "v_mfma_f32_16x16x32_bf16 v[160:163], a[116:119], a[20:23], v[160:163]\n"
      "v_exp_f32 v170, v170\n"
      "v_mfma_f32_16x16x32_bf16 v[160:163], a[120:123], a[24:27], v[160:163]\n"
      "buffer_load_dwordx4 v241, s[40:43], s101 offen offset:1024 nt lds\n"
      "v_mfma_f32_16x16x32_bf16 v[160:163], a[124:127], a[28:31], v[160:163]\n"
      "v_exp_f32 v171, v171\n"
      "v_mfma_f32_16x16x32_bf16 v[164:167], a[140:143], a[28:31], v[196:199]\n"
      "ds_read_b64_tr_b16 a[188:189], v231 offset:16384\n"
      "ds_read_b64_tr_b16 a[190:191], v231 offset:20480\n"
      "v_mfma_f32_16x16x32_bf16 v[164:167], a[136:139], a[24:27], v[164:167]\n"
      "v_exp_f32 v172, v172\n"
      "v_mfma_f32_16x16x32_bf16 v[164:167], a[132:135], a[20:23], v[164:167]\n"
      "ds_read_b64_tr_b16 a[192:193], v232 offset:16384\n"
      "v_add_f32 v242, v242, v168\n"
      "v_mfma_f32_16x16x32_bf16 v[164:167], a[128:131], a[16:19], v[164:167]\n"
      "v_exp_f32 v173, v173\n"
      "v_mfma_f32_16x16x32_bf16 v[0:3], a[144:147], v[180:183], v[0:3]\n"
      "s_waitcnt lgkmcnt(4)\n"
      "v_add_f32 v243, v243, v169\n"
      "v_cvt_pk_bf16_f32 v188, v168, v169\n"
      "v_mfma_f32_16x16x32_bf16 v[0:3], a[176:179], v[184:187], v[0:3]\n"
      "s_waitcnt lgkmcnt(3)\n"
      "v_exp_f32 v174, v174\n"
      "v_mfma_f32_16x16x32_bf16 v[4:7], a[180:183], v[184:187], v[4:7]\n"
      "v_add_f32 v242, v242, v171\n"
      "v_cvt_pk_bf16_f32 v189, v170, v171\n"
      "v_mfma_f32_16x16x32_bf16 v[4:7], a[148:151], v[180:183], v[4:7]\n"
      "v_exp_f32 v175, v175\n"
      "v_mfma_f32_16x16x32_bf16 v[8:11], a[152:155], v[180:183], v[8:11]\n"
      "v_add_f32 v243, v243, v170\n"
      "v_add_f32 v242, v242, v172\n"
      "v_mfma_f32_16x16x32_bf16 v[8:11], a[184:187], v[184:187], v[8:11]\n"
      "s_waitcnt lgkmcnt(1)\n"
      "v_exp_f32 v160, v160\n"
      "v_mfma_f32_16x16x32_bf16 v[12:15], a[188:191], v[184:187], v[12:15]\n"
      "v_add_f32 v243, v243, v173\n"
      "v_cvt_pk_bf16_f32 v190, v172, v173\n"
      "v_mfma_f32_16x16x32_bf16 v[12:15], a[156:159], v[180:183], v[12:15]\n"
      "v_exp_f32 v161, v161\n"
      "v_mfma_f32_16x16x32_bf16 v[168:171], a[80:83], a[32:35], v[200:203]\n"
      "v_add_f32 v242, v242, v174\n"
      "v_cvt_pk_bf16_f32 v191, v174, v175\n"
      "v_mfma_f32_16x16x32_bf16 v[168:171], a[84:87], a[36:39], v[168:171]\n"
      "v_exp_f32 v162, v162\n"
      "v_mfma_f32_16x16x32_bf16 v[168:171], a[88:91], a[40:43], v[168:171]\n"
      "v_add_f32 v243, v243, v175\n"
      "v_add_f32 v242, v242, v160\n"
      "v_mfma_f32_16x16x32_bf16 v[168:171], a[92:95], a[44:47], v[168:171]\n"
      "v_exp_f32 v163, v163\n"
      "v_mfma_f32_16x16x32_bf16 v[172:175], a[108:111], a[44:47], v[200:203]\n"
      "buffer_load_dwordx4 v240, s[40:43], s101 offen offset:2048 lds\n"
      "v_mfma_f32_16x16x32_bf16 v[172:175], a[104:107], a[40:43], v[172:175]\n"
      "v_exp_f32 v164, v164\n"
      "v_mfma_f32_16x16x32_bf16 v[172:175], a[100:103], a[36:39], v[172:175]\n"
      "v_add_f32 v243, v243, v161\n"
      "v_cvt_pk_bf16_f32 v176, v160, v161\n"
      "v_mfma_f32_16x16x32_bf16 v[172:175], a[96:99], a[32:35], v[172:175]\n"
      "v_exp_f32 v165, v165\n"
      "v_mfma_f32_16x16x32_bf16 v[16:19], a[160:163], v[180:183], v[16:19]\n"
      "s_waitcnt lgkmcnt(0)\n"
      "v_add_f32 v242, v242, v162\n"
      "v_add_f32 v243, v243, v163\n"
      "v_mfma_f32_16x16x32_bf16 v[16:19], a[192:195], v[184:187], v[16:19]\n"
      "v_exp_f32 v166, v166\n"
      "v_mfma_f32_16x16x32_bf16 v[20:23], a[196:199], v[184:187], v[20:23]\n"
      "v_cvt_pk_bf16_f32 v177, v162, v163\n"
      "v_cvt_pk_bf16_f32 v178, v164, v165\n"
      "v_mfma_f32_16x16x32_bf16 v[20:23], a[164:167], v[180:183], v[20:23]\n"
      "v_exp_f32 v167, v167\n"
      "v_mfma_f32_16x16x32_bf16 v[24:27], a[168:171], v[180:183], v[24:27]\n"
      "v_add_f32 v242, v242, v164\n"
      "v_add_f32 v243, v243, v165\n"
      "v_mfma_f32_16x16x32_bf16 v[24:27], a[200:203], v[184:187], v[24:27]\n"
      "v_add_f32 v242, v242, v166\n"
      "v_cvt_pk_bf16_f32 v179, v166, v167\n"
      "v_mfma_f32_16x16x32_bf16 v[28:31], a[204:207], v[184:187], v[28:31]\n"
      "v_add_f32 v243, v243, v167\n"
      "v_mfma_f32_16x16x32_bf16 v[28:31], a[172:175], v[180:183], v[28:31]\n"
      "v_exp_f32 v168, v168\n"
      "v_mfma_f32_16x16x32_bf16 a[208:211], a[228:231], v[180:183], a[208:211]\n"
      "v_add_f32 v244, v244, v168\n"
      "v_mfma_f32_16x16x32_bf16 a[208:211], a[228:231], v[184:187], a[208:211]\n"
      "v_exp_f32 v169, v169\n"
      "v_mfma_f32_16x16x32_bf16 v[160:163], a[112:115], a[32:35], v[200:203]\n"
      "v_add_f32 v245, v245, v169\n"
      "v_cvt_pk_bf16_f32 v180, v168, v169\n"
      "v_mfma_f32_16x16x32_bf16 v[160:163], a[116:119], a[36:39], v[160:163]\n"
      "v_exp_f32 v170, v170\n"
      "v_mfma_f32_16x16x32_bf16 v[160:163], a[120:123], a[40:43], v[160:163]\n"
      "v_add_f32 v244, v244, v170\n"
      "v_mfma_f32_16x16x32_bf16 v[160:163], a[124:127], a[44:47], v[160:163]\n"
      "v_exp_f32 v171, v171\n"
      "v_mfma_f32_16x16x32_bf16 v[164:167], a[140:143], a[44:47], v[200:203]\n"
      "buffer_load_dwordx4 v241, s[40:43], s101 offen offset:3072 nt lds\n"
      "v_mfma_f32_16x16x32_bf16 v[164:167], a[136:139], a[40:43], v[164:167]\n"
      "s_add_u32 s52, s52, 0x100\n"
      "v_exp_f32 v172, v172\n"
      "v_mfma_f32_16x16x32_bf16 v[164:167], a[132:135], a[36:39], v[164:167]\n"
      "s_or_b32 s52, s52, 0x1bc00\n"
      "v_add_f32 v245, v245, v171\n"
      "v_cvt_pk_bf16_f32 v181, v170, v171\n"
      "v_mfma_f32_16x16x32_bf16 v[164:167], a[128:131], a[32:35], v[164:167]\n"
      "v_exp_f32 v173, v173\n"
      "v_mfma_f32_16x16x32_bf16 v[32:35], a[144:147], v[188:191], v[32:35]\n"
      "s_xor_b32 s101, s52, s100\n"
      "v_add_f32 v244, v244, v172\n"
      "v_cvt_pk_bf16_f32 v182, v172, v173\n"
      "v_mfma_f32_16x16x32_bf16 v[32:35], a[176:179], v[176:179], v[32:35]\n"
      "v_exp_f32 v174, v174\n"
      "v_mfma_f32_16x16x32_bf16 v[36:39], a[180:183], v[176:179], v[36:39]\n"
      "v_add_f32 v245, v245, v173\n"
      "v_add_f32 v244, v244, v174\n"
      "v_mfma_f32_16x16x32_bf16 v[36:39], a[148:151], v[188:191], v[36:39]\n"
      "v_exp_f32 v175, v175\n"
      "v_mfma_f32_16x16x32_bf16 v[40:43], a[152:155], v[188:191], v[40:43]\n"
      "v_add_f32 v245, v245, v175\n"
      "v_cvt_pk_bf16_f32 v183, v174, v175\n"
      "v_mfma_f32_16x16x32_bf16 v[40:43], a[184:187], v[176:179], v[40:43]\n"
      "v_exp_f32 v160, v160\n"
      "v_mfma_f32_16x16x32_bf16 v[44:47], a[188:191], v[176:179], v[44:47]\n"
      "s_getreg_b32 s69, hwreg(7, 0, 4)\n"
      "v_add_f32 v244, v244, v160\n"
      "v_mfma_f32_16x16x32_bf16 v[44:47], a[156:159], v[188:191], v[44:47]\n"
      "s_waitcnt vmcnt(8) lgkmcnt(0)\n"
      "s_barrier\n"
      "v_exp_f32 v161, v161\n"
      "v_mfma_f32_16x16x32_bf16 v[168:171], a[80:83], a[48:51], v[204:207]\n"
      "ds_read_b128 a[80:83], v212 offset:32768\n"
      "v_add_f32 v245, v245, v161\n"
      "v_mfma_f32_16x16x32_bf16 v[168:171], a[84:87], a[52:55], v[168:171]\n"
      "v_exp_f32 v162, v162\n"
      "v_mfma_f32_16x16x32_bf16 v[168:171], a[88:91], a[56:59], v[168:171]\n"
      "v_add_f32 v244, v244, v162\n"
      "v_cvt_pk_bf16_f32 v184, v160, v161\n"
      "v_mfma_f32_16x16x32_bf16 v[168:171], a[92:95], a[60:63], v[168:171]\n"
      "v_exp_f32 v163, v163\n"
      "v_mfma_f32_16x16x32_bf16 v[172:175], a[108:111], a[60:63], v[204:207]\n"
      "ds_read_b128 a[108:111], v215 offset:36864\n"
      "v_add_f32 v245, v245, v163\n"
      "v_mfma_f32_16x16x32_bf16 v[172:175], a[104:107], a[56:59], v[172:175]\n"
      "v_exp_f32 v164, v164\n"
      "v_mfma_f32_16x16x32_bf16 v[172:175], a[100:103], a[52:55], v[172:175]\n"
      "ds_read_b128 a[88:91], v214 offset:32768\n"
      "v_cvt_pk_bf16_f32 v185, v162, v163\n"
      "v_mfma_f32_16x16x32_bf16 v[172:175], a[96:99], a[48:51], v[172:175]\n"
      "v_exp_f32 v165, v165\n"
      "v_mfma_f32_16x16x32_bf16 v[48:51], a[160:163], v[188:191], v[48:51]\n"
      "ds_read_b128 a[104:107], v214 offset:36864\n"
      "v_add_f32 v244, v244, v165\n"
      "v_mfma_f32_16x16x32_bf16 v[48:51], a[192:195], v[176:179], v[48:51]\n"
      "s_mov_b32 m0, s54\n"
      "v_exp_f32 v166, v166\n"
      "v_mfma_f32_16x16x32_bf16 v[52:55], a[196:199], v[176:179], v[52:55]\n"
      "buffer_load_dwordx4 v236, s[36:39], s101 offen offset:0 lds\n"
      "v_mfma_f32_16x16x32_bf16 v[52:55], a[164:167], v[188:191], v[52:55]\n"
      "v_exp_f32 v167, v167\n"
      "v_mfma_f32_16x16x32_bf16 v[56:59], a[168:171], v[188:191], v[56:59]\n"
      "ds_read_b128 a[92:95], v215 offset:32768\n"
      "v_add_f32 v245, v245, v164\n"
      "v_mfma_f32_16x16x32_bf16 v[56:59], a[200:203], v[176:179], v[56:59]\n"
      "v_exp_f32 v168, v168\n"
      "v_mfma_f32_16x16x32_bf16 v[60:63], a[204:207], v[176:179], v[60:63]\n"
      "ds_read_b128 a[84:87], v213 offset:32768\n"
      "v_cvt_pk_bf16_f32 v186, v164, v165\n"
      "v_mfma_f32_16x16x32_bf16 v[60:63], a[172:175], v[188:191], v[60:63]\n"
      "v_exp_f32 v169, v169\n"
      "v_mfma_f32_16x16x32_bf16 v[160:163], a[112:115], a[48:51], v[204:207]\n"
      "v_add_f32 v244, v244, v167\n"
      "v_cvt_pk_bf16_f32 v187, v166, v167\n"
      "v_mfma_f32_16x16x32_bf16 v[160:163], a[116:119], a[52:55], v[160:163]\n"
      "v_exp_f32 v170, v170\n"
      "v_mfma_f32_16x16x32_bf16 v[160:163], a[120:123], a[56:59], v[160:163]\n"
      "ds_read_b128 a[116:119], v217 offset:32768\n"
      "v_add_f32 v245, v245, v166\n"
      "v_mfma_f32_16x16x32_bf16 v[160:163], a[124:127], a[60:63], v[160:163]\n"
      "v_exp_f32 v171, v171\n"
      "v_mfma_f32_16x16x32_bf16 v[164:167], a[140:143], a[60:63], v[204:207]\n"
      "ds_read_b128 a[140:143], v219 offset:36864\n"
      "v_cvt_pk_bf16_f32 v189, v170, v171\n"
      "v_mfma_f32_16x16x32_bf16 v[164:167], a[136:139], a[56:59], v[164:167]\n"
      "v_exp_f32 v172, v172\n"
      "v_mfma_f32_16x16x32_bf16 v[164:167], a[132:135], a[52:55], v[164:167]\n"
      "ds_read_b128 a[100:103], v213 offset:36864\n"
      "v_cvt_pk_bf16_f32 v188, v168, v169\n"
      "v_mfma_f32_16x16x32_bf16 v[164:167], a[128:131], a[48:51], v[164:167]\n"
      "v_exp_f32 v173, v173\n"
      "v_mfma_f32_16x16x32_bf16 v[64:67], a[144:147], v[180:183], v[64:67]\n"
      "ds_read_b128 a[136:139], v218 offset:36864\n"
      "v_cvt_pk_bf16_f32 v190, v172, v173\n"
      "v_mfma_f32_16x16x32_bf16 v[64:67], a[176:179], v[184:187], v[64:67]\n"
      "v_exp_f32 v174, v174\n"
      "v_mfma_f32_16x16x32_bf16 v[68:71], a[180:183], v[184:187], v[68:71]\n"
      "ds_read_b128 a[96:99], v212 offset:36864\n"
      "v_mfma_f32_16x16x32_bf16 v[68:71], a[148:151], v[180:183], v[68:71]\n"
      "v_exp_f32 v175, v175\n"
      "v_mfma_f32_16x16x32_bf16 v[72:75], a[152:155], v[180:183], v[72:75]\n"
      "buffer_load_dwordx4 v237, s[36:39], s101 offen offset:1024 lds\n"
      "v_mfma_f32_16x16x32_bf16 v[72:75], a[184:187], v[184:187], v[72:75]\n"
      "v_exp_f32 v160, v160\n"
      "v_mfma_f32_16x16x32_bf16 v[76:79], a[188:191], v[184:187], v[76:79]\n"
      "ds_read_b128 a[132:135], v217 offset:36864\n"
      "v_cvt_pk_bf16_f32 v191, v174, v175\n"
      "v_mfma_f32_16x16x32_bf16 v[76:79], a[156:159], v[180:183], v[76:79]\n"
      "s_waitcnt lgkmcnt(11)\n"
      "v_exp_f32 v161, v161\n"
      "L_ent1%=:\n"
      "v_mfma_f32_16x16x32_bf16 v[168:171], a[80:83], a[64:67], v[208:211]\n"
      "s_waitcnt lgkmcnt(6)\n"
      "ds_read_b128 a[124:127], v219 offset:32768\n"
      "v_cvt_pk_bf16_f32 v176, v160, v161\n"
      "v_mfma_f32_16x16x32_bf16 v[168:171], a[84:87], a[68:71], v[168:171]\n"
      "v_exp_f32 v162, v162\n"
      "v_mfma_f32_16x16x32_bf16 v[168:171], a[88:91], a[72:75], v[168:171]\n"
      "ds_read_b128 a[128:131], v216 offset:36864\n"
      "v_mfma_f32_16x16x32_bf16 v[168:171], a[92:95], a[76:79], v[168:171]\n"
      "v_exp_f32 v163, v163\n"
      "v_mfma_f32_16x16x32_bf16 v[172:175], a[108:111], a[76:79], v[208:211]\n"
      "ds_read_b128 a[120:123], v218 offset:32768\n"
      "v_cvt_pk_bf16_f32 v177, v162, v163\n"
      "v_mfma_f32_16x16x32_bf16 v[172:175], a[104:107], a[72:75], v[172:175]\n"
      "s_waitcnt lgkmcnt(6)\n"
      "v_exp_f32 v164, v164\n"
      "v_mfma_f32_16x16x32_bf16 v[172:175], a[100:103], a[68:71], v[172:175]\n"
      "s_waitcnt lgkmcnt(4)\n"
      "ds_read_b128 a[112:115], v216 offset:32768\n"
      "v_mfma_f32_16x16x32_bf16 v[172:175], a[96:99], a[64:67], v[172:175]\n"
      "v_exp_f32 v165, v165\n"
      "v_mfma_f32_16x16x32_bf16 v[80:83], a[160:163], v[180:183], v[80:83]\n"
      "v_cvt_pk_bf16_f32 v178, v164, v165\n"
      "v_mfma_f32_16x16x32_bf16 v[80:83], a[192:195], v[184:187], v[80:83]\n"
      "v_exp_f32 v166, v166\n"
      "v_mfma_f32_16x16x32_bf16 v[84:87], a[196:199], v[184:187], v[84:87]\n"
      "s_cmp_gt_u32 s69, 5\n"
      "s_cbranch_scc1 L_ev1%=\n"
      "v_mfma_f32_16x16x32_bf16 v[84:87], a[164:167], v[180:183], v[84:87]\n"
      "v_exp_f32 v167, v167\n"
      "v_mfma_f32_16x16x32_bf16 v[88:91], a[168:171], v[180:183], v[88:91]\n"
      "v_cvt_pk_bf16_f32 v179, v166, v167\n"
      "v_mfma_f32_16x16x32_bf16 v[88:91], a[200:203], v[184:187], v[88:91]\n"
      "v_exp_f32 v168, v168\n"
      "v_mfma_f32_16x16x32_bf16 v[92:95], a[204:207], v[184:187], v[92:95]\n"
      "buffer_load_dwordx4 v238, s[36:39], s101 offen offset:2048 lds\n"
      "v_mfma_f32_16x16x32_bf16 v[92:95], a[172:175], v[180:183], v[92:95]\n"
      "v_exp_f32 v169, v169\n"
      "v_mfma_f32_16x16x32_bf16 v[160:163], a[80:83], a[0:3], v[192:195]\n"
      "v_add_f32 v246, v246, v169\n"
      "v_cvt_pk_bf16_f32 v180, v168, v169\n"
      "v_mfma_f32_16x16x32_bf16 v[160:163], a[84:87], a[4:7], v[160:163]\n"
      "v_exp_f32 v170, v170\n"
      "v_mfma_f32_16x16x32_bf16 v[160:163], a[88:91], a[8:11], v[160:163]\n"
      "v_add_f32 v247, v247, v168\n"
      "v_add_f32 v246, v246, v170\n"
      "v_mfma_f32_16x16x32_bf16 v[160:163], a[92:95], a[12:15], v[160:163]\n"
      "v_exp_f32 v171, v171\n"
      "v_mfma_f32_16x16x32_bf16 v[164:167], a[108:111], a[12:15], v[192:195]\n"
      "v_add_f32 v247, v247, v171\n"
      "v_cvt_pk_bf16_f32 v181, v170, v171\n"
      "v_mfma_f32_16x16x32_bf16 v[164:167], a[104:107], a[8:11], v[164:167]\n"
      "v_exp_f32 v172, v172\n"
      "v_mfma_f32_16x16x32_bf16 v[164:167], a[100:103], a[4:7], v[164:167]\n"
      "v_add_f32 v246, v246, v172\n"
      "v_mfma_f32_16x16x32_bf16 v[164:167], a[96:99], a[0:3], v[164:167]\n"
      "v_exp_f32 v173, v173\n"
      "v_mfma_f32_16x16x32_bf16 v[96:99], a[144:147], v[188:191], v[96:99]\n"
      "v_add_f32 v247, v247, v173\n"
      "v_cvt_pk_bf16_f32 v182, v172, v173\n"
      "v_mfma_f32_16x16x32_bf16 v[96:99], a[176:179], v[176:179], v[96:99]\n"
      "v_exp_f32 v174, v174\n"
      "v_mfma_f32_16x16x32_bf16 v[100:103], a[180:183], v[176:179], v[100:103]\n"
      "ds_read_b64_tr_b16 a[146:147], v220 offset:36864\n"
      "ds_read_b64_tr_b16 a[182:183], v229 offset:36864\n"
      "v_mfma_f32_16x16x32_bf16 v[100:103], a[148:151], v[188:191], v[100:103]\n"
      "v_exp_f32 v175, v175\n"
      "v_mfma_f32_16x16x32_bf16 v[104:107], a[152:155], v[188:191], v[104:107]\n"
      "ds_read_b64_tr_b16 a[150:151], v221 offset:36864\n"
      "v_add_f32 v246, v246, v175\n"
      "v_mfma_f32_16x16x32_bf16 v[104:107], a[184:187], v[176:179], v[104:107]\n"
      "v_exp_f32 v160, v160\n"
      "v_mfma_f32_16x16x32_bf16 v[108:111], a[188:191], v[176:179], v[108:111]\n"
      "ds_read_b64_tr_b16 a[144:145], v220 offset:32768\n"
      "v_cvt_pk_bf16_f32 v183, v174, v175\n"
      "v_mfma_f32_16x16x32_bf16 v[108:111], a[156:159], v[188:191], v[108:111]\n"
      "s_waitcnt lgkmcnt(4)\n"
      "v_exp_f32 v161, v161\n"
      "v_mfma_f32_16x16x32_bf16 v[168:171], a[112:115], a[0:3], v[192:195]\n"
      "buffer_load_dwordx4 v239, s[36:39], s101 offen offset:3072 lds\n"
      "v_mfma_f32_16x16x32_bf16 v[168:171], a[116:119], a[4:7], v[168:171]\n"
      "v_exp_f32 v162, v162\n"
      "v_mfma_f32_16x16x32_bf16 v[168:171], a[120:123], a[8:11], v[168:171]\n"
      "v_add_f32 v247, v247, v174\n"
      "v_cvt_pk_bf16_f32 v184, v160, v161\n"
      "v_mfma_f32_16x16x32_bf16 v[168:171], a[124:127], a[12:15], v[168:171]\n"
      "v_exp_f32 v163, v163\n"
      "v_mfma_f32_16x16x32_bf16 v[172:175], a[140:143], a[12:15], v[192:195]\n"
      "ds_read_b64_tr_b16 a[148:149], v221 offset:32768\n"
      "ds_read_b64_tr_b16 a[154:155], v222 offset:36864\n"
      "v_mfma_f32_16x16x32_bf16 v[172:175], a[136:139], a[8:11], v[172:175]\n"
      "v_exp_f32 v164, v164\n"
      "v_mfma_f32_16x16x32_bf16 v[172:175], a[132:135], a[4:7], v[172:175]\n"
      "ds_read_b64_tr_b16 a[156:157], v223 offset:32768\n"
      "ds_read_b64_tr_b16 a[158:159], v223 offset:36864\n"
      "v_mfma_f32_16x16x32_bf16 v[172:175], a[128:131], a[0:3], v[172:175]\n"
      "v_exp_f32 v165, v165\n"
      "v_mfma_f32_16x16x32_bf16 v[112:115], a[160:163], v[188:191], v[112:115]\n"
      "v_cvt_pk_bf16_f32 v185, v162, v163\n"
      "v_cvt_pk_bf16_f32 v186, v164, v165\n"
      "v_mfma_f32_16x16x32_bf16 v[112:115], a[192:195], v[176:179], v[112:115]\n"
      "v_exp_f32 v166, v166\n"
      "v_mfma_f32_16x16x32_bf16 v[116:119], a[196:199], v[176:179], v[116:119]\n"
      "ds_read_b64_tr_b16 a[152:153], v222 offset:32768\n"
      "ds_read_b64_tr_b16 a[160:161], v224 offset:32768\n"
      "v_mfma_f32_16x16x32_bf16 v[116:119], a[164:167], v[188:191], v[116:119]\n"
      "v_exp_f32 v167, v167\n"
      "v_mfma_f32_16x16x32_bf16 v[120:123], a[168:171], v[188:191], v[120:123]\n"
      "ds_read_b64_tr_b16 a[162:163], v224 offset:36864\n"
      "ds_read_b64_tr_b16 a[170:171], v226 offset:36864\n"
      "v_mfma_f32_16x16x32_bf16 v[120:123], a[200:203], v[176:179], v[120:123]\n"
      "ds_read_b64_tr_b16 a[196:197], v233 offset:32768\n"
      "ds_read_b64_tr_b16 a[198:199], v233 offset:36864\n"
      "v_mfma_f32_16x16x32_bf16 v[124:127], a[204:207], v[176:179], v[124:127]\n"
      "ds_read_b64_tr_b16 a[204:205], v235 offset:32768\n"
      "ds_read_b64_tr_b16 a[206:207], v235 offset:36864\n"
      "v_mfma_f32_16x16x32_bf16 v[124:127], a[172:175], v[188:191], v[124:127]\n"
      "v_exp_f32 v168, v168\n"
      "v_mfma_f32_16x16x32_bf16 a[220:223], a[228:231], v[188:191], a[220:223]\n"
      "ds_read_b64_tr_b16 a[166:167], v225 offset:36864\n"
      "ds_read_b64_tr_b16 a[168:169], v226 offset:32768\n"
      "v_mfma_f32_16x16x32_bf16 a[220:223], a[228:231], v[176:179], a[220:223]\n"
      "s_mov_b32 m0, s58\n"
      "v_exp_f32 v169, v169\n"
      "v_mfma_f32_16x16x32_bf16 v[160:163], a[80:83], a[16:19], v[196:199]\n"
      "buffer_load_dwordx4 v240, s[40:43], s101 offen offset:0 lds\n"
      "v_mfma_f32_16x16x32_bf16 v[160:163], a[84:87], a[20:23], v[160:163]\n"
      "v_exp_f32 v170, v170\n"
      "v_mfma_f32_16x16x32_bf16 v[160:163], a[88:91], a[24:27], v[160:163]\n"
      "ds_read_b64_tr_b16 a[164:165], v225 offset:32768\n"
      "v_cvt_pk_bf16_f32 v187, v166, v167\n"
      "v_mfma_f32_16x16x32_bf16 v[160:163], a[92:95], a[28:31], v[160:163]\n"
      "v_exp_f32 v171, v171\n"
      "v_mfma_f32_16x16x32_bf16 v[164:167], a[108:111], a[28:31], v[196:199]\n"
      "ds_read_b64_tr_b16 a[172:173], v227 offset:32768\n"
      "ds_read_b64_tr_b16 a[174:175], v227 offset:36864\n"
      "v_mfma_f32_16x16x32_bf16 v[164:167], a[104:107], a[24:27], v[164:167]\n"
      "v_exp_f32 v172, v172\n"
      "v_mfma_f32_16x16x32_bf16 v[164:167], a[100:103], a[20:23], v[164:167]\n"
      "ds_read_b64_tr_b16 a[200:201], v234 offset:32768\n"
      "ds_read_b64_tr_b16 a[202:203], v234 offset:36864\n"
      "v_mfma_f32_16x16x32_bf16 v[164:167], a[96:99], a[16:19], v[164:167]\n"
      "s_waitcnt lgkmcnt(15)\n"
      "v_exp_f32 v173, v173\n"
      "v_mfma_f32_16x16x32_bf16 v[128:131], a[144:147], v[180:183], v[128:131]\n"
      "s_waitcnt lgkmcnt(15)\n"
      "ds_read_b64_tr_b16 a[186:187], v230 offset:36864\n"
      "ds_read_b64_tr_b16 a[194:195], v232 offset:36864\n"
      "v_mfma_f32_16x16x32_bf16 v[132:135], a[148:151], v[180:183], v[132:135]\n"
      "s_waitcnt lgkmcnt(15)\n"
      "v_exp_f32 v174, v174\n"
      "v_mfma_f32_16x16x32_bf16 v[136:139], a[152:155], v[180:183], v[136:139]\n"
      "ds_read_b64_tr_b16 a[176:177], v228 offset:32768\n"
      "ds_read_b64_tr_b16 a[184:185], v230 offset:32768\n"
      "v_mfma_f32_16x16x32_bf16 v[140:143], a[156:159], v[180:183], v[140:143]\n"
      "s_waitcnt lgkmcnt(15)\n"
      "v_exp_f32 v175, v175\n"
      "v_mfma_f32_16x16x32_bf16 v[144:147], a[160:163], v[180:183], v[144:147]\n"
      "s_waitcnt lgkmcnt(8)\n"
      "ds_read_b64_tr_b16 a[178:179], v228 offset:36864\n"
      "ds_read_b64_tr_b16 a[180:181], v229 offset:32768\n"
      "v_mfma_f32_16x16x32_bf16 v[148:151], a[164:167], v[180:183], v[148:151]\n"
      "v_exp_f32 v160, v160\n"
      "v_mfma_f32_16x16x32_bf16 v[152:155], a[168:171], v[180:183], v[152:155]\n"
      "s_waitcnt lgkmcnt(8)\n"
      "v_cvt_pk_bf16_f32 v188, v168, v169\n"
      "v_cvt_pk_bf16_f32 v189, v170, v171\n"
      "v_mfma_f32_16x16x32_bf16 v[156:159], a[172:175], v[180:183], v[156:159]\n"
      "v_exp_f32 v161, v161\n"
      "v_mfma_f32_16x16x32_bf16 v[168:171], a[112:115], a[16:19], v[196:199]\n"
      "v_cvt_pk_bf16_f32 v190, v172, v173\n"
      "v_cvt_pk_bf16_f32 v191, v174, v175\n"
      "v_mfma_f32_16x16x32_bf16 v[168:171], a[116:119], a[20:23], v[168:171]\n"
      "v_exp_f32 v162, v162\n"
      "v_mfma_f32_16x16x32_bf16 v[168:171], a[120:123], a[24:27], v[168:171]\n"
      "buffer_load_dwordx4 v241, s[40:43], s101 offen offset:1024 nt lds\n"
      "v_mfma_f32_16x16x32_bf16 v[168:171], a[124:127], a[28:31], v[168:171]\n"
      "v_exp_f32 v163, v163\n"
      "v_mfma_f32_16x16x32_bf16 v[172:175], a[140:143], a[28:31], v[196:199]\n"
      "ds_read_b64_tr_b16 a[188:189], v231 offset:32768\n"
      "ds_read_b64_tr_b16 a[190:191], v231 offset:36864\n"
      "v_mfma_f32_16x16x32_bf16 v[172:175], a[136:139], a[24:27], v[172:175]\n"
      "v_exp_f32 v164, v164\n"
      "v_mfma_f32_16x16x32_bf16 v[172:175], a[132:135], a[20:23], v[172:175]\n"
      "ds_read_b64_tr_b16 a[192:193], v232 offset:32768\n"
      "v_add_f32 v242, v242, v160\n"
      "v_mfma_f32_16x16x32_bf16 v[172:175], a[128:131], a[16:19], v[172:175]\n"
      "v_exp_f32 v165, v165\n"
      "v_mfma_f32_16x16x32_bf16 v[0:3], a[144:147], v[184:187], v[0:3]\n"
      "s_waitcnt lgkmcnt(4)\n"
      "v_add_f32 v243, v243, v161\n"
      "v_cvt_pk_bf16_f32 v176, v160, v161\n"
      "v_mfma_f32_16x16x32_bf16 v[0:3], a[176:179], v[188:191], v[0:3]\n"
      "s_waitcnt lgkmcnt(3)\n"
      "v_exp_f32 v166, v166\n"
      "v_mfma_f32_16x16x32_bf16 v[4:7], a[180:183], v[188:191], v[4:7]\n"
      "v_add_f32 v242, v242, v163\n"
      "v_cvt_pk_bf16_f32 v177, v162, v163\n"
      "v_mfma_f32_16x16x32_bf16 v[4:7], a[148:151], v[184:187], v[4:7]\n"
      "v_exp_f32 v167, v167\n"
      "v_mfma_f32_16x16x32_bf16 v[8:11], a[152:155], v[184:187], v[8:11]\n"
      "v_add_f32 v243, v243, v162\n"
      "v_add_f32 v242, v242, v164\n"
      "v_mfma_f32_16x16x32_bf16 v[8:11], a[184:187], v[188:191], v[8:11]\n"
      "s_waitcnt lgkmcnt(1)\n"
      "v_exp_f32 v168, v168\n"
      "v_mfma_f32_16x16x32_bf16 v[12:15], a[188:191], v[188:191], v[12:15]\n"
      "v_add_f32 v243, v243, v165\n"
      "v_cvt_pk_bf16_f32 v178, v164, v165\n"
      "v_mfma_f32_16x16x32_bf16 v[12:15], a[156:159], v[184:187], v[12:15]\n"
      "v_exp_f32 v169, v169\n"
      "v_mfma_f32_16x16x32_bf16 v[160:163], a[80:83], a[32:35], v[200:203]\n"
      "v_add_f32 v242, v242, v166\n"
      "v_cvt_pk_bf16_f32 v179, v166, v167\n"
      "v_mfma_f32_16x16x32_bf16 v[160:163], a[84:87], a[36:39], v[160:163]\n"
      "v_exp_f32 v170, v170\n"
      "v_mfma_f32_16x16x32_bf16 v[160:163], a[88:91], a[40:43], v[160:163]\n"
      "v_add_f32 v243, v243, v167\n"
      "v_add_f32 v242, v242, v168\n"
      "v_mfma_f32_16x16x32_bf16 v[160:163], a[92:95], a[44:47], v[160:163]\n"
      "v_exp_f32 v171, v171\n"
      "v_mfma_f32_16x16x32_bf16 v[164:167], a[108:111], a[44:47], v[200:203]\n"
      "buffer_load_dwordx4 v240, s[40:43], s101 offen offset:2048 lds\n"
      "v_mfma_f32_16x16x32_bf16 v[164:167], a[104:107], a[40:43], v[164:167]\n"
      "v_exp_f32 v172, v172\n"
      "v_mfma_f32_16x16x32_bf16 v[164:167], a[100:103], a[36:39], v[164:167]\n"
      "v_add_f32 v243, v243, v169\n"
      "v_cvt_pk_bf16_f32 v180, v168, v169\n"
      "v_mfma_f32_16x16x32_bf16 v[164:167], a[96:99], a[32:35], v[164:167]\n"
      "v_exp_f32 v173, v173\n"
      "v_mfma_f32_16x16x32_bf16 v[16:19], a[160:163], v[184:187], v[16:19]\n"
      "s_waitcnt lgkmcnt(0)\n"
      "v_add_f32 v242, v242, v170\n"
      "v_add_f32 v243, v243, v171\n"
      "v_mfma_f32_16x16x32_bf16 v[16:19], a[192:195], v[188:191], v[16:19]\n"
      "v_exp_f32 v174, v174\n"
      "v_mfma_f32_16x16x32_bf16 v[20:23], a[196:199], v[188:191], v[20:23]\n"
      "v_cvt_pk_bf16_f32 v181, v170, v171\n"
      "v_cvt_pk_bf16_f32 v182, v172, v173\n"
      "v_mfma_f32_16x16x32_bf16 v[20:23], a[164:167], v[184:187], v[20:23]\n"
      "v_exp_f32 v175, v175\n"
      "v_mfma_f32_16x16x32_bf16 v[24:27], a[168:171], v[184:187], v[24:27]\n"
      "v_add_f32 v242, v242, v172\n"
      "v_add_f32 v243, v243, v173\n"
      "v_mfma_f32_16x16x32_bf16 v[24:27], a[200:203], v[188:191], v[24:27]\n"
      "v_add_f32 v242, v242, v174\n"
      "v_cvt_pk_bf16_f32 v183, v174, v175\n"
      "v_mfma_f32_16x16x32_bf16 v[28:31], a[204:207], v[188:191], v[28:31]\n"
      "v_add_f32 v243, v243, v175\n"
      "v_mfma_f32_16x16x32_bf16 v[28:31], a[172:175], v[184:187], v[28:31]\n"
      "v_exp_f32 v160, v160\n"
      "v_mfma_f32_16x16x32_bf16 a[208:211], a[228:231], v[184:187], a[208:211]\n"
      "v_add_f32 v244, v244, v160\n"
      "v_mfma_f32_16x16x32_bf16 a[208:211], a[228:231], v[188:191], a[208:211]\n"
      "v_exp_f32 v161, v161\n"
      "v_mfma_f32_16x16x32_bf16 v[168:171], a[112:115], a[32:35], v[200:203]\n"
      "v_add_f32 v245, v245, v161\n"
      "v_cvt_pk_bf16_f32 v184, v160, v161\n"
      "v_mfma_f32_16x16x32_bf16 v[168:171], a[116:119], a[36:39], v[168:171]\n"
      "v_exp_f32 v162, v162\n"
      "v_mfma_f32_16x16x32_bf16 v[168:171], a[120:123], a[40:43], v[168:171]\n"
      "v_add_f32 v244, v244, v162\n"
      "v_mfma_f32_16x16x32_bf16 v[168:171], a[124:127], a[44:47], v[168:171]\n"
      "v_exp_f32 v163, v163\n"
      "v_mfma_f32_16x16x32_bf16 v[172:175], a[140:143], a[44:47], v[200:203]\n"
      "buffer_load_dwordx4 v241, s[40:43], s101 offen offset:3072 nt lds\n"
      "v_mfma_f32_16x16x32_bf16 v[172:175], a[136:139], a[40:43], v[172:175]\n"
      "s_add_u32 s52, s52, 0x100\n"
      "v_exp_f32 v164, v164\n"
      "v_mfma_f32_16x16x32_bf16 v[172:175], a[132:135], a[36:39], v[172:175]\n"
      "s_or_b32 s52, s52, 0x1bc00\n"
      "v_add_f32 v245, v245, v163\n"
      "v_cvt_pk_bf16_f32 v185, v162, v163\n"
      "v_mfma_f32_16x16x32_bf16 v[172:175], a[128:131], a[32:35], v[172:175]\n"
      "v_exp_f32 v165, v165\n"
      "v_mfma_f32_16x16x32_bf16 v[32:35], a[144:147], v[176:179], v[32:35]\n"
      "s_xor_b32 s101, s52, s100\n"
      "v_add_f32 v244, v244, v164\n"
      "v_cvt_pk_bf16_f32 v186, v164, v165\n"
      "v_mfma_f32_16x16x32_bf16 v[32:35], a[176:179], v[180:183], v[32:35]\n"
      "v_exp_f32 v166, v166\n"
      "v_mfma_f32_16x16x32_bf16 v[36:39], a[180:183], v[180:183], v[36:39]\n"
      "v_add_f32 v245, v245, v165\n"
      "v_add_f32 v244, v244, v166\n"
      "v_mfma_f32_16x16x32_bf16 v[36:39], a[148:151], v[176:179], v[36:39]\n"
      "v_exp_f32 v167, v167\n"
      "v_mfma_f32_16x16x32_bf16 v[40:43], a[152:155], v[176:179], v[40:43]\n"
      "v_add_f32 v245, v245, v167\n"
      "v_cvt_pk_bf16_f32 v187, v166, v167\n"
      "v_mfma_f32_16x16x32_bf16 v[40:43], a[184:187], v[180:183], v[40:43]\n"
      "v_exp_f32 v168, v168\n"
      "v_mfma_f32_16x16x32_bf16 v[44:47], a[188:191], v[180:183], v[44:47]\n"
      "s_getreg_b32 s69, hwreg(7, 0, 4)\n"
      "v_add_f32 v244, v244, v168\n"
      "v_mfma_f32_16x16x32_bf16 v[44:47], a[156:159], v[176:179], v[44:47]\n"
      "s_waitcnt vmcnt(8) lgkmcnt(0)\n"
      "s_barrier\n"
      "v_exp_f32 v169, v169\n"
      "v_mfma_f32_16x16x32_bf16 v[160:163], a[80:83], a[48:51], v[204:207]\n"
      "ds_read_b128 a[80:83], v212 offset:49152\n"
      "v_add_f32 v245, v245, v169\n"
      "v_mfma_f32_16x16x32_bf16 v[160:163], a[84:87], a[52:55], v[160:163]\n"
      "v_exp_f32 v170, v170\n"
      "v_mfma_f32_16x16x32_bf16 v[160:163], a[88:91], a[56:59], v[160:163]\n"
      "v_add_f32 v244, v244, v170\n"
      "v_cvt_pk_bf16_f32 v188, v168, v169\n"
      "v_mfma_f32_16x16x32_bf16 v[160:163], a[92:95], a[60:63], v[160:163]\n"
      "v_exp_f32 v171, v171\n"
      "v_mfma_f32_16x16x32_bf16 v[164:167], a[108:111], a[60:63], v[204:207]\n"
      "ds_read_b128 a[108:111], v215 offset:53248\n"
      "v_add_f32 v245, v245, v171\n"
      "v_mfma_f32_16x16x32_bf16 v[164:167], a[104:107], a[56:59], v[164:167]\n"
      "v_exp_f32 v172, v172\n"
      "v_mfma_f32_16x16x32_bf16 v[164:167], a[100:103], a[52:55], v[164:167]\n"
      "ds_read_b128 a[88:91], v214 offset:49152\n"
      "v_cvt_pk_bf16_f32 v189, v170, v171\n"
      "v_mfma_f32_16x16x32_bf16 v[164:167], a[96:99], a[48:51], v[164:167]\n"
      "v_exp_f32 v173, v173\n"
      "v_mfma_f32_16x16x32_bf16 v[48:51], a[160:163], v[176:179], v[48:51]\n"
      "ds_read_b128 a[104:107], v214 offset:53248\n"
      "v_add_f32 v244, v244, v173\n"
      "v_mfma_f32_16x16x32_bf16 v[48:51], a[192:195], v[180:183], v[48:51]\n"
      "s_mov_b32 m0, s55\n"
      "v_exp_f32 v174, v174\n"
      "v_mfma_f32_16x16x32_bf16 v[52:55], a[196:199], v[180:183], v[52:55]\n"
      "buffer_load_dwordx4 v236, s[36:39], s101 offen offset:0 lds\n"
      "v_mfma_f32_16x16x32_bf16 v[52:55], a[164:167], v[176:179], v[52:55]\n"
      "v_exp_f32 v175, v175\n"
      "v_mfma_f32_16x16x32_bf16 v[56:59], a[168:171], v[176:179], v[56:59]\n"
      "ds_read_b128 a[92:95], v215 offset:49152\n"
      "v_add_f32 v245, v245, v172\n"
      "v_mfma_f32_16x16x32_bf16 v[56:59], a[200:203], v[180:183], v[56:59]\n"
      "v_exp_f32 v160, v160\n"
      "v_mfma_f32_16x16x32_bf16 v[60:63], a[204:207], v[180:183], v[60:63]\n"
      "ds_read_b128 a[84:87], v213 offset:49152\n"
      "v_cvt_pk_bf16_f32 v190, v172, v173\n"
      "v_mfma_f32_16x16x32_bf16 v[60:63], a[172:175], v[176:179], v[60:63]\n"
      "v_exp_f32 v161, v161\n"
      "v_mfma_f32_16x16x32_bf16 v[168:171], a[112:115], a[48:51], v[204:207]\n"
      "v_add_f32 v244, v244, v175\n"
      "v_cvt_pk_bf16_f32 v191, v174, v175\n"
      "v_mfma_f32_16x16x32_bf16 v[168:171], a[116:119], a[52:55], v[168:171]\n"
      "v_exp_f32 v162, v162\n"
      "v_mfma_f32_16x16x32_bf16 v[168:171], a[120:123], a[56:59], v[168:171]\n"
      "ds_read_b128 a[116:119], v217 offset:49152\n"
      "v_add_f32 v245, v245, v174\n"
      "v_mfma_f32_16x16x32_bf16 v[168:171], a[124:127], a[60:63], v[168:171]\n"
      "v_exp_f32 v163, v163\n"
      "v_mfma_f32_16x16x32_bf16 v[172:175], a[140:143], a[60:63], v[204:207]\n"
      "ds_read_b128 a[140:143], v219 offset:53248\n"
      "v_cvt_pk_bf16_f32 v177, v162, v163\n"
      "v_mfma_f32_16x16x32_bf16 v[172:175], a[136:139], a[56:59], v[172:175]\n"
      "v_exp_f32 v164, v164\n"
      "v_mfma_f32_16x16x32_bf16 v[172:175], a[132:135], a[52:55], v[172:175]\n"
      "ds_read_b128 a[100:103], v213 offset:53248\n"
      "v_cvt_pk_bf16_f32 v176, v160, v161\n"
      "v_mfma_f32_16x16x32_bf16 v[172:175], a[128:131], a[48:51], v[172:175]\n"
      "v_exp_f32 v165, v165\n"
      "v_mfma_f32_16x16x32_bf16 v[64:67], a[144:147], v[184:187], v[64:67]\n"
      "ds_read_b128 a[136:139], v218 offset:53248\n"
      "v_cvt_pk_bf16_f32 v178, v164, v165\n"
      "v_mfma_f32_16x16x32_bf16 v[64:67], a[176:179], v[188:191], v[64:67]\n"
      "v_exp_f32 v166, v166\n"
      "v_mfma_f32_16x16x32_bf16 v[68:71], a[180:183], v[188:191], v[68:71]\n"
      "ds_read_b128 a[96:99], v212 offset:53248\n"
      "v_mfma_f32_16x16x32_bf16 v[68:71], a[148:151], v[184:187], v[68:71]\n"
      "v_exp_f32 v167, v167\n"
      "v_mfma_f32_16x16x32_bf16 v[72:75], a[152:155], v[184:187], v[72:75]\n"
      "buffer_load_dwordx4 v237, s[36:39], s101 offen offset:1024 lds\n"
      "v_mfma_f32_16x16x32_bf16 v[72:75], a[184:187], v[188:191], v[72:75]\n"
      "v_exp_f32 v168, v168\n"
      "v_mfma_f32_16x16x32_bf16 v[76:79], a[188:191], v[188:191], v[76:79]\n"
      "ds_read_b128 a[132:135], v217 offset:53248\n"
      "v_cvt_pk_bf16_f32 v179, v166, v167\n"
      "v_mfma_f32_16x16x32_bf16 v[76:79], a[156:159], v[184:187], v[76:79]\n"
      "s_waitcnt lgkmcnt(11)\n"
      "v_exp_f32 v169, v169\n"
      "L_ent2%=:\n"
      "v_mfma_f32_16x16x32_bf16 v[160:163], a[80:83], a[64:67], v[208:211]\n"
      "s_waitcnt lgkmcnt(6)\n"
      "ds_read_b128 a[124:127], v219 offset:49152\n"
      "v_cvt_pk_bf16_f32 v180, v168, v169\n"
      "v_mfma_f32_16x16x32_bf16 v[160:163], a[84:87], a[68:71], v[160:163]\n"
      "v_exp_f32 v170, v170\n"
      "v_mfma_f32_16x16x32_bf16 v[160:163], a[88:91], a[72:75], v[160:163]\n"
      "ds_read_b128 a[128:131], v216 offset:53248\n"
      "v_mfma_f32_16x16x32_bf16 v[160:163], a[92:95], a[76:79], v[160:163]\n"
      "v_exp_f32 v171, v171\n"
      "v_mfma_f32_16x16x32_bf16 v[164:167], a[108:111], a[76:79], v[208:211]\n"
      "ds_read_b128 a[120:123], v218 offset:49152\n"
      "v_cvt_pk_bf16_f32 v181, v170, v171\n"
      "v_mfma_f32_16x16x32_bf16 v[164:167], a[104:107], a[72:75], v[164:167]\n"
      "s_waitcnt lgkmcnt(6)\n"
      "v_exp_f32 v172, v172\n"
      "v_mfma_f32_16x16x32_bf16 v[164:167], a[100:103], a[68:71], v[164:167]\n"
      "s_waitcnt lgkmcnt(4)\n"
      "ds_read_b128 a[112:115], v216 offset:49152\n"
      "v_mfma_f32_16x16x32_bf16 v[164:167], a[96:99], a[64:67], v[164:167]\n"
      "v_exp_f32 v173, v173\n"
      "v_mfma_f32_16x16x32_bf16 v[80:83], a[160:163], v[184:187], v[80:83]\n"
      "v_cvt_pk_bf16_f32 v182, v172, v173\n"
      "v_mfma_f32_16x16x32_bf16 v[80:83], a[192:195], v[188:191], v[80:83]\n"
      "v_exp_f32 v174, v174\n"
      "v_mfma_f32_16x16x32_bf16 v[84:87], a[196:199], v[188:191], v[84:87]\n"
      "s_cmp_gt_u32 s69, 5\n"
      "s_cbranch_scc1 L_ev2%=\n"
      "v_mfma_f32_16x16x32_bf16 v[84:87], a[164:167], v[184:187], v[84:87]\n"
      "v_exp_f32 v175, v175\n"
      "v_mfma_f32_16x16x32_bf16 v[88:91], a[168:171], v[184:187], v[88:91]\n"
      "v_cvt_pk_bf16_f32 v183, v174, v175\n"
      "v_mfma_f32_16x16x32_bf16 v[88:91], a[200:203], v[188:191], v[88:91]\n"
      "v_exp_f32 v160, v160\n"
      "v_mfma_f32_16x16x32_bf16 v[92:95], a[204:207], v[188:191], v[92:95]\n"
      "buffer_load_dwordx4 v238, s[36:39], s101 offen offset:2048 lds\n"
      "v_mfma_f32_16x16x32_bf16 v[92:95], a[172:175], v[184:187], v[92:95]\n"
      "v_exp_f32 v161, v161\n"
      "v_mfma_f32_16x16x32_bf16 v[168:171], a[80:83], a[0:3], v[192:195]\n"
      "v_add_f32 v246, v246, v161\n"
      "v_cvt_pk_bf16_f32 v184, v160, v161\n"
      "v_mfma_f32_16x16x32_bf16 v[168:171], a[84:87], a[4:7], v[168:171]\n"
      "v_exp_f32 v162, v162\n"
      "v_mfma_f32_16x16x32_bf16 v[168:171], a[88:91], a[8:11], v[168:171]\n"
      "v_add_f32 v247, v247, v160\n"
      "v_add_f32 v246, v246, v162\n"
      "v_mfma_f32_16x16x32_bf16 v[168:171], a[92:95], a[12:15], v[168:171]\n"
      "v_exp_f32 v163, v163\n"
      "v_mfma_f32_16x16x32_bf16 v[172:175], a[108:111], a[12:15], v[192:195]\n"
      "v_add_f32 v247, v247, v163\n"
      "v_cvt_pk_bf16_f32 v185, v162, v163\n"
      "v_mfma_f32_16x16x32_bf16 v[172:175], a[104:107], a[8:11], v[172:175]\n"
      "v_exp_f32 v164, v164\n"
      "v_mfma_f32_16x16x32_bf16 v[172:175], a[100:103], a[4:7], v[172:175]\n"
      "v_add_f32 v246, v246, v164\n"
      "v_mfma_f32_16x16x32_bf16 v[172:175], a[96:99], a[0:3], v[172:175]\n"
      "v_exp_f32 v165, v165\n"
      "v_mfma_f32_16x16x32_bf16 v[96:99], a[144:147], v[176:179], v[96:99]\n"
      "v_add_f32 v247, v247, v165\n"
      "v_cvt_pk_bf16_f32 v186, v164, v165\n"
      "v_mfma_f32_16x16x32_bf16 v[96:99], a[176:179], v[180:183], v[96:99]\n"
      "v_exp_f32 v166, v166\n"
      "v_mfma_f32_16x16x32_bf16 v[100:103], a[180:183], v[180:183], v[100:103]\n"
      "ds_read_b64_tr_b16 a[146:147], v220 offset:53248\n"
      "ds_read_b64_tr_b16 a[182:183], v229 offset:53248\n"
      "v_mfma_f32_16x16x32_bf16 v[100:103], a[148:151], v[176:179], v[100:103]\n"
      "v_exp_f32 v167, v167\n"
      "v_mfma_f32_16x16x32_bf16 v[104:107], a[152:155], v[176:179], v[104:107]\n"
      "ds_read_b64_tr_b16 a[150:151], v221 offset:53248\n"
      "v_add_f32 v246, v246, v167\n"
      "v_mfma_f32_16x16x32_bf16 v[104:107], a[184:187], v[180:183], v[104:107]\n"
      "v_exp_f32 v168, v168\n"
      "v_mfma_f32_16x16x32_bf16 v[108:111], a[188:191], v[180:183], v[108:111]\n"
      "ds_read_b64_tr_b16 a[144:145], v220 offset:49152\n"
      "v_cvt_pk_bf16_f32 v187, v166, v167\n"
      "v_mfma_f32_16x16x32_bf16 v[108:111], a[156:159], v[176:179], v[108:111]\n"
      "s_waitcnt lgkmcnt(4)\n"
      "v_exp_f32 v169, v169\n"
      "v_mfma_f32_16x16x32_bf16 v[160:163], a[112:115], a[0:3], v[192:195]\n"
      "buffer_load_dwordx4 v239, s[36:39], s101 offen offset:3072 lds\n"
      "v_mfma_f32_16x16x32_bf16 v[160:163], a[116:119], a[4:7], v[160:163]\n"
      "v_exp_f32 v170, v170\n"
      "v_mfma_f32_16x16x32_bf16 v[160:163], a[120:123], a[8:11], v[160:163]\n"
      "v_add_f32 v247, v247, v166\n"
      "v_cvt_pk_bf16_f32 v188, v168, v169\n"
      "v_mfma_f32_16x16x32_bf16 v[160:163], a[124:127], a[12:15], v[160:163]\n"
      "v_exp_f32 v171, v171\n"
      "v_mfma_f32_16x16x32_bf16 v[164:167], a[140:143], a[12:15], v[192:195]\n"
      "ds_read_b64_tr_b16 a[148:149], v221 offset:49152\n"
      "ds_read_b64_tr_b16 a[154:155], v222 offset:53248\n"
      "v_mfma_f32_16x16x32_bf16 v[164:167], a[136:139], a[8:11], v[164:167]\n"
      "v_exp_f32 v172, v172\n"
      "v_mfma_f32_16x16x32_bf16 v[164:167], a[132:135], a[4:7], v[164:167]\n"
      "ds_read_b64_tr_b16 a[156:157], v223 offset:49152\n"
      "ds_read_b64_tr_b16 a[158:159], v223 offset:53248\n"
      "v_mfma_f32_16x16x32_bf16 v[164:167], a[128:131], a[0:3], v[164:167]\n"
      "v_exp_f32 v173, v173\n"
      "v_mfma_f32_16x16x32_bf16 v[112:115], a[160:163], v[176:179], v[112:115]\n"
      "v_cvt_pk_bf16_f32 v189, v170, v171\n"
      "v_cvt_pk_bf16_f32 v190, v172, v173\n"
      "v_mfma_f32_16x16x32_bf16 v[112:115], a[192:195], v[180:183], v[112:115]\n"
      "v_exp_f32 v174, v174\n"
      "v_mfma_f32_16x16x32_bf16 v[116:119], a[196:199], v[180:183], v[116:119]\n"
      "ds_read_b64_tr_b16 a[152:153], v222 offset:49152\n"
      "ds_read_b64_tr_b16 a[160:161], v224 offset:49152\n"
      "v_mfma_f32_16x16x32_bf16 v[116:119], a[164:167], v[176:179], v[116:119]\n"
      "v_exp_f32 v175, v175\n"
      "v_mfma_f32_16x16x32_bf16 v[120:123], a[168:171], v[176:179], v[120:123]\n"
      "ds_read_b64_tr_b16 a[162:163], v224 offset:53248\n"
      "ds_read_b64_tr_b16 a[170:171], v226 offset:53248\n"
      "v_mfma_f32_16x16x32_bf16 v[120:123], a[200:203], v[180:183], v[120:123]\n"
      "ds_read_b64_tr_b16 a[196:197], v233 offset:49152\n"
      "ds_read_b64_tr_b16 a[198:199], v233 offset:53248\n"
      "v_mfma_f32_16x16x32_bf16 v[124:127], a[204:207], v[180:183], v[124:127]\n"
      "ds_read_b64_tr_b16 a[204:205], v235 offset:49152\n"
      "ds_read_b64_tr_b16 a[206:207], v235 offset:53248\n"
      "v_mfma_f32_16x16x32_bf16 v[124:127], a[172:175], v[176:179], v[124:127]\n"
      "v_exp_f32 v160, v160\n"
      "v_mfma_f32_16x16x32_bf16 a[220:223], a[228:231], v[176:179], a[220:223]\n"
      "ds_read_b64_tr_b16 a[166:167], v225 offset:53248\n"
      "ds_read_b64_tr_b16 a[168:169], v226 offset:49152\n"
      "v_mfma_f32_16x16x32_bf16 a[220:223], a[228:231], v[180:183], a[220:223]\n"
      "s_mov_b32 m0, s59\n"
      "v_exp_f32 v161, v161\n"
      "v_mfma_f32_16x16x32_bf16 v[168:171], a[80:83], a[16:19], v[196:199]\n"
      "buffer_load_dwordx4 v240, s[40:43], s101 offen offset:0 lds\n"
      "v_mfma_f32_16x16x32_bf16 v[168:171], a[84:87], a[20:23], v[168:171]\n"
      "v_exp_f32 v162, v162\n"
      "v_mfma_f32_16x16x32_bf16 v[168:171], a[88:91], a[24:27], v[168:171]\n"
      "ds_read_b64_tr_b16 a[164:165], v225 offset:49152\n"
      "v_cvt_pk_bf16_f32 v191, v174, v175\n"
      "v_mfma_f32_16x16x32_bf16 v[168:171], a[92:95], a[28:31], v[168:171]\n"
      "v_exp_f32 v163, v163\n"
      "v_mfma_f32_16x16x32_bf16 v[172:175], a[108:111], a[28:31], v[196:199]\n"
      "ds_read_b64_tr_b16 a[172:173], v227 offset:49152\n"
      "ds_read_b64_tr_b16 a[174:175], v227 offset:53248\n"
      "v_mfma_f32_16x16x32_bf16 v[172:175], a[104:107], a[24:27], v[172:175]\n"
      "v_exp_f32 v164, v164\n"
      "v_mfma_f32_16x16x32_bf16 v[172:175], a[100:103], a[20:23], v[172:175]\n"
      "ds_read_b64_tr_b16 a[200:201], v234 offset:49152\n"
      "ds_read_b64_tr_b16 a[202:203], v234 offset:53248\n"
      "v_mfma_f32_16x16x32_bf16 v[172:175], a[96:99], a[16:19], v[172:175]\n"
      "s_waitcnt lgkmcnt(15)\n"
      "v_exp_f32 v165, v165\n"
      "v_mfma_f32_16x16x32_bf16 v[128:131], a[144:147], v[184:187], v[128:131]\n"
      "s_waitcnt lgkmcnt(15)\n"
      "ds_read_b64_tr_b16 a[186:187], v230 offset:53248\n"
      "ds_read_b64_tr_b16 a[194:195], v232 offset:53248\n"
      "v_mfma_f32_16x16x32_bf16 v[132:135], a[148:151], v[184:187], v[132:135]\n"
      "s_waitcnt lgkmcnt(15)\n"
      "v_exp_f32 v166, v166\n"
      "v_mfma_f32_16x16x32_bf16 v[136:139], a[152:155], v[184:187], v[136:139]\n"
      "ds_read_b64_tr_b16 a[176:177], v228 offset:49152\n"
      "ds_read_b64_tr_b16 a[184:185], v230 offset:49152\n"
      "v_mfma_f32_16x16x32_bf16 v[140:143], a[156:159], v[184:187], v[140:143]\n"
      "s_waitcnt lgkmcnt(15)\n"
      "v_exp_f32 v167, v167\n"
      "v_mfma_f32_16x16x32_bf16 v[144:147], a[160:163], v[184:187], v[144:147]\n"
      "s_waitcnt lgkmcnt(8)\n"
      "ds_read_b64_tr_b16 a[178:179], v228 offset:53248\n"
      "ds_read_b64_tr_b16 a[180:181], v229 offset:49152\n"
      "v_mfma_f32_16x16x32_bf16 v[148:151], a[164:167], v[184:187], v[148:151]\n"
      "v_exp_f32 v168, v168\n"
      "v_mfma_f32_16x16x32_bf16 v[152:155], a[168:171], v[184:187], v[152:155]\n"
      "s_waitcnt lgkmcnt(8)\n"
      "v_cvt_pk_bf16_f32 v176, v160, v161\n"
      "v_cvt_pk_bf16_f32 v177, v162, v163\n"
      "v_mfma_f32_16x16x32_bf16 v[156:159], a[172:175], v[184:187], v[156:159]\n"
      "v_exp_f32 v169, v169\n"
      "v_mfma_f32_16x16x32_bf16 v[160:163], a[112:115], a[16:19], v[196:199]\n"
      "v_cvt_pk_bf16_f32 v178, v164, v165\n"
      "v_cvt_pk_bf16_f32 v179, v166, v167\n"
      "v_mfma_f32_16x16x32_bf16 v[160:163], a[116:119], a[20:23], v[160:163]\n"
      "v_exp_f32 v170, v170\n"
      "v_mfma_f32_16x16x32_bf16 v[160:163], a[120:123], a[24:27], v[160:163]\n"
      "buffer_load_dwordx4 v241, s[40:43], s101 offen offset:1024 nt lds\n"
      "v_mfma_f32_16x16x32_bf16 v[160:163], a[124:127], a[28:31], v[160:163]\n"
      "v_exp_f32 v171, v171\n"
      "v_mfma_f32_16x16x32_bf16 v[164:167], a[140:143], a[28:31], v[196:199]\n"
      "ds_read_b64_tr_b16 a[188:189], v231 offset:49152\n"
      "ds_read_b64_tr_b16 a[190:191], v231 offset:53248\n"
      "v_mfma_f32_16x16x32_bf16 v[164:167], a[136:139], a[24:27], v[164:167]\n"
      "v_exp_f32 v172, v172\n"
      "v_mfma_f32_16x16x32_bf16 v[164:167], a[132:135], a[20:23], v[164:167]\n"
      "ds_read_b64_tr_b16 a[192:193], v232 offset:49152\n"
      "v_add_f32 v242, v242, v168\n"
      "v_mfma_f32_16x16x32_bf16 v[164:167], a[128:131], a[16:19], v[164:167]\n"
      "v_exp_f32 v173, v173\n"
      "v_mfma_f32_16x16x32_bf16 v[0:3], a[144:147], v[188:191], v[0:3]\n"
      "s_waitcnt lgkmcnt(4)\n"
      "v_add_f32 v243, v243, v169\n"
      "v_cvt_pk_bf16_f32 v180, v168, v169\n"
      "v_mfma_f32_16x16x32_bf16 v[0:3], a[176:179], v[176:179], v[0:3]\n"
      "s_waitcnt lgkmcnt(3)\n"
      "v_exp_f32 v174, v174\n"
      "v_mfma_f32_16x16x32_bf16 v[4:7], a[180:183], v[176:179], v[4:7]\n"
      "v_add_f32 v242, v242, v171\n"
      "v_cvt_pk_bf16_f32 v181, v170, v171\n"
      "v_mfma_f32_16x16x32_bf16 v[4:7], a[148:151], v[188:191], v[4:7]\n"
      "v_exp_f32 v175, v175\n"
      "v_mfma_f32_16x16x32_bf16 v[8:11], a[152:155], v[188:191], v[8:11]\n"
      "v_add_f32 v243, v243, v170\n"
      "v_add_f32 v242, v242, v172\n"
      "v_mfma_f32_16x16x32_bf16 v[8:11], a[184:187], v[176:179], v[8:11]\n"
      "s_waitcnt lgkmcnt(1)\n"
      "v_exp_f32 v160, v160\n"
      "v_mfma_f32_16x16x32_bf16 v[12:15], a[188:191], v[176:179], v[12:15]\n"
      "v_add_f32 v243, v243, v173\n"
      "v_cvt_pk_bf16_f32 v182, v172, v173\n"
      "v_mfma_f32_16x16x32_bf16 v[12:15], a[156:159], v[188:191], v[12:15]\n"
      "v_exp_f32 v161, v161\n"
      "v_mfma_f32_16x16x32_bf16 v[168:171], a[80:83], a[32:35], v[200:203]\n"
      "v_add_f32 v242, v242, v174\n"
      "v_cvt_pk_bf16_f32 v183, v174, v175\n"
      "v_mfma_f32_16x16x32_bf16 v[168:171], a[84:87], a[36:39], v[168:171]\n"
      "v_exp_f32 v162, v162\n"
      "v_mfma_f32_16x16x32_bf16 v[168:171], a[88:91], a[40:43], v[168:171]\n"
      "v_add_f32 v243, v243, v175\n"
      "v_add_f32 v242, v242, v160\n"
      "v_mfma_f32_16x16x32_bf16 v[168:171], a[92:95], a[44:47], v[168:171]\n"
      "v_exp_f32 v163, v163\n"
      "v_mfma_f32_16x16x32_bf16 v[172:175], a[108:111], a[44:47], v[200:203]\n"
      "buffer_load_dwordx4 v240, s[40:43], s101 offen offset:2048 lds\n"
      "v_mfma_f32_16x16x32_bf16 v[172:175], a[104:107], a[40:43], v[172:175]\n"
      "v_exp_f32 v164, v164\n"
      "v_mfma_f32_16x16x32_bf16 v[172:175], a[100:103], a[36:39], v[172:175]\n"
      "v_add_f32 v243, v243, v161\n"
      "v_cvt_pk_bf16_f32 v184, v160, v161\n"
      "v_mfma_f32_16x16x32_bf16 v[172:175], a[96:99], a[32:35], v[172:175]\n"
      "v_exp_f32 v165, v165\n"
      "v_mfma_f32_16x16x32_bf16 v[16:19], a[160:163], v[188:191], v[16:19]\n"
      "s_waitcnt lgkmcnt(0)\n"
      "v_add_f32 v242, v242, v162\n"
      "v_add_f32 v243, v243, v163\n"
      "v_mfma_f32_16x16x32_bf16 v[16:19], a[192:195], v[176:179], v[16:19]\n"
      "v_exp_f32 v166, v166\n"
      "v_mfma_f32_16x16x32_bf16 v[20:23], a[196:199], v[176:179], v[20:23]\n"
      "v_cvt_pk_bf16_f32 v185, v162, v163\n"
      "v_cvt_pk_bf16_f32 v186, v164, v165\n"
      "v_mfma_f32_16x16x32_bf16 v[20:23], a[164:167], v[188:191], v[20:23]\n"
      "v_exp_f32 v167, v167\n"
      "v_mfma_f32_16x16x32_bf16 v[24:27], a[168:171], v[188:191], v[24:27]\n"
      "v_add_f32 v242, v242, v164\n"
      "v_add_f32 v243, v243, v165\n"
      "v_mfma_f32_16x16x32_bf16 v[24:27], a[200:203], v[176:179], v[24:27]\n"
      "v_add_f32 v242, v242, v166\n"
      "v_cvt_pk_bf16_f32 v187, v166, v167\n"
      "v_mfma_f32_16x16x32_bf16 v[28:31], a[204:207], v[176:179], v[28:31]\n"
      "v_add_f32 v243, v243, v167\n"
      "v_mfma_f32_16x16x32_bf16 v[28:31], a[172:175], v[188:191], v[28:31]\n"
      "v_exp_f32 v168, v168\n"
      "v_mfma_f32_16x16x32_bf16 a[208:211], a[228:231], v[188:191], a[208:211]\n"
      "v_add_f32 v244, v244, v168\n"
      "v_mfma_f32_16x16x32_bf16 a[208:211], a[228:231], v[176:179], a[208:211]\n"
      "v_exp_f32 v169, v169\n"
      "v_mfma_f32_16x16x32_bf16 v[160:163], a[112:115], a[32:35], v[200:203]\n"
      "v_add_f32 v245, v245, v169\n"
      "v_cvt_pk_bf16_f32 v188, v168, v169\n"
      "v_mfma_f32_16x16x32_bf16 v[160:163], a[116:119], a[36:39], v[160:163]\n"
      "v_exp_f32 v170, v170\n"
      "v_mfma_f32_16x16x32_bf16 v[160:163], a[120:123], a[40:43], v[160:163]\n"
      "v_add_f32 v244, v244, v170\n"
      "v_mfma_f32_16x16x32_bf16 v[160:163], a[124:127], a[44:47], v[160:163]\n"
      "v_exp_f32 v171, v171\n"
      "v_mfma_f32_16x16x32_bf16 v[164:167], a[140:143], a[44:47], v[200:203]\n"
      "buffer_load_dwordx4 v241, s[40:43], s101 offen offset:3072 nt lds\n"
      "v_mfma_f32_16x16x32_bf16 v[164:167], a[136:139], a[40:43], v[164:167]\n"
      "s_add_u32 s52, s52, 0x100\n"
      "v_exp_f32 v172, v172\n"
      "v_mfma_f32_16x16x32_bf16 v[164:167], a[132:135], a[36:39], v[164:167]\n"
      "s_or_b32 s52, s52, 0x1bc00\n"
      "v_add_f32 v245, v245, v171\n"
      "v_cvt_pk_bf16_f32 v189, v170, v171\n"
      "v_mfma_f32_16x16x32_bf16 v[164:167], a[128:131], a[32:35], v[164:167]\n"
      "v_exp_f32 v173, v173\n"
      "v_mfma_f32_16x16x32_bf16 v[32:35], a[144:147], v[180:183], v[32:35]\n"
      "s_xor_b32 s101, s52, s100\n"
      "v_add_f32 v244, v244, v172\n"
      "v_cvt_pk_bf16_f32 v190, v172, v173\n"
      "v_mfma_f32_16x16x32_bf16 v[32:35], a[176:179], v[184:187], v[32:35]\n"
      "v_exp_f32 v174, v174\n"
      "v_mfma_f32_16x16x32_bf16 v[36:39], a[180:183], v[184:187], v[36:39]\n"
      "v_add_f32 v245, v245, v173\n"
      "v_add_f32 v244, v244, v174\n"
      "v_mfma_f32_16x16x32_bf16 v[36:39], a[148:151], v[180:183], v[36:39]\n"
      "v_exp_f32 v175, v175\n"
      "v_mfma_f32_16x16x32_bf16 v[40:43], a[152:155], v[180:183], v[40:43]\n"
      "v_add_f32 v245, v245, v175\n"
      "v_cvt_pk_bf16_f32 v191, v174, v175\n"
      "v_mfma_f32_16x16x32_bf16 v[40:43], a[184:187], v[184:187], v[40:43]\n"
      "v_exp_f32 v160, v160\n"
      "v_mfma_f32_16x16x32_bf16 v[44:47], a[188:191], v[184:187], v[44:47]\n"
      "s_getreg_b32 s69, hwreg(7, 0, 4)\n"
      "v_add_f32 v244, v244, v160\n"
      "v_mfma_f32_16x16x32_bf16 v[44:47], a[156:159], v[180:183], v[44:47]\n"
      "s_waitcnt vmcnt(8) lgkmcnt(0)\n"
      "s_barrier\n"
      "v_exp_f32 v161, v161\n"
      "v_mfma_f32_16x16x32_bf16 v[168:171], a[80:83], a[48:51], v[204:207]\n"
      "ds_read_b128 a[80:83], v212 offset:0\n"
      "v_add_f32 v245, v245, v161\n"
      "v_mfma_f32_16x16x32_bf16 v[168:171], a[84:87], a[52:55], v[168:171]\n"
      "v_exp_f32 v162, v162\n"
      "v_mfma_f32_16x16x32_bf16 v[168:171], a[88:91], a[56:59], v[168:171]\n"
      "v_add_f32 v244, v244, v162\n"
      "v_cvt_pk_bf16_f32 v176, v160, v161\n"
      "v_mfma_f32_16x16x32_bf16 v[168:171], a[92:95], a[60:63], v[168:171]\n"
      "v_exp_f32 v163, v163\n"
      "v_mfma_f32_16x16x32_bf16 v[172:175], a[108:111], a[60:63], v[204:207]\n"
      "ds_read_b128 a[108:111], v215 offset:4096\n"
      "v_add_f32 v245, v245, v163\n"
      "v_mfma_f32_16x16x32_bf16 v[172:175], a[104:107], a[56:59], v[172:175]\n"
      "v_exp_f32 v164, v164\n"
      "v_mfma_f32_16x16x32_bf16 v[172:175], a[100:103], a[52:55], v[172:175]\n"
      "ds_read_b128 a[88:91], v214 offset:0\n"
      "v_cvt_pk_bf16_f32 v177, v162, v163\n"
      "v_mfma_f32_16x16x32_bf16 v[172:175], a[96:99], a[48:51], v[172:175]\n"
      "v_exp_f32 v165, v165\n"
      "v_mfma_f32_16x16x32_bf16 v[48:51], a[160:163], v[180:183], v[48:51]\n"
      "ds_read_b128 a[104:107], v214 offset:4096\n"
      "v_add_f32 v244, v244, v165\n"
      "v_mfma_f32_16x16x32_bf16 v[48:51], a[192:195], v[184:187], v[48:51]\n"
      "s_mov_b32 m0, s56\n"
      "v_exp_f32 v166, v166\n"
      "v_mfma_f32_16x16x32_bf16 v[52:55], a[196:199], v[184:187], v[52:55]\n"
      "buffer_load_dwordx4 v236, s[36:39], s101 offen offset:0 lds\n"
      "v_mfma_f32_16x16x32_bf16 v[52:55], a[164:167], v[180:183], v[52:55]\n"
      "v_exp_f32 v167, v167\n"
      "v_mfma_f32_16x16x32_bf16 v[56:59], a[168:171], v[180:183], v[56:59]\n"
      "ds_read_b128 a[92:95], v215 offset:0\n"
      "v_add_f32 v245, v245, v164\n"
      "v_mfma_f32_16x16x32_bf16 v[56:59], a[200:203], v[184:187], v[56:59]\n"
      "v_exp_f32 v168, v168\n"
      "v_mfma_f32_16x16x32_bf16 v[60:63], a[204:207], v[184:187], v[60:63]\n"
      "ds_read_b128 a[84:87], v213 offset:0\n"
      "v_cvt_pk_bf16_f32 v178, v164, v165\n"
      "v_mfma_f32_16x16x32_bf16 v[60:63], a[172:175], v[180:183], v[60:63]\n"
      "v_exp_f32 v169, v169\n"
      "v_mfma_f32_16x16x32_bf16 v[160:163], a[112:115], a[48:51], v[204:207]\n"
      "v_add_f32 v244, v244, v167\n"
      "v_cvt_pk_bf16_f32 v179, v166, v167\n"
      "v_mfma_f32_16x16x32_bf16 v[160:163], a[116:119], a[52:55], v[160:163]\n"
      "v_exp_f32 v170, v170\n"
      "v_mfma_f32_16x16x32_bf16 v[160:163], a[120:123], a[56:59], v[160:163]\n"
      "ds_read_b128 a[116:119], v217 offset:0\n"
      "v_add_f32 v245, v245, v166\n"
      "v_mfma_f32_16x16x32_bf16 v[160:163], a[124:127], a[60:63], v[160:163]\n"
      "v_exp_f32 v171, v171\n"
      "v_mfma_f32_16x16x32_bf16 v[164:167], a[140:143], a[60:63], v[204:207]\n"
      "ds_read_b128 a[140:143], v219 offset:4096\n"
      "v_cvt_pk_bf16_f32 v181, v170, v171\n"
      "v_mfma_f32_16x16x32_bf16 v[164:167], a[136:139], a[56:59], v[164:167]\n"
      "v_exp_f32 v172, v172\n"
      "v_mfma_f32_16x16x32_bf16 v[164:167], a[132:135], a[52:55], v[164:167]\n"
      "ds_read_b128 a[100:103], v213 offset:4096\n"
      "v_cvt_pk_bf16_f32 v180, v168, v169\n"
      "v_mfma_f32_16x16x32_bf16 v[164:167], a[128:131], a[48:51], v[164:167]\n"
      "v_exp_f32 v173, v173\n"
      "v_mfma_f32_16x16x32_bf16 v[64:67], a[144:147], v[188:191], v[64:67]\n"
      "ds_read_b128 a[136:139], v218 offset:4096\n"
      "v_cvt_pk_bf16_f32 v182, v172, v173\n"
      "v_mfma_f32_16x16x32_bf16 v[64:67], a[176:179], v[176:179], v[64:67]\n"
      "v_exp_f32 v174, v174\n"
      "v_mfma_f32_16x16x32_bf16 v[68:71], a[180:183], v[176:179], v[68:71]\n"
      "ds_read_b128 a[96:99], v212 offset:4096\n"
      "v_mfma_f32_16x16x32_bf16 v[68:71], a[148:151], v[188:191], v[68:71]\n"
      "v_exp_f32 v175, v175\n"
      "v_mfma_f32_16x16x32_bf16 v[72:75], a[152:155], v[188:191], v[72:75]\n"
      "buffer_load_dwordx4 v237, s[36:39], s101 offen offset:1024 lds\n"
      "v_mfma_f32_16x16x32_bf16 v[72:75], a[184:187], v[176:179], v[72:75]\n"
      "v_exp_f32 v160, v160\n"
      "v_mfma_f32_16x16x32_bf16 v[76:79], a[188:191], v[176:179], v[76:79]\n"
      "s_cmp_lt_u32 s52, 0x9bd00\n"
      "ds_read_b128 a[132:135], v217 offset:4096\n"
      "v_cvt_pk_bf16_f32 v183, v174, v175\n"
      "v_mfma_f32_16x16x32_bf16 v[76:79], a[156:159], v[188:191], v[76:79]\n"
      "s_waitcnt lgkmcnt(11)\n"
      "v_exp_f32 v161, v161\n"
      "s_cbranch_scc1 L_loop%=\n"
      "L_epi%=:\n"
      "v_mfma_f32_16x16x32_bf16 v[168:171], a[80:83], a[0:3], v[192:195]\n"
      "s_waitcnt lgkmcnt(6)\n"
      "ds_read_b128 a[112:115], v216 offset:0\n"
      "v_cvt_pk_bf16_f32 v184, v160, v161\n"
      "v_mov_b32 v241, v208\n"
      "v_add_f32 v246, v246, v247\n"
      "v_add_f32 v242, v242, v243\n"
      "v_add_f32 v244, v244, v245\n"
      "v_mov_b32 v247, v246\n"
      "v_mfma_f32_16x16x32_bf16 v[168:171], a[84:87], a[4:7], v[168:171]\n"
      "ds_read_b128 a[120:123], v218 offset:0\n"
      "v_mov_b32 v243, v242\n"
      "v_mov_b32 v245, v244\n"
      "v_permlane16_swap_b32 v246, v247\n"
      "v_permlane16_swap_b32 v242, v243\n"
      "v_mfma_f32_16x16x32_bf16 v[168:171], a[88:91], a[8:11], v[168:171]\n"
      "ds_read_b128 a[124:127], v219 offset:0\n"
      "v_permlane16_swap_b32 v244, v245\n"
      "v_add_f32 v246, v246, v247\n"
      "v_add_f32 v242, v242, v243\n"
      "v_add_f32 v244, v244, v245\n"
      "v_mov_b32 v247, v246\n"
      "v_mfma_f32_16x16x32_bf16 v[168:171], a[92:95], a[12:15], v[168:171]\n"
      "v_exp_f32 v162, v162\n"
      "v_mov_b32 v243, v242\n"
      "v_mov_b32 v245, v244\n"
      "v_permlane32_swap_b32 v246, v247\n"
      "v_mfma_f32_16x16x32_bf16 v[172:175], a[108:111], a[12:15], v[192:195]\n"
      "v_exp_f32 v163, v163\n"
      "v_permlane32_swap_b32 v242, v243\n"
      "v_permlane32_swap_b32 v244, v245\n"
      "v_add_f32 v246, v246, v247\n"
      "v_add_f32 v242, v242, v243\n"
      "v_mfma_f32_16x16x32_bf16 v[172:175], a[104:107], a[8:11], v[172:175]\n"
      "s_waitcnt lgkmcnt(6)\n"
      "ds_read_b128 a[128:131], v216 offset:4096\n"
      "v_cvt_pk_bf16_f32 v185, v162, v163\n"
      "v_add_f32 v244, v244, v245\n"
      "v_mfma_f32_16x16x32_bf16 v[172:175], a[100:103], a[4:7], v[172:175]\n"
      "s_waitcnt lgkmcnt(5)\n"
      "v_exp_f32 v164, v164\n"
      "v_mfma_f32_16x16x32_bf16 v[172:175], a[96:99], a[0:3], v[172:175]\n"
      "v_exp_f32 v165, v165\n"
      "v_mfma_f32_16x16x32_bf16 v[80:83], a[160:163], v[188:191], v[80:83]\n"
      "v_exp_f32 v168, v168\n"
      "v_mfma_f32_16x16x32_bf16 v[80:83], a[192:195], v[176:179], v[80:83]\n"
      "v_exp_f32 v166, v166\n"
      "v_mfma_f32_16x16x32_bf16 v[84:87], a[196:199], v[176:179], v[84:87]\n"
      "v_exp_f32 v169, v169\n"
      "v_mfma_f32_16x16x32_bf16 v[84:87], a[164:167], v[188:191], v[84:87]\n"
      "v_exp_f32 v167, v167\n"
      "v_mfma_f32_16x16x32_bf16 v[88:91], a[168:171], v[188:191], v[88:91]\n"
      "v_cvt_pk_bf16_f32 v186, v164, v165\n"
      "v_cvt_pk_bf16_f32 v187, v166, v167\n"
      "v_mfma_f32_16x16x32_bf16 v[88:91], a[200:203], v[176:179], v[88:91]\n"
      "v_exp_f32 v170, v170\n"
      "v_mfma_f32_16x16x32_bf16 v[92:95], a[204:207], v[176:179], v[92:95]\n"
      "v_exp_f32 v171, v171\n"
      "v_mfma_f32_16x16x32_bf16 v[92:95], a[172:175], v[188:191], v[92:95]\n"
      "s_waitcnt lgkmcnt(3)\n"
      "v_exp_f32 v172, v172\n"
      "v_mfma_f32_16x16x32_bf16 v[160:163], a[112:115], a[0:3], v[192:195]\n"
      "v_cvt_pk_bf16_f32 v188, v168, v169\n"
      "v_cvt_pk_bf16_f32 v189, v170, v171\n"
      "v_mfma_f32_16x16x32_bf16 v[160:163], a[116:119], a[4:7], v[160:163]\n"
      "s_waitcnt lgkmcnt(2)\n"
      "v_exp_f32 v173, v173\n"
      "v_mfma_f32_16x16x32_bf16 v[160:163], a[120:123], a[8:11], v[160:163]\n"
      "s_waitcnt lgkmcnt(1)\n"
      "v_exp_f32 v174, v174\n"
      "v_mfma_f32_16x16x32_bf16 v[160:163], a[124:127], a[12:15], v[160:163]\n"
      "v_exp_f32 v175, v175\n"
      "v_mfma_f32_16x16x32_bf16 v[164:167], a[140:143], a[12:15], v[192:195]\n"
      "v_cvt_pk_bf16_f32 v190, v172, v173\n"
      "v_cvt_pk_bf16_f32 v191, v174, v175\n"
      "v_mfma_f32_16x16x32_bf16 v[164:167], a[136:139], a[8:11], v[164:167]\n"
      "v_mfma_f32_16x16x32_bf16 v[164:167], a[132:135], a[4:7], v[164:167]\n"
      "s_waitcnt lgkmcnt(0)\n"
      "v_mfma_f32_16x16x32_bf16 v[164:167], a[128:131], a[0:3], v[164:167]\n"
      "v_exp_f32 v160, v160\n"
      "v_mfma_f32_16x16x32_bf16 v[96:99], a[144:147], v[180:183], v[96:99]\n"
      "v_exp_f32 v161, v161\n"
      "v_mfma_f32_16x16x32_bf16 v[96:99], a[176:179], v[184:187], v[96:99]\n"
      "v_exp_f32 v162, v162\n"
      "v_mfma_f32_16x16x32_bf16 v[100:103], a[180:183], v[184:187], v[100:103]\n"
      "v_exp_f32 v163, v163\n"
      "v_mfma_f32_16x16x32_bf16 v[100:103], a[148:151], v[180:183], v[100:103]\n"
      "v_cvt_pk_bf16_f32 v176, v160, v161\n"
      "v_cvt_pk_bf16_f32 v177, v162, v163\n"
      "v_mfma_f32_16x16x32_bf16 v[104:107], a[152:155], v[180:183], v[104:107]\n"
      "ds_read_b64_tr_b16 a[144:145], v220 offset:0\n"
      "ds_read_b64_tr_b16 a[146:147], v220 offset:4096\n"
      "v_mfma_f32_16x16x32_bf16 v[104:107], a[184:187], v[184:187], v[104:107]\n"
      "ds_read_b64_tr_b16 a[176:177], v228 offset:0\n"
      "ds_read_b64_tr_b16 a[178:179], v228 offset:4096\n"
      "v_mfma_f32_16x16x32_bf16 v[108:111], a[188:191], v[184:187], v[108:111]\n"
      "v_exp_f32 v164, v164\n"
      "v_mfma_f32_16x16x32_bf16 v[108:111], a[156:159], v[180:183], v[108:111]\n"
      "v_exp_f32 v165, v165\n"
      "v_mfma_f32_16x16x32_bf16 v[168:171], a[80:83], a[64:67], v[208:211]\n"
      "v_exp_f32 v166, v166\n"
      "v_mfma_f32_16x16x32_bf16 v[168:171], a[84:87], a[68:71], v[168:171]\n"
      "v_exp_f32 v167, v167\n"
      "v_mfma_f32_16x16x32_bf16 v[168:171], a[88:91], a[72:75], v[168:171]\n"
      "ds_read_b64_tr_b16 a[180:181], v229 offset:0\n"
      "ds_read_b64_tr_b16 a[182:183], v229 offset:4096\n"
      "v_mfma_f32_16x16x32_bf16 v[168:171], a[92:95], a[76:79], v[168:171]\n"
      "ds_read_b64_tr_b16 a[148:149], v221 offset:0\n"
      "ds_read_b64_tr_b16 a[150:151], v221 offset:4096\n"
      "v_mfma_f32_16x16x32_bf16 v[172:175], a[108:111], a[76:79], v[208:211]\n"
      "v_cvt_pk_bf16_f32 v178, v164, v165\n"
      "v_cvt_pk_bf16_f32 v179, v166, v167\n"
      "v_mfma_f32_16x16x32_bf16 v[172:175], a[104:107], a[72:75], v[172:175]\n"
      "ds_read_b64_tr_b16 a[152:153], v222 offset:0\n"
      "ds_read_b64_tr_b16 a[154:155], v222 offset:4096\n"
      "v_mfma_f32_16x16x32_bf16 v[172:175], a[100:103], a[68:71], v[172:175]\n"
      "ds_read_b64_tr_b16 a[184:185], v230 offset:0\n"
      "ds_read_b64_tr_b16 a[186:187], v230 offset:4096\n"
      "v_mfma_f32_16x16x32_bf16 v[172:175], a[96:99], a[64:67], v[172:175]\n"
      "ds_read_b64_tr_b16 a[188:189], v231 offset:0\n"
      "ds_read_b64_tr_b16 a[190:191], v231 offset:4096\n"
      "v_mfma_f32_16x16x32_bf16 v[112:115], a[160:163], v[180:183], v[112:115]\n"
      "ds_read_b64_tr_b16 a[156:157], v223 offset:0\n"
      "ds_read_b64_tr_b16 a[158:159], v223 offset:4096\n"
      "v_mfma_f32_16x16x32_bf16 v[112:115], a[192:195], v[184:187], v[112:115]\n"
      "v_exp_f32 v168, v168\n"
      "v_mfma_f32_16x16x32_bf16 v[116:119], a[196:199], v[184:187], v[116:119]\n"
      "v_exp_f32 v169, v169\n"
      "v_mfma_f32_16x16x32_bf16 v[116:119], a[164:167], v[180:183], v[116:119]\n"
      "v_exp_f32 v170, v170\n"
      "v_mfma_f32_16x16x32_bf16 v[120:123], a[168:171], v[180:183], v[120:123]\n"
      "v_exp_f32 v171, v171\n"
      "v_mfma_f32_16x16x32_bf16 v[120:123], a[200:203], v[184:187], v[120:123]\n"
      "ds_read_b64_tr_b16 a[160:161], v224 offset:0\n"
      "ds_read_b64_tr_b16 a[162:163], v224 offset:4096\n"
      "v_mfma_f32_16x16x32_bf16 v[124:127], a[204:207], v[184:187], v[124:127]\n"
      "ds_read_b64_tr_b16 a[192:193], v232 offset:0\n"
      "ds_read_b64_tr_b16 a[194:195], v232 offset:4096\n"
      "v_mfma_f32_16x16x32_bf16 v[124:127], a[172:175], v[180:183], v[124:127]\n"
      "ds_read_b64_tr_b16 a[196:197], v233 offset:0\n"
      "ds_read_b64_tr_b16 a[198:199], v233 offset:4096\n"
      "v_mfma_f32_16x16x32_bf16 a[220:223], a[228:231], v[180:183], a[220:223]\n"
      "ds_read_b64_tr_b16 a[164:165], v225 offset:0\n"
      "ds_read_b64_tr_b16 a[166:167], v225 offset:4096\n"
      "v_mfma_f32_16x16x32_bf16 a[220:223], a[228:231], v[184:187], a[220:223]\n"
      "v_cvt_pk_bf16_f32 v180, v168, v169\n"
      "v_cvt_pk_bf16_f32 v181, v170, v171\n"
      "v_mfma_f32_16x16x32_bf16 v[160:163], a[80:83], a[16:19], v[196:199]\n"
      "v_exp_f32 v172, v172\n"
      "v_mfma_f32_16x16x32_bf16 v[160:163], a[84:87], a[20:23], v[160:163]\n"
      "v_exp_f32 v173, v173\n"
      "v_mfma_f32_16x16x32_bf16 v[160:163], a[88:91], a[24:27], v[160:163]\n"
      "v_exp_f32 v174, v174\n"
      "v_mfma_f32_16x16x32_bf16 v[160:163], a[92:95], a[28:31], v[160:163]\n"
      "v_exp_f32 v175, v175\n"
      "v_mfma_f32_16x16x32_bf16 v[164:167], a[108:111], a[28:31], v[196:199]\n"
      "ds_read_b64_tr_b16 a[168:169], v226 offset:0\n"
      "ds_read_b64_tr_b16 a[170:171], v226 offset:4096\n"
      "v_mfma_f32_16x16x32_bf16 v[164:167], a[104:107], a[24:27], v[164:167]\n"
      "ds_read_b64_tr_b16 a[200:201], v234 offset:0\n"
      "ds_read_b64_tr_b16 a[202:203], v234 offset:4096\n"
      "v_mfma_f32_16x16x32_bf16 v[164:167], a[100:103], a[20:23], v[164:167]\n"
      "v_cvt_pk_bf16_f32 v182, v172, v173\n"
      "v_cvt_pk_bf16_f32 v183, v174, v175\n"
      "v_mfma_f32_16x16x32_bf16 v[164:167], a[96:99], a[16:19], v[164:167]\n"
      "ds_read_b64_tr_b16 a[204:205], v235 offset:0\n"
      "ds_read_b64_tr_b16 a[206:207], v235 offset:4096\n"
      "v_mfma_f32_16x16x32_bf16 a[208:211], a[228:231], v[188:191], a[208:211]\n"
      "ds_read_b64_tr_b16 a[172:173], v227 offset:0\n"
      "ds_read_b64_tr_b16 a[174:175], v227 offset:4096\n"
      "v_mfma_f32_16x16x32_bf16 a[208:211], a[228:231], v[176:179], a[208:211]\n"
      "s_waitcnt lgkmcnt(15)\n"
      "v_exp_f32 v160, v160\n"
      "v_mfma_f32_16x16x32_bf16 v[0:3], a[144:147], v[188:191], v[0:3]\n"
      "s_waitcnt lgkmcnt(15)\n"
      "v_exp_f32 v161, v161\n"
      "v_mfma_f32_16x16x32_bf16 v[0:3], a[176:179], v[176:179], v[0:3]\n"
      "s_waitcnt lgkmcnt(15)\n"
      "v_exp_f32 v162, v162\n"
      "v_mfma_f32_16x16x32_bf16 v[4:7], a[180:183], v[176:179], v[4:7]\n"
      "s_waitcnt lgkmcnt(15)\n"
      "v_exp_f32 v163, v163\n"
      "v_mfma_f32_16x16x32_bf16 v[4:7], a[148:151], v[188:191], v[4:7]\n"
      "s_waitcnt lgkmcnt(15)\n"
      "v_cvt_pk_bf16_f32 v184, v160, v161\n"
      "v_cvt_pk_bf16_f32 v185, v162, v163\n"
      "v_accvgpr_read_b32 v192, a208\n"
      "v_subrev_u32 v254, 0x35800000, v192\n"
      "v_max_u32 v255, v255, v254\n"
      "v_rcp_f32 v192, v192\n"
      "v_mfma_f32_16x16x32_bf16 v[8:11], a[152:155], v[188:191], v[8:11]\n"
      "s_waitcnt lgkmcnt(15)\n"
      "v_exp_f32 v164, v164\n"
      "v_mfma_f32_16x16x32_bf16 v[8:11], a[184:187], v[176:179], v[8:11]\n"
      "s_waitcnt lgkmcnt(15)\n"
      "v_exp_f32 v165, v165\n"
      "v_mfma_f32_16x16x32_bf16 v[12:15], a[188:191], v[176:179], v[12:15]\n"
      "s_waitcnt lgkmcnt(15)\n"
      "v_exp_f32 v166, v166\n"
      "v_mfma_f32_16x16x32_bf16 v[12:15], a[156:159], v[188:191], v[12:15]\n"
      "v_exp_f32 v167, v167\n"
      "v_mul_f32 v0, v0, v192\n"
      "v_mul_f32 v1, v1, v192\n"
      "v_mul_f32 v2, v2, v192\n"
      "v_mul_f32 v3, v3, v192\n"
      "v_mul_f32 v4, v4, v192\n"
      "v_mfma_f32_16x16x32_bf16 v[168:171], a[112:115], a[16:19], v[196:199]\n"
      "v_cvt_pk_bf16_f32 v186, v164, v165\n"
      "v_cvt_pk_bf16_f32 v187, v166, v167\n"
      "v_mul_f32 v5, v5, v192\n"
      "v_mul_f32 v6, v6, v192\n"
      "v_mul_f32 v7, v7, v192\n"
      "v_cvt_pk_bf16_f32 v0, v0, v1\n"
      "v_cvt_pk_bf16_f32 v1, v2, v3\n"
      "v_mfma_f32_16x16x32_bf16 v[168:171], a[116:119], a[20:23], v[168:171]\n"
      "v_cvt_pk_bf16_f32 v2, v4, v5\n"
      "v_cvt_pk_bf16_f32 v3, v6, v7\n"
      "v_mfma_f32_16x16x32_bf16 v[168:171], a[120:123], a[24:27], v[168:171]\n"
      "ds_write2_b64 v250, v[0:1], v[2:3] offset0:0 offset1:4\n"
      "v_mfma_f32_16x16x32_bf16 v[168:171], a[124:127], a[28:31], v[168:171]\n"
      "v_mul_f32 v8, v8, v192\n"
      "v_mul_f32 v9, v9, v192\n"
      "v_mul_f32 v10, v10, v192\n"
      "v_mul_f32 v11, v11, v192\n"
      "v_mul_f32 v12, v12, v192\n"
      "v_mul_f32 v13, v13, v192\n"
      "v_mul_f32 v14, v14, v192\n"
      "v_mfma_f32_16x16x32_bf16 v[172:175], a[140:143], a[28:31], v[196:199]\n"
      "v_mul_f32 v15, v15, v192\n"
      "v_cvt_pk_bf16_f32 v8, v8, v9\n"
      "v_cvt_pk_bf16_f32 v9, v10, v11\n"
      "v_cvt_pk_bf16_f32 v10, v12, v13\n"
      "v_cvt_pk_bf16_f32 v11, v14, v15\n"
      "v_mfma_f32_16x16x32_bf16 v[172:175], a[136:139], a[24:27], v[172:175]\n"
      "ds_write2_b64 v250, v[8:9], v[10:11] offset0:8 offset1:12\n"
      "ds_read_b128 v[4:7], v251 offset:0\n"
      "ds_read_b128 v[12:15], v251 offset:1152\n"
      "v_mfma_f32_16x16x32_bf16 v[172:175], a[132:135], a[20:23], v[172:175]\n"
      "v_mfma_f32_16x16x32_bf16 v[172:175], a[128:131], a[16:19], v[172:175]\n"
      "s_waitcnt lgkmcnt(15)\n"
      "v_exp_f32 v168, v168\n"
      "v_mfma_f32_16x16x32_bf16 v[16:19], a[160:163], v[188:191], v[16:19]\n"
      "s_waitcnt lgkmcnt(15)\n"
      "v_exp_f32 v169, v169\n"
      "v_mfma_f32_16x16x32_bf16 v[16:19], a[192:195], v[176:179], v[16:19]\n"
      "s_waitcnt lgkmcnt(14)\n"
      "v_exp_f32 v170, v170\n"
      "v_mfma_f32_16x16x32_bf16 v[20:23], a[196:199], v[176:179], v[20:23]\n"
      "s_waitcnt lgkmcnt(12)\n"
      "v_exp_f32 v171, v171\n"
      "v_mfma_f32_16x16x32_bf16 v[20:23], a[164:167], v[188:191], v[20:23]\n"
      "s_waitcnt lgkmcnt(10)\n"
      "v_exp_f32 v172, v172\n"
      "v_mfma_f32_16x16x32_bf16 v[24:27], a[168:171], v[188:191], v[24:27]\n"
      "s_waitcnt lgkmcnt(8)\n"
      "v_exp_f32 v173, v173\n"
      "v_mfma_f32_16x16x32_bf16 v[24:27], a[200:203], v[176:179], v[24:27]\n"
      "s_waitcnt lgkmcnt(6)\n"
      "v_exp_f32 v174, v174\n"
      "v_mfma_f32_16x16x32_bf16 v[28:31], a[204:207], v[176:179], v[28:31]\n"
      "s_waitcnt lgkmcnt(4)\n"
      "v_exp_f32 v175, v175\n"
      "v_mfma_f32_16x16x32_bf16 v[28:31], a[172:175], v[188:191], v[28:31]\n"
      "v_mul_f32 v16, v16, v192\n"
      "v_mul_f32 v17, v17, v192\n"
      "v_mul_f32 v18, v18, v192\n"
      "v_mul_f32 v19, v19, v192\n"
      "v_mul_f32 v20, v20, v192\n"
      "v_mul_f32 v21, v21, v192\n"
      "v_mul_f32 v22, v22, v192\n"
      "v_mfma_f32_16x16x32_bf16 v[160:163], a[80:83], a[32:35], v[200:203]\n"
      "v_cvt_pk_bf16_f32 v188, v168, v169\n"
      "v_cvt_pk_bf16_f32 v189, v170, v171\n"
      "v_mul_f32 v23, v23, v192\n"
      "v_cvt_pk_bf16_f32 v16, v16, v17\n"
      "v_cvt_pk_bf16_f32 v17, v18, v19\n"
      "v_cvt_pk_bf16_f32 v18, v20, v21\n"
      "v_cvt_pk_bf16_f32 v19, v22, v23\n"
      "v_mfma_f32_16x16x32_bf16 v[160:163], a[84:87], a[36:39], v[160:163]\n"
      "ds_write2_b64 v250, v[16:17], v[18:19] offset0:0 offset1:4\n"
      "v_cvt_pk_bf16_f32 v190, v172, v173\n"
      "v_cvt_pk_bf16_f32 v191, v174, v175\n"
      "v_mfma_f32_16x16x32_bf16 v[160:163], a[88:91], a[40:43], v[160:163]\n"
      "s_waitcnt lgkmcnt(2)\n"
      "buffer_store_dwordx4 v[4:7], v248, s[48:51], s24 offen offset:0 sc0 sc1\n"
      "v_mfma_f32_16x16x32_bf16 v[160:163], a[92:95], a[44:47], v[160:163]\n"
      "s_waitcnt lgkmcnt(1)\n"
      "buffer_store_dwordx4 v[12:15], v248, s[48:51], s24 offen offset:2048 sc0 sc1\n"
      "v_mfma_f32_16x16x32_bf16 a[232:235], v[4:7], v[12:15], a[232:235]\n"
      "v_mul_f32 v24, v24, v192\n"
      "v_mfma_f32_16x16x32_bf16 v[164:167], a[108:111], a[44:47], v[200:203]\n"
      "v_mul_f32 v25, v25, v192\n"
      "v_mul_f32 v26, v26, v192\n"
      "v_mul_f32 v27, v27, v192\n"
      "v_mul_f32 v28, v28, v192\n"
      "v_mul_f32 v29, v29, v192\n"
      "v_mul_f32 v30, v30, v192\n"
      "v_mul_f32 v31, v31, v192\n"
      "v_mfma_f32_16x16x32_bf16 v[164:167], a[104:107], a[40:43], v[164:167]\n"
      "v_cvt_pk_bf16_f32 v24, v24, v25\n"
      "v_cvt_pk_bf16_f32 v25, v26, v27\n"
      "v_cvt_pk_bf16_f32 v26, v28, v29\n"
      "v_cvt_pk_bf16_f32 v27, v30, v31\n"
      "v_mfma_f32_16x16x32_bf16 v[164:167], a[100:103], a[36:39], v[164:167]\n"
      "ds_write2_b64 v250, v[24:25], v[26:27] offset0:8 offset1:12\n"
      "ds_read_b128 v[20:23], v251 offset:0\n"
      "ds_read_b128 v[28:31], v251 offset:1152\n"
      "v_mfma_f32_16x16x32_bf16 v[164:167], a[96:99], a[32:35], v[164:167]\n"
      "v_exp_f32 v160, v160\n"
      "v_mfma_f32_16x16x32_bf16 a[224:227], a[228:231], v[180:183], a[224:227]\n"
      "v_exp_f32 v161, v161\n"
      "v_mfma_f32_16x16x32_bf16 v[128:131], a[144:147], v[180:183], v[128:131]\n"
      "v_exp_f32 v162, v162\n"
      "v_mfma_f32_16x16x32_bf16 v[132:135], a[148:151], v[180:183], v[132:135]\n"
      "v_exp_f32 v163, v163\n"
      "v_mfma_f32_16x16x32_bf16 v[136:139], a[152:155], v[180:183], v[136:139]\n"
      "v_cvt_pk_bf16_f32 v176, v160, v161\n"
      "v_cvt_pk_bf16_f32 v177, v162, v163\n"
      "v_mfma_f32_16x16x32_bf16 v[140:143], a[156:159], v[180:183], v[140:143]\n"
      "v_exp_f32 v164, v164\n"
      "v_accvgpr_read_b32 v240, a224\n"
      "v_add_f32 v240, v240, v246\n"
      "v_mfma_f32_16x16x32_bf16 v[144:147], a[160:163], v[180:183], v[144:147]\n"
      "ds_write_b64 v249, v[240:241] offset:4096\n"
      "v_exp_f32 v165, v165\n"
      "v_mfma_f32_16x16x32_bf16 v[148:151], a[164:167], v[180:183], v[148:151]\n"
      "v_exp_f32 v166, v166\n"
      "v_mfma_f32_16x16x32_bf16 v[152:155], a[168:171], v[180:183], v[152:155]\n"
      "v_exp_f32 v167, v167\n"
      "v_mfma_f32_16x16x32_bf16 v[156:159], a[172:175], v[180:183], v[156:159]\n"
      "v_cvt_pk_bf16_f32 v178, v164, v165\n"
      "v_cvt_pk_bf16_f32 v179, v166, v167\n"
      "v_mfma_f32_16x16x32_bf16 v[168:171], a[112:115], a[32:35], v[200:203]\n"
      "ds_write_b128 v249, v[144:147] offset:0\n"
      "v_mfma_f32_16x16x32_bf16 v[168:171], a[116:119], a[36:39], v[168:171]\n"
      "ds_write_b128 v249, v[148:151] offset:1024\n"
      "v_mfma_f32_16x16x32_bf16 v[168:171], a[120:123], a[40:43], v[168:171]\n"
      "ds_write_b128 v249, v[152:155] offset:2048\n"
      "v_mfma_f32_16x16x32_bf16 v[168:171], a[124:127], a[44:47], v[168:171]\n"
      "ds_write_b128 v249, v[156:159] offset:3072\n"
      "s_nop 1\n"
      "v_xor_b32 v249, 0x2000, v249\n"
      "v_mfma_f32_16x16x32_bf16 v[172:175], a[140:143], a[44:47], v[200:203]\n"
      "v_subrev_u32 v254, 0x35800000, v240\n"
      "v_max_u32 v255, v255, v254\n"
      "v_mfma_f32_16x16x32_bf16 v[172:175], a[136:139], a[40:43], v[172:175]\n"
      "s_waitcnt lgkmcnt(6)\n"
      "buffer_store_dwordx4 v[20:23], v252, s[48:51], s24 offen offset:0 sc0 sc1\n"
      "v_mfma_f32_16x16x32_bf16 v[172:175], a[132:135], a[36:39], v[172:175]\n"
      "s_waitcnt lgkmcnt(5)\n"
      "buffer_store_dwordx4 v[28:31], v252, s[48:51], s24 offen offset:2048 sc0 sc1\n"
      "v_mfma_f32_16x16x32_bf16 a[232:235], v[20:23], v[28:31], a[232:235]\n"
      "v_mfma_f32_16x16x32_bf16 v[172:175], a[128:131], a[32:35], v[172:175]\n"
      "v_exp_f32 v168, v168\n"
      "v_mfma_f32_16x16x32_bf16 a[212:215], a[228:231], v[184:187], a[212:215]\n"
      "v_exp_f32 v169, v169\n"
      "v_mfma_f32_16x16x32_bf16 a[212:215], a[228:231], v[188:191], a[212:215]\n"
      "v_exp_f32 v170, v170\n"
      "v_mfma_f32_16x16x32_bf16 v[32:35], a[144:147], v[184:187], v[32:35]\n"
      "v_exp_f32 v171, v171\n"
      "v_mfma_f32_16x16x32_bf16 v[32:35], a[176:179], v[188:191], v[32:35]\n"
      "v_cvt_pk_bf16_f32 v180, v168, v169\n"
      "v_cvt_pk_bf16_f32 v181, v170, v171\n"
      "v_mfma_f32_16x16x32_bf16 v[36:39], a[180:183], v[188:191], v[36:39]\n"
      "v_exp_f32 v172, v172\n"
      "v_mfma_f32_16x16x32_bf16 v[36:39], a[148:151], v[184:187], v[36:39]\n"
      "v_exp_f32 v173, v173\n"
      "v_accvgpr_read_b32 v196, a212\n"
      "v_add_f32 v196, v196, v242\n"
      "v_subrev_u32 v254, 0x35800000, v196\n"
      "v_max_u32 v255, v255, v254\n"
      "v_mfma_f32_16x16x32_bf16 v[40:43], a[152:155], v[184:187], v[40:43]\n"
      "v_exp_f32 v174, v174\n"
      "v_rcp_f32 v196, v196\n"
      "v_mfma_f32_16x16x32_bf16 v[40:43], a[184:187], v[188:191], v[40:43]\n"
      "v_exp_f32 v175, v175\n"
      "v_mfma_f32_16x16x32_bf16 v[44:47], a[188:191], v[188:191], v[44:47]\n"
      "v_cvt_pk_bf16_f32 v182, v172, v173\n"
      "v_cvt_pk_bf16_f32 v183, v174, v175\n"
      "v_mfma_f32_16x16x32_bf16 v[44:47], a[156:159], v[184:187], v[44:47]\n"
      "v_mul_f32 v32, v32, v196\n"
      "v_mul_f32 v33, v33, v196\n"
      "v_mul_f32 v34, v34, v196\n"
      "v_mul_f32 v35, v35, v196\n"
      "v_mul_f32 v36, v36, v196\n"
      "v_mul_f32 v37, v37, v196\n"
      "v_mul_f32 v38, v38, v196\n"
      "v_mfma_f32_16x16x32_bf16 v[160:163], a[80:83], a[48:51], v[204:207]\n"
      "v_mul_f32 v39, v39, v196\n"
      "v_cvt_pk_bf16_f32 v32, v32, v33\n"
      "v_cvt_pk_bf16_f32 v33, v34, v35\n"
      "v_cvt_pk_bf16_f32 v34, v36, v37\n"
      "v_cvt_pk_bf16_f32 v35, v38, v39\n"
      "v_mfma_f32_16x16x32_bf16 v[160:163], a[84:87], a[52:55], v[160:163]\n"
      "ds_write2_b64 v250, v[32:33], v[34:35] offset0:0 offset1:4\n"
      "v_mfma_f32_16x16x32_bf16 v[160:163], a[88:91], a[56:59], v[160:163]\n"
      "v_mfma_f32_16x16x32_bf16 v[160:163], a[92:95], a[60:63], v[160:163]\n"
      "v_mul_f32 v40, v40, v196\n"
      "v_mul_f32 v41, v41, v196\n"
      "v_mul_f32 v42, v42, v196\n"
      "v_mul_f32 v43, v43, v196\n"
      "v_mul_f32 v44, v44, v196\n"
      "v_mul_f32 v45, v45, v196\n"
      "v_mul_f32 v46, v46, v196\n"
      "v_mfma_f32_16x16x32_bf16 v[164:167], a[108:111], a[60:63], v[204:207]\n"
      "v_mul_f32 v47, v47, v196\n"
      "v_cvt_pk_bf16_f32 v40, v40, v41\n"
      "v_cvt_pk_bf16_f32 v41, v42, v43\n"
      "v_cvt_pk_bf16_f32 v42, v44, v45\n"
      "v_cvt_pk_bf16_f32 v43, v46, v47\n"
      "v_mfma_f32_16x16x32_bf16 v[164:167], a[104:107], a[56:59], v[164:167]\n"
      "ds_write2_b64 v250, v[40:41], v[42:43] offset0:8 offset1:12\n"
      "ds_read_b128 v[36:39], v251 offset:0\n"
      "ds_read_b128 v[44:47], v251 offset:1152\n"
      "s_waitcnt lgkmcnt(0)\n"
      "v_mfma_f32_16x16x32_bf16 v[164:167], a[100:103], a[52:55], v[164:167]\n"
      "s_barrier\n"
      "ds_read_b64 v[236:237], v249 offset:4096\n"
      "ds_read_b128 v[144:147], v249 offset:0\n"
      "ds_read_b128 v[148:151], v249 offset:1024\n"
      "ds_read_b128 v[152:155], v249 offset:2048\n"
      "ds_read_b128 v[156:159], v249 offset:3072\n"
      "v_mfma_f32_16x16x32_bf16 v[164:167], a[96:99], a[48:51], v[164:167]\n"
      "v_exp_f32 v160, v160\n"
      "v_mfma_f32_16x16x32_bf16 v[48:51], a[160:163], v[184:187], v[48:51]\n"
      "v_exp_f32 v161, v161\n"
      "v_mfma_f32_16x16x32_bf16 v[48:51], a[192:195], v[188:191], v[48:51]\n"
      "v_exp_f32 v162, v162\n"
      "v_mfma_f32_16x16x32_bf16 v[52:55], a[196:199], v[188:191], v[52:55]\n"
      "v_exp_f32 v163, v163\n"
      "v_mfma_f32_16x16x32_bf16 v[52:55], a[164:167], v[184:187], v[52:55]\n"
      "v_exp_f32 v164, v164\n"
      "v_mfma_f32_16x16x32_bf16 v[56:59], a[168:171], v[184:187], v[56:59]\n"
      "v_exp_f32 v165, v165\n"
      "v_mfma_f32_16x16x32_bf16 v[56:59], a[200:203], v[188:191], v[56:59]\n"
      "v_exp_f32 v166, v166\n"
      "v_mfma_f32_16x16x32_bf16 v[60:63], a[204:207], v[188:191], v[60:63]\n"
      "v_exp_f32 v167, v167\n"
      "v_mfma_f32_16x16x32_bf16 v[60:63], a[172:175], v[184:187], v[60:63]\n"
      "v_mul_f32 v48, v48, v196\n"
      "v_mul_f32 v49, v49, v196\n"
      "v_mul_f32 v50, v50, v196\n"
      "v_mul_f32 v51, v51, v196\n"
      "v_mul_f32 v52, v52, v196\n"
      "v_mul_f32 v53, v53, v196\n"
      "v_mul_f32 v54, v54, v196\n"
      "v_mfma_f32_16x16x32_bf16 v[168:171], a[112:115], a[48:51], v[204:207]\n"
      "v_cvt_pk_bf16_f32 v184, v160, v161\n"
      "v_cvt_pk_bf16_f32 v185, v162, v163\n"
      "v_mul_f32 v55, v55, v196\n"
      "v_cvt_pk_bf16_f32 v48, v48, v49\n"
      "v_cvt_pk_bf16_f32 v49, v50, v51\n"
      "v_cvt_pk_bf16_f32 v50, v52, v53\n"
      "v_cvt_pk_bf16_f32 v51, v54, v55\n"
      "v_mfma_f32_16x16x32_bf16 v[168:171], a[116:119], a[52:55], v[168:171]\n"
      "ds_write2_b64 v250, v[48:49], v[50:51] offset0:0 offset1:4\n"
      "v_cvt_pk_bf16_f32 v186, v164, v165\n"
      "v_cvt_pk_bf16_f32 v187, v166, v167\n"
      "v_mfma_f32_16x16x32_bf16 v[168:171], a[120:123], a[56:59], v[168:171]\n"
      "buffer_store_dwordx4 v[36:39], v248, s[48:51], s25 offen offset:0 sc0 sc1\n"
      "v_mfma_f32_16x16x32_bf16 v[168:171], a[124:127], a[60:63], v[168:171]\n"
      "buffer_store_dwordx4 v[44:47], v248, s[48:51], s25 offen offset:2048 sc0 sc1\n"
      "v_mfma_f32_16x16x32_bf16 a[232:235], v[36:39], v[44:47], a[232:235]\n"
      "v_mul_f32 v56, v56, v196\n"
      "v_mfma_f32_16x16x32_bf16 v[172:175], a[140:143], a[60:63], v[204:207]\n"
      "v_mul_f32 v57, v57, v196\n"
      "v_mul_f32 v58, v58, v196\n"
      "v_mul_f32 v59, v59, v196\n"
      "v_mul_f32 v60, v60, v196\n"
      "v_mul_f32 v61, v61, v196\n"
      "v_mul_f32 v62, v62, v196\n"
      "v_mul_f32 v63, v63, v196\n"
      "v_mfma_f32_16x16x32_bf16 v[172:175], a[136:139], a[56:59], v[172:175]\n"
      "v_cvt_pk_bf16_f32 v56, v56, v57\n"
      "v_cvt_pk_bf16_f32 v57, v58, v59\n"
      "v_cvt_pk_bf16_f32 v58, v60, v61\n"
      "v_cvt_pk_bf16_f32 v59, v62, v63\n"
      "v_mfma_f32_16x16x32_bf16 v[172:175], a[132:135], a[52:55], v[172:175]\n"
      "ds_write2_b64 v250, v[56:57], v[58:59] offset0:8 offset1:12\n"
      "ds_read_b128 v[52:55], v251 offset:0\n"
      "ds_read_b128 v[60:63], v251 offset:1152\n"
      "v_mfma_f32_16x16x32_bf16 v[172:175], a[128:131], a[48:51], v[172:175]\n"
      "v_exp_f32 v168, v168\n"
      "s_waitcnt lgkmcnt(8)\n"
      "v_min_f32 v238, v241, v237\n"
      "v_sub_f32 v239, v238, v241\n"
      "v_sub_f32 v253, v238, v237\n"
      "v_exp_f32 v239, v239\n"
      "v_mfma_f32_16x16x32_bf16 a[216:219], a[228:231], v[176:179], a[216:219]\n"
      "v_exp_f32 v169, v169\n"
      "v_exp_f32 v253, v253\n"
      "v_subrev_u32 v254, 0x35800000, v236\n"
      "v_max_u32 v255, v255, v254\n"
      "v_mul_f32 v240, v240, v239\n"
      "v_mfma_f32_16x16x32_bf16 a[216:219], a[228:231], v[180:183], a[216:219]\n"
      "v_exp_f32 v170, v170\n"
      "v_fma_f32 v240, v236, v253, v240\n"
      "v_rcp_f32 v238, v240\n"
      "v_subrev_u32 v254, 0x35800000, v240\n"
      "v_max_u32 v255, v255, v254\n"
      "v_mfma_f32_16x16x32_bf16 v[64:67], a[144:147], v[176:179], v[64:67]\n"
      "v_exp_f32 v171, v171\n"
      "v_mul_f32 v239, v239, v238\n"
      "v_mul_f32 v253, v253, v238\n"
      "v_mul_f32 v128, v128, v239\n"
      "v_mfma_f32_16x16x32_bf16 v[64:67], a[176:179], v[180:183], v[64:67]\n"
      "s_waitcnt lgkmcnt(1)\n"
      "buffer_store_dwordx4 v[52:55], v252, s[48:51], s25 offen offset:0 sc0 sc1\n"
      "v_exp_f32 v172, v172\n"
      "v_mfma_f32_16x16x32_bf16 v[68:71], a[180:183], v[180:183], v[68:71]\n"
      "s_waitcnt lgkmcnt(0)\n"
      "buffer_store_dwordx4 v[60:63], v252, s[48:51], s25 offen offset:2048 sc0 sc1\n"
      "v_exp_f32 v173, v173\n"
      "v_mfma_f32_16x16x32_bf16 v[68:71], a[148:151], v[176:179], v[68:71]\n"
      "v_exp_f32 v174, v174\n"
      "v_mfma_f32_16x16x32_bf16 a[232:235], v[52:55], v[60:63], a[232:235]\n"
      "v_mul_f32 v129, v129, v239\n"
      "v_mul_f32 v130, v130, v239\n"
      "v_mul_f32 v131, v131, v239\n"
      "v_mul_f32 v132, v132, v239\n"
      "v_mfma_f32_16x16x32_bf16 v[72:75], a[152:155], v[176:179], v[72:75]\n"
      "v_exp_f32 v175, v175\n"
      "v_mul_f32 v133, v133, v239\n"
      "v_mul_f32 v134, v134, v239\n"
      "v_mul_f32 v135, v135, v239\n"
      "v_fma_f32 v128, v144, v253, v128\n"
      "v_fma_f32 v129, v145, v253, v129\n"
      "v_mfma_f32_16x16x32_bf16 v[72:75], a[184:187], v[180:183], v[72:75]\n"
      "v_cvt_pk_bf16_f32 v188, v168, v169\n"
      "v_cvt_pk_bf16_f32 v189, v170, v171\n"
      "v_fma_f32 v130, v146, v253, v130\n"
      "v_fma_f32 v131, v147, v253, v131\n"
      "v_fma_f32 v132, v148, v253, v132\n"
      "v_fma_f32 v133, v149, v253, v133\n"
      "v_fma_f32 v134, v150, v253, v134\n"
      "v_mfma_f32_16x16x32_bf16 v[76:79], a[188:191], v[180:183], v[76:79]\n"
      "v_cvt_pk_bf16_f32 v190, v172, v173\n"
      "v_cvt_pk_bf16_f32 v191, v174, v175\n"
      "v_fma_f32 v135, v151, v253, v135\n"
      "v_cvt_pk_bf16_f32 v128, v128, v129\n"
      "v_cvt_pk_bf16_f32 v129, v130, v131\n"
      "v_cvt_pk_bf16_f32 v130, v132, v133\n"
      "v_cvt_pk_bf16_f32 v131, v134, v135\n"
      "v_mfma_f32_16x16x32_bf16 v[76:79], a[156:159], v[176:179], v[76:79]\n"
      "ds_write2_b64 v250, v[128:129], v[130:131] offset0:0 offset1:4\n"
      "v_mul_f32 v136, v136, v239\n"
      "v_mfma_f32_16x16x32_bf16 v[80:83], a[160:163], v[176:179], v[80:83]\n"
      "v_mul_f32 v137, v137, v239\n"
      "v_mul_f32 v138, v138, v239\n"
      "v_mul_f32 v139, v139, v239\n"
      "v_mul_f32 v140, v140, v239\n"
      "v_mul_f32 v141, v141, v239\n"
      "v_mul_f32 v142, v142, v239\n"
      "v_mul_f32 v143, v143, v239\n"
      "v_mfma_f32_16x16x32_bf16 v[80:83], a[192:195], v[180:183], v[80:83]\n"
      "v_fma_f32 v136, v152, v253, v136\n"
      "v_fma_f32 v137, v153, v253, v137\n"
      "v_fma_f32 v138, v154, v253, v138\n"
      "v_fma_f32 v139, v155, v253, v139\n"
      "v_fma_f32 v140, v156, v253, v140\n"
      "v_fma_f32 v141, v157, v253, v141\n"
      "v_fma_f32 v142, v158, v253, v142\n"
      "v_mfma_f32_16x16x32_bf16 v[84:87], a[196:199], v[180:183], v[84:87]\n"
      "v_fma_f32 v143, v159, v253, v143\n"
      "v_cvt_pk_bf16_f32 v136, v136, v137\n"
      "v_cvt_pk_bf16_f32 v137, v138, v139\n"
      "v_cvt_pk_bf16_f32 v138, v140, v141\n"
      "v_cvt_pk_bf16_f32 v139, v142, v143\n"
      "v_mfma_f32_16x16x32_bf16 v[84:87], a[164:167], v[176:179], v[84:87]\n"
      "ds_write2_b64 v250, v[136:137], v[138:139] offset0:8 offset1:12\n"
      "ds_read_b128 v[132:135], v251 offset:0\n"
      "ds_read_b128 v[140:143], v251 offset:1152\n"
      "v_mfma_f32_16x16x32_bf16 v[88:91], a[168:171], v[176:179], v[88:91]\n"
      "v_accvgpr_read_b32 v200, a216\n"
      "v_add_f32 v200, v200, v244\n"
      "v_subrev_u32 v254, 0x35800000, v200\n"
      "v_max_u32 v255, v255, v254\n"
      "v_rcp_f32 v200, v200\n"
      "s_nop 0\n"
      "v_mul_f32 v64, v64, v200\n"
      "v_mfma_f32_16x16x32_bf16 v[88:91], a[200:203], v[180:183], v[88:91]\n"
      "v_mul_f32 v65, v65, v200\n"
      "v_mul_f32 v66, v66, v200\n"
      "v_mul_f32 v67, v67, v200\n"
      "v_mul_f32 v68, v68, v200\n"
      "v_mul_f32 v69, v69, v200\n"
      "v_mul_f32 v70, v70, v200\n"
      "v_mul_f32 v71, v71, v200\n"
      "v_mfma_f32_16x16x32_bf16 v[92:95], a[204:207], v[180:183], v[92:95]\n"
      "v_cvt_pk_bf16_f32 v64, v64, v65\n"
      "v_cvt_pk_bf16_f32 v65, v66, v67\n"
      "v_cvt_pk_bf16_f32 v66, v68, v69\n"
      "v_cvt_pk_bf16_f32 v67, v70, v71\n"
      "v_mfma_f32_16x16x32_bf16 v[92:95], a[172:175], v[176:179], v[92:95]\n"
      "ds_write2_b64 v250, v[64:65], v[66:67] offset0:0 offset1:4\n"
      "v_mfma_f32_16x16x32_bf16 a[220:223], a[228:231], v[184:187], a[220:223]\n"
      "s_waitcnt lgkmcnt(2)\n"
      "buffer_store_dwordx4 v[132:135], v248, s[48:51], s65 offen offset:0 sc0 sc1\n"
      "v_mfma_f32_16x16x32_bf16 a[220:223], a[228:231], v[188:191], a[220:223]\n"
      "s_waitcnt lgkmcnt(1)\n"
      "buffer_store_dwordx4 v[140:143], v248, s[48:51], s65 offen offset:2048 sc0 sc1\n"
      "v_mfma_f32_16x16x32_bf16 a[232:235], v[132:135], v[140:143], a[232:235]\n"
      "v_mul_f32 v72, v72, v200\n"
      "v_mfma_f32_16x16x32_bf16 v[96:99], a[144:147], v[184:187], v[96:99]\n"
      "v_mul_f32 v73, v73, v200\n"
      "v_mul_f32 v74, v74, v200\n"
      "v_mul_f32 v75, v75, v200\n"
      "v_mul_f32 v76, v76, v200\n"
      "v_mul_f32 v77, v77, v200\n"
      "v_mul_f32 v78, v78, v200\n"
      "v_mul_f32 v79, v79, v200\n"
      "v_mfma_f32_16x16x32_bf16 v[96:99], a[176:179], v[188:191], v[96:99]\n"
      "v_cvt_pk_bf16_f32 v72, v72, v73\n"
      "v_cvt_pk_bf16_f32 v73, v74, v75\n"
      "v_cvt_pk_bf16_f32 v74, v76, v77\n"
      "v_cvt_pk_bf16_f32 v75, v78, v79\n"
      "v_mfma_f32_16x16x32_bf16 v[100:103], a[180:183], v[188:191], v[100:103]\n"
      "ds_write2_b64 v250, v[72:73], v[74:75] offset0:8 offset1:12\n"
      "ds_read_b128 v[68:71], v251 offset:0\n"
      "ds_read_b128 v[76:79], v251 offset:1152\n"
      "v_mfma_f32_16x16x32_bf16 v[100:103], a[148:151], v[184:187], v[100:103]\n"
      "v_mul_f32 v80, v80, v200\n"
      "v_mul_f32 v81, v81, v200\n"
      "v_mul_f32 v82, v82, v200\n"
      "v_mul_f32 v83, v83, v200\n"
      "v_mul_f32 v84, v84, v200\n"
      "v_mul_f32 v85, v85, v200\n"
      "v_mul_f32 v86, v86, v200\n"
      "v_mfma_f32_16x16x32_bf16 v[104:107], a[152:155], v[184:187], v[104:107]\n"
      "v_mul_f32 v87, v87, v200\n"
      "v_cvt_pk_bf16_f32 v80, v80, v81\n"
      "v_cvt_pk_bf16_f32 v81, v82, v83\n"
      "v_cvt_pk_bf16_f32 v82, v84, v85\n"
      "v_cvt_pk_bf16_f32 v83, v86, v87\n"
      "v_mfma_f32_16x16x32_bf16 v[104:107], a[184:187], v[188:191], v[104:107]\n"
      "ds_write2_b64 v250, v[80:81], v[82:83] offset0:0 offset1:4\n"
      "v_mfma_f32_16x16x32_bf16 v[108:111], a[188:191], v[188:191], v[108:111]\n"
      "s_waitcnt lgkmcnt(2)\n"
      "buffer_store_dwordx4 v[68:71], v248, s[48:51], s26 offen offset:0 sc0 sc1\n"
      "v_mfma_f32_16x16x32_bf16 v[108:111], a[156:159], v[184:187], v[108:111]\n"
      "s_waitcnt lgkmcnt(1)\n"
      "buffer_store_dwordx4 v[76:79], v248, s[48:51], s26 offen offset:2048 sc0 sc1\n"
      "v_mfma_f32_16x16x32_bf16 a[232:235], v[68:71], v[76:79], a[232:235]\n"
      "v_mul_f32 v88, v88, v200\n"
      "v_mfma_f32_16x16x32_bf16 v[112:115], a[160:163], v[184:187], v[112:115]\n"
      "v_mul_f32 v89, v89, v200\n"
      "v_mul_f32 v90, v90, v200\n"
      "v_mul_f32 v91, v91, v200\n"
      "v_mul_f32 v92, v92, v200\n"
      "v_mul_f32 v93, v93, v200\n"
      "v_mul_f32 v94, v94, v200\n"
      "v_mul_f32 v95, v95, v200\n"
      "v_mfma_f32_16x16x32_bf16 v[112:115], a[192:195], v[188:191], v[112:115]\n"
      "v_cvt_pk_bf16_f32 v88, v88, v89\n"
      "v_cvt_pk_bf16_f32 v89, v90, v91\n"
      "v_cvt_pk_bf16_f32 v90, v92, v93\n"
      "v_cvt_pk_bf16_f32 v91, v94, v95\n"
      "v_mfma_f32_16x16x32_bf16 v[116:119], a[196:199], v[188:191], v[116:119]\n"
      "ds_write2_b64 v250, v[88:89], v[90:91] offset0:8 offset1:12\n"
      "ds_read_b128 v[84:87], v251 offset:0\n"
      "ds_read_b128 v[92:95], v251 offset:1152\n"
      "v_mfma_f32_16x16x32_bf16 v[116:119], a[164:167], v[184:187], v[116:119]\n"
      "v_accvgpr_read_b32 v204, a220\n"
      "v_subrev_u32 v254, 0x35800000, v204\n"
      "v_max_u32 v255, v255, v254\n"
      "v_rcp_f32 v204, v204\n"
      "s_nop 0\n"
      "v_mul_f32 v96, v96, v204\n"
      "v_mul_f32 v97, v97, v204\n"
      "v_mfma_f32_16x16x32_bf16 v[120:123], a[168:171], v[184:187], v[120:123]\n"
      "v_mul_f32 v98, v98, v204\n"
      "v_mul_f32 v99, v99, v204\n"
      "v_mul_f32 v100, v100, v204\n"
      "v_mul_f32 v101, v101, v204\n"
      "v_mul_f32 v102, v102, v204\n"
      "v_mul_f32 v103, v103, v204\n"
      "v_cvt_pk_bf16_f32 v96, v96, v97\n"
      "v_mfma_f32_16x16x32_bf16 v[120:123], a[200:203], v[188:191], v[120:123]\n"
      "v_cvt_pk_bf16_f32 v97, v98, v99\n"
      "v_cvt_pk_bf16_f32 v98, v100, v101\n"
      "v_cvt_pk_bf16_f32 v99, v102, v103\n"
      "v_mfma_f32_16x16x32_bf16 v[124:127], a[204:207], v[188:191], v[124:127]\n"
      "ds_write2_b64 v250, v[96:97], v[98:99] offset0:0 offset1:4\n"
      "v_mfma_f32_16x16x32_bf16 v[124:127], a[172:175], v[184:187], v[124:127]\n"
      "s_waitcnt lgkmcnt(2)\n"
      "buffer_store_dwordx4 v[84:87], v252, s[48:51], s26 offen offset:0 sc0 sc1\n"
      "s_waitcnt lgkmcnt(1)\n"
      "buffer_store_dwordx4 v[92:95], v252, s[48:51], s26 offen offset:2048 sc0 sc1\n"
      "v_mfma_f32_16x16x32_bf16 a[232:235], v[84:87], v[92:95], a[232:235]\n"
      "v_mul_f32 v104, v104, v204\n"
      "v_mul_f32 v105, v105, v204\n"
      "v_mul_f32 v106, v106, v204\n"
      "v_mul_f32 v107, v107, v204\n"
      "v_mul_f32 v108, v108, v204\n"
      "v_mul_f32 v109, v109, v204\n"
      "v_mul_f32 v110, v110, v204\n"
      "v_mul_f32 v111, v111, v204\n"
      "v_cvt_pk_bf16_f32 v104, v104, v105\n"
      "v_cvt_pk_bf16_f32 v105, v106, v107\n"
      "v_cvt_pk_bf16_f32 v106, v108, v109\n"
      "v_cvt_pk_bf16_f32 v107, v110, v111\n"
      "ds_write2_b64 v250, v[104:105], v[106:107] offset0:8 offset1:12\n"
      "ds_read_b128 v[100:103], v251 offset:0\n"
      "ds_read_b128 v[108:111], v251 offset:1152\n"
      "v_mul_f32 v112, v112, v204\n"
      "v_mul_f32 v113, v113, v204\n"
      "v_mul_f32 v114, v114, v204\n"
      "v_mul_f32 v115, v115, v204\n"
      "v_mul_f32 v116, v116, v204\n"
      "v_mul_f32 v117, v117, v204\n"
      "v_mul_f32 v118, v118, v204\n"
      "v_mul_f32 v119, v119, v204\n"
      "v_cvt_pk_bf16_f32 v112, v112, v113\n"
      "v_cvt_pk_bf16_f32 v113, v114, v115\n"
      "v_cvt_pk_bf16_f32 v114, v116, v117\n"
      "v_cvt_pk_bf16_f32 v115, v118, v119\n"
      "ds_write2_b64 v250, v[112:113], v[114:115] offset0:0 offset1:4\n"
      "s_waitcnt lgkmcnt(2)\n"
      "buffer_store_dwordx4 v[100:103], v248, s[48:51], s27 offen offset:0 sc0 sc1\n"
      "s_waitcnt lgkmcnt(1)\n"
      "buffer_store_dwordx4 v[108:111], v248, s[48:51], s27 offen offset:2048 sc0 sc1\n"
      "v_mfma_f32_16x16x32_bf16 a[232:235], v[100:103], v[108:111], a[232:235]\n"
      "v_pk_mul_f32 v[120:121], v[120:121], v[204:205] op_sel_hi:[1,0]\n"
      "v_pk_mul_f32 v[122:123], v[122:123], v[204:205] op_sel_hi:[1,0]\n"
      "v_pk_mul_f32 v[124:125], v[124:125], v[204:205] op_sel_hi:[1,0]\n"
      "v_pk_mul_f32 v[126:127], v[126:127], v[204:205] op_sel_hi:[1,0]\n"
      "v_cvt_pk_bf16_f32 v120, v120, v121\n"
      "v_cvt_pk_bf16_f32 v121, v122, v123\n"
      "v_cvt_pk_bf16_f32 v122, v124, v125\n"
      "v_cvt_pk_bf16_f32 v123, v126, v127\n"
      "ds_write2_b64 v250, v[120:121], v[122:123] offset0:8 offset1:12\n"
      "ds_read_b128 v[116:119], v251 offset:0\n"
      "ds_read_b128 v[124:127], v251 offset:1152\n"
      "s_waitcnt lgkmcnt(1)\n"
      "buffer_store_dwordx4 v[116:119], v252, s[48:51], s27 offen offset:0 sc0 sc1\n"
      "s_waitcnt lgkmcnt(0)\n"
      "buffer_store_dwordx4 v[124:127], v252, s[48:51], s27 offen offset:2048 sc0 sc1\n"
      "v_mfma_f32_16x16x32_bf16 a[232:235], v[116:119], v[124:127], a[232:235]\n"
      "v_cmp_lt_u32 vcc, 0x37000000, v255\n"
      "s_mov_b64 s[66:67], vcc\n"
      "v_mov_b32 v254, 0x207\n"
      "s_nop 4\n"
      "v_accvgpr_read_b32 v250, a232\n"
      "v_accvgpr_read_b32 v251, a233\n"
      "v_accvgpr_read_b32 v252, a234\n"
      "v_accvgpr_read_b32 v253, a235\n"
      "v_fma_f32 v253, v250, 0, v253\n"
      "v_fma_f32 v253, v251, 0, v253\n"
      "v_fma_f32 v253, v252, 0, v253\n"
      "v_cmp_class_f32 vcc, v253, v254\n"
      "s_or_b64 s[66:67], s[66:67], vcc\n"
      "s_or_b32 %[flag], s66, s67\n"
      "s_mov_b32 %[w], s64\n"
      : [flag] "=&s"(flag), [w] "=&s"(w), [hd] "=&s"(hd), [kp] "+s"(kp), [tid] "+v"(tid)
      : [h] "s"(blockIdx.x)
      : "v1", "v2", "v3", "v4", "v5", "v6", "v7", "v8", "v9", "v10", "v11", "v12", "v13", "v14", "v15", "v16", "v17", "v18", "v19", "v20", "v21", "v22", "v23", "v24", "v25", "v26", "v27", "v28", "v29", "v30", "v31", "v32", "v33", "v34", "v35", "v36", "v37", "v38", "v39", "v40", "v41", "v42", "v43", "v44", "v45", "v46", "v47", "v48", "v49", "v50", "v51", "v52", "v53", "v54", "v55", "v56", "v57", "v58", "v59", "v60", "v61", "v62", "v63", "v64", "v65", "v66", "v67", "v68", "v69", "v70", "v71", "v72", "v73", "v74", "v75", "v76", "v77", "v78", "v79", "v80", "v81", "v82", "v83", "v84", "v85", "v86", "v87", "v88", "v89", "v90", "v91", "v92", "v93", "v94", "v95", "v96", "v97", "v98", "v99", "v100", "v101", "v102", "v103", "v104", "v105", "v106", "v107", "v108", "v109", "v110", "v111", "v112", "v113", "v114", "v115", "v116", "v117", "v118", "v119", "v120", "v121", "v122", "v123", "v124", "v125", "v126", "v127", "v128", "v129", "v130", "v131", "v132", "v133", "v134", "v135", "v136", "v137", "v138", "v139", "v140", "v141", "v142", "v143", "v144", "v145", "v146", "v147", "v148", "v149", "v150", "v151", "v152", "v153", "v154", "v155", "v156", "v157", "v158", "v159", "v160", "v161", "v162", "v163", "v164", "v165", "v166", "v167", "v168", "v169", "v170", "v171", "v172", "v173", "v174", "v175", "v176", "v177", "v178", "v179", "v180", "v181", "v182", "v183", "v184", "v185", "v186", "v187", "v188", "v189", "v190", "v191", "v192", "v193", "v194", "v195", "v196", "v197", "v198", "v199", "v200", "v201", "v202", "v203", "v204", "v205", "v206", "v207", "v208", "v209", "v210", "v211", "v212", "v213", "v214", "v215", "v216", "v217", "v218", "v219", "v220", "v221", "v222", "v223", "v224", "v225", "v226", "v227", "v228", "v229", "v230", "v231", "v232", "v233", "v234", "v235", "v236", "v237", "v238", "v239", "v240", "v241", "v242", "v243", "v244", "v245", "v246", "v247", "v248", "v249", "v250", "v251", "v252", "v253", "v254", "v255", "a0", "a1", "a2", "a3", "a4", "a5", "a6", "a7", "a8", "a9", "a10", "a11", "a12", "a13", "a14", "a15", "a16", "a17", "a18", "a19", "a20", "a21", "a22", "a23", "a24", "a25", "a26", "a27", "a28", "a29", "a30", "a31", "a32", "a33", "a34", "a35", "a36", "a37", "a38", "a39", "a40", "a41", "a42", "a43", "a44", "a45", "a46", "a47", "a48", "a49", "a50", "a51", "a52", "a53", "a54", "a55", "a56", "a57", "a58", "a59", "a60", "a61", "a62", "a63", "a64", "a65", "a66", "a67", "a68", "a69", "a70", "a71", "a72", "a73", "a74", "a75", "a76", "a77", "a78", "a79", "a80", "a81", "a82", "a83", "a84", "a85", "a86", "a87", "a88", "a89", "a90", "a91", "a92", "a93", "a94", "a95", "a96", "a97", "a98", "a99", "a100", "a101", "a102", "a103", "a104", "a105", "a106", "a107", "a108", "a109", "a110", "a111", "a112", "a113", "a114", "a115", "a116", "a117", "a118", "a119", "a120", "a121", "a122", "a123", "a124", "a125", "a126", "a127", "a128", "a129", "a130", "a131", "a132", "a133", "a134", "a135", "a136", "a137", "a138", "a139", "a140", "a141", "a142", "a143", "a144", "a145", "a146", "a147", "a148", "a149", "a150", "a151", "a152", "a153", "a154", "a155", "a156", "a157", "a158", "a159", "a160", "a161", "a162", "a163", "a164", "a165", "a166", "a167", "a168", "a169", "a170", "a171", "a172", "a173", "a174", "a175", "a176", "a177", "a178", "a179", "a180", "a181", "a182", "a183", "a184", "a185", "a186", "a187", "a188", "a189", "a190", "a191", "a192", "a193", "a194", "a195", "a196", "a197", "a198", "a199", "a200", "a201", "a202", "a203", "a204", "a205", "a206", "a207", "a208", "a209", "a210", "a211", "a212", "a213", "a214", "a215", "a216", "a217", "a218", "a219", "a220", "a221", "a222", "a223", "a224", "a225", "a226", "a227", "a228", "a229", "a230", "a231", "a232", "a233", "a234", "a235", "a236", "a237", "a238", "a239", "a240", "a241", "a242", "a243", "a244", "a245", "a246", "a247", "a248", "a249", "a250", "a251", "a252", "a253", "a254", "a255", "s24", "s25", "s26", "s27", "s28", "s29", "s30", "s31", "s36", "s37", "s38", "s39", "s40", "s41", "s42", "s43", "s44", "s45", "s46", "s47", "s48", "s49", "s50", "s51", "s52", "s53", "s54", "s55", "s56", "s57", "s58", "s59", "s60", "s61", "s62", "s63", "s64", "s65", "s66", "s67", "s68", "s69", "s70", "s71", "s72", "s73", "s74", "s75", "s76", "s77", "s78", "s79", "s80", "s81", "s82", "s83", "s84", "s85", "s86", "s87", "s88", "s89", "s90", "s91", "s92", "s93", "s94", "s95", "s96", "s97", "s98", "s99", "s100", "s101", "vcc", "scc", "m0", "memory");
  if (flag) {
    asm volatile("s_waitcnt vmcnt(0)" ::: "memory");
    const u16* qh = kp[0] + (size_t)hd * (SQ * DH);
    const u16* kh = kp[1] + (size_t)hd * (SK * DH);
    const u16* vh = kp[2] + (size_t)hd * (SK * DH);
    u16* oh = (u16*)kp[3] + (size_t)hd * (SQ * DH);
    const int lane = __builtin_amdgcn_mbcnt_hi(~0u, __builtin_amdgcn_mbcnt_lo(~0u, 0u));
    const float c = 0.08838834764831845f * 1.4426950408889634f;
    for (int it = 0; it < 80; ++it) {
      const int row = it < 64 ? (int)w * 64 + it : 256 + (int)(w >> 1) * 16 + (it - 64);
      const u16* qr = qh + row * DH;
      float m = -INFINITY;
      for (int i = 0; i < SK / 64; ++i) m = fmaxf(m, dot128(qr, kh + (i * 64 + lane) * DH));
      for (int s = 32; s; s >>= 1) m = fmaxf(m, __shfl_xor(m, s));
      float l = 0.f, a0 = 0.f, a1 = 0.f;
      for (int i = 0; i < SK / 64; ++i) {
        const float p = exp2f((dot128(qr, kh + (i * 64 + lane) * DH) - m) * c);
        l += p;
        for (int j = 0; j < 64; ++j) {
          const float pj = __uint_as_float(__builtin_amdgcn_readlane(__float_as_uint(p), j));
          const unsigned x = *(const unsigned*)(vh + (i * 64 + j) * DH + 2 * lane);
          a0 = fmaf(pj, lo16(x), a0); a1 = fmaf(pj, hi16(x), a1);
        }
      }
      for (int s = 32; s; s >>= 1) l += __shfl_xor(l, s);
      const float inv = 1.f / l;
      if (it < 64 || (unsigned)(lane >> 5) == (w & 1)) {
        __hip_bfloat16* dst = (__hip_bfloat16*)(oh + row * DH + 2 * lane);
        dst[0] = __float2bfloat16(a0 * inv);
        dst[1] = __float2bfloat16(a1 * inv);
      }
    }
  }
}
}

extern "C" void kernel_launch(void* const* d_in, const int* in_sizes, int n_in, void* d_out, int out_size, void* d_ws, size_t ws_size,
                              hipStream_t stream) {
  (void)in_sizes; (void)n_in; (void)out_size; (void)ws_size;
  (void)hipFuncSetAttribute((const void*)mha_fwd, hipFuncAttributeMaxDynamicSharedMemorySize, 163840);
  mha_fwd<<<256, 256, 163840, stream>>>((const u16*)d_in[0], (const u16*)d_in[1], (const u16*)d_in[2], (u16*)d_out, (unsigned*)d_ws);
}
```
